```python
import torch

def _softmax(x, axis=-1):
    return torch.softmax(x, dim=axis)
def _gelu(x):
    return torch.nn.functional.gelu(x, approximate="tanh")
def _silu(x):
    return torch.nn.functional.silu(x)
def _sigmoid(x):
    return torch.sigmoid(x)
def _layernorm(x, axis=-1, eps=1e-5):
    m = x.mean(dim=axis, keepdim=True)
    v = x.var(dim=axis, keepdim=True, unbiased=False)
    return (x - m) / torch.sqrt(v + eps)
def _scatter_add(tgt, idx, src, axis=0):
    return tgt.clone().index_add_(axis, idx, src)
def _segment_sum(src, seg, n):
    out = torch.zeros((n,) + src.shape[1:], dtype=src.dtype)
    return out.index_add_(0, seg, src)
def _pad(x, pw):
    flat = tuple(v for pair in reversed(pw) for v in pair)
    return torch.nn.functional.pad(x, flat)

kernel_name = 'recurrent_scan__ssm_conv_s5__geglu_ffn__prompt'

def setup_inputs():

    _w = (lambda a: (lambda t: t.to(torch.bfloat16) if t.is_floating_point() else t)(torch.as_tensor(a)))
    def _A_setup():
        B, S, D = 1, 2048, 1024
        P = D
        Lambda_re = -0.5 * (lambda _x: torch.exp(torch.as_tensor(_x)))((lambda *s: torch.randn(*s, dtype=torch.bfloat16) * 0.2)(P))
        Lambda_im = (lambda *s: torch.randn(*s, dtype=torch.bfloat16) * 0.4)(P) * 1.0
        B_re = (lambda *s: torch.randn(*s, dtype=torch.bfloat16) * 0.2)(P, D) * (D ** -0.5)
        B_im = (lambda *s: torch.randn(*s, dtype=torch.bfloat16) * 0.2)(P, D) * (D ** -0.5)
        C_re = (lambda *s: torch.randn(*s, dtype=torch.bfloat16) * 0.2)(D, P) * (P ** -0.5)
        C_im = (lambda *s: torch.randn(*s, dtype=torch.bfloat16) * 0.2)(D, P) * (P ** -0.5)
        log_dt = (lambda *s: torch.randn(*s, dtype=torch.bfloat16) * 0.2)(P) * 0.3 - 2.0
        Dskip = (lambda *s: torch.randn(*s, dtype=torch.bfloat16) * 0.05)(D)
        return {
            "x": _w((lambda *s: torch.randn(*s, dtype=torch.bfloat16) * 0.05)(B, S, D)),
            "Lambda_re": _w(Lambda_re),
            "Lambda_im": _w(Lambda_im),
            "B_re": _w(B_re),
            "B_im": _w(B_im),
            "C_re": _w(C_re),
            "C_im": _w(C_im),
            "log_dt": _w(log_dt),
            "Dskip": _w(Dskip),
        }
    _A_in = _A_setup()

    def _B_setup():
        B, S, D, F = 1, 2048, 1024, 3584
        return {
            "x": _w((lambda *s: torch.randn(*s, dtype=torch.bfloat16) * 0.05)(B, S, D)),
            "w_gate": _w((lambda *s: torch.randn(*s, dtype=torch.bfloat16) * 0.05)(D, F)),
            "w_up": _w((lambda *s: torch.randn(*s, dtype=torch.bfloat16) * 0.05)(D, F)),
            "w_down": _w((lambda *s: torch.randn(*s, dtype=torch.bfloat16) * 0.05)(F, D)),
        }
    _B_in = _B_setup()

    return {
        "A_x": _A_in["x"],
        "A_Lambda_re": _A_in["Lambda_re"],
        "A_Lambda_im": _A_in["Lambda_im"],
        "A_B_re": _A_in["B_re"],
        "A_B_im": _A_in["B_im"],
        "A_C_re": _A_in["C_re"],
        "A_C_im": _A_in["C_im"],
        "A_log_dt": _A_in["log_dt"],
        "A_Dskip": _A_in["Dskip"],
        "B_x": _B_in["x"],
        "B_w_gate": _B_in["w_gate"],
        "B_w_up": _B_in["w_up"],
        "B_w_down": _B_in["w_down"],
    }

def reference(A_x, A_Lambda_re, A_Lambda_im, A_B_re, A_B_im, A_C_re, A_C_im, A_log_dt, A_Dskip, B_x, B_w_gate, B_w_up, B_w_down):
    def _A_ref(x, Lambda_re, Lambda_im, B_re, B_im, C_re, C_im, log_dt, Dskip):
        Bsz, L, H = x.shape
        Lam = Lambda_re + 1j * Lambda_im
        Bmat = B_re + 1j * B_im
        Cmat = C_re + 1j * C_im
        dt = torch.exp(log_dt)
        Lam_bar = torch.exp(dt * Lam)
        B_bar = ((Lam_bar - 1.0) / Lam)[:, None] * Bmat
        Bu = torch.einsum("bld,pd->blp", x + 0j, B_bar)
        h = torch.zeros_like(Bu[:, 0, :])
        hs = []
        for t in range(L):
            h = Lam_bar[None, :] * h + Bu[:, t, :]
            hs.append(h)
        hseq = torch.stack(hs, 1)
        y = 2.0 * torch.einsum("blp,dp->bld", hseq, Cmat).real + x * Dskip[None, None, :]
        return y

    def _B_ref(x, w_gate, w_up, w_down):
        return (_gelu(x @ w_gate) * (x @ w_up)) @ w_down

    x = A_x
    xn = _layernorm(x, axis=-1)
    s = _A_ref(xn, A_Lambda_re, A_Lambda_im, A_B_re, A_B_im, A_C_re, A_C_im, A_log_dt, A_Dskip)
    g = _silu(_B_ref(xn, B_w_gate, B_w_up, B_w_down))
    return x + s * g
```

```cpp
#include <hip/hip_runtime.h>
#include <hip/hip_bf16.h>
#include <stdint.h>

typedef __attribute__((ext_vector_type(8))) short bf16x8;
typedef __attribute__((ext_vector_type(4))) float f32x4;
typedef __attribute__((ext_vector_type(4))) unsigned short u16x4;

constexpr int S_ = 2048;
constexpr int D_ = 1024;
constexpr int P_ = 1024;
constexpr int F_ = 3584;
constexpr int N1_ = 9216;
constexpr int K2_ = 5632;

constexpr size_t OFF_XN = 0;
constexpr size_t OFF_W1T = OFF_XN + (size_t)S_ * D_ * 2;
constexpr size_t OFF_W2T = OFF_W1T + (size_t)N1_ * D_ * 2;
constexpr size_t OFF_A2 = OFF_W2T + (size_t)D_ * K2_ * 2;
constexpr size_t OFF_BU = OFF_A2 + (size_t)S_ * K2_ * 2;
constexpr size_t OFF_LAM = OFF_BU + (size_t)S_ * 2048 * 4;

__device__ __forceinline__ float bf2f(unsigned short v) { return __uint_as_float(((unsigned)v) << 16); }
__device__ __forceinline__ unsigned short f2bf(float f) {
  unsigned u = __float_as_uint(f);
  unsigned r = 0x7FFFu + ((u >> 16) & 1u);
  return (unsigned short)((u + r) >> 16);
}
__device__ __forceinline__ float rbf(float f) { return bf2f(f2bf(f)); }

__global__ __launch_bounds__(256) void k_ln(const unsigned short* __restrict__ x, unsigned short* __restrict__ xn) {
  int wave = threadIdx.x >> 6, lane = threadIdx.x & 63;
  int row = blockIdx.x * 4 + wave;
  const bf16x8* src = reinterpret_cast<const bf16x8*>(x + (size_t)row * D_);
  bf16x8 v0 = src[lane], v1 = src[64 + lane];
  float f[16];
#pragma unroll
  for (int j = 0; j < 8; ++j) { f[j] = bf2f((unsigned short)v0[j]); f[8 + j] = bf2f((unsigned short)v1[j]); }
  float s = 0.f;
#pragma unroll
  for (int j = 0; j < 16; ++j) s += f[j];
#pragma unroll
  for (int o = 32; o >= 1; o >>= 1) s += __shfl_xor(s, o);
  float mean = s * (1.0f / D_);
  float q = 0.f;
#pragma unroll
  for (int j = 0; j < 16; ++j) { float d = f[j] - mean; q += d * d; }
#pragma unroll
  for (int o = 32; o >= 1; o >>= 1) q += __shfl_xor(q, o);
  float var = q * (1.0f / D_);
  float mb = rbf(mean), vb = rbf(var);
  float den = rbf(sqrtf(rbf(vb + 1e-5f)));
  bf16x8 o0, o1;
#pragma unroll
  for (int j = 0; j < 8; ++j) {
    o0[j] = (short)f2bf(rbf(f[j] - mb) / den);
    o1[j] = (short)f2bf(rbf(f[8 + j] - mb) / den);
  }
  bf16x8* dst = reinterpret_cast<bf16x8*>(xn + (size_t)row * D_);
  dst[lane] = o0; dst[64 + lane] = o1;
}

__device__ __forceinline__ int w1_row_gate(int col) { int tn = col / 112, c = col % 112; return tn * 288 + (c < 64 ? c : 144 + (c - 64)); }
__device__ __forceinline__ int w1_row_up(int col)   { int tn = col / 112, c = col % 112; return tn * 288 + (c < 64 ? 64 + c : 192 + (c - 64)); }
__device__ __forceinline__ int w1_row_bre(int p)    { int tn = p >> 5, c = p & 31; return tn * 288 + (c < 16 ? 128 + c : 240 + (c - 16)); }
__device__ __forceinline__ int w1_row_bim(int p)    { int tn = p >> 5, c = p & 31; return tn * 288 + 256 + c; }

template <int MODE>
__global__ __launch_bounds__(256) void k_tr(const unsigned short* __restrict__ src, unsigned short* __restrict__ dst, int R, int C) {
  __shared__ unsigned short tile[64][66];
  int r0 = blockIdx.y * 64, c0 = blockIdx.x * 64;
  int tid = threadIdx.x;
  for (int i = tid; i < 64 * 64; i += 256) { int r = i >> 6, c = i & 63; tile[r][c] = src[(size_t)(r0 + r) * C + c0 + c]; }
  __syncthreads();
  for (int i = tid; i < 64 * 64; i += 256) {
    int c = i >> 6, r = i & 63;
    int col = c0 + c;
    size_t drow;
    if (MODE == 0) drow = (size_t)w1_row_gate(col) * D_;
    else if (MODE == 1) drow = (size_t)w1_row_up(col) * D_;
    else drow = (size_t)col * K2_;
    dst[drow + r0 + r] = tile[r][c];
  }
}

__global__ __launch_bounds__(256) void k_bbar(const unsigned short* __restrict__ lre, const unsigned short* __restrict__ lim,
                                              const unsigned short* __restrict__ bre, const unsigned short* __restrict__ bim,
                                              const unsigned short* __restrict__ logdt, unsigned short* __restrict__ w1t,
                                              float2* __restrict__ lam) {
  int p = blockIdx.x;
  float lr = bf2f(lre[p]), li = bf2f(lim[p]);
  float dt = rbf(expf(bf2f(logdt[p])));
  float zr = dt * lr, zi = dt * li;
  float e = expf(zr);
  float br_ = e * cosf(zi), bi_ = e * sinf(zi);
  float nr = br_ - 1.0f, ni = bi_;
  float den = lr * lr + li * li;
  float cr = (nr * lr + ni * li) / den, ci = (ni * lr - nr * li) / den;
  if (threadIdx.x == 0) lam[p] = make_float2(br_, bi_);
  size_t rr = (size_t)w1_row_bre(p) * D_, ri = (size_t)w1_row_bim(p) * D_;
  for (int d = threadIdx.x; d < D_; d += 256) {
    float a = bf2f(bre[(size_t)p * D_ + d]), b = bf2f(bim[(size_t)p * D_ + d]);
    w1t[rr + d] = f2bf(cr * a - ci * b);
    w1t[ri + d] = f2bf(cr * b + ci * a);
  }
}

__global__ __launch_bounds__(256) void k_cprep(const unsigned short* __restrict__ cre, const unsigned short* __restrict__ cim,
                                               unsigned short* __restrict__ w2t) {
  int d = blockIdx.x;
  for (int p = threadIdx.x; p < P_; p += 256) {
    w2t[(size_t)d * K2_ + F_ + p] = cre[(size_t)d * P_ + p];
    w2t[(size_t)d * K2_ + F_ + P_ + p] = cim[(size_t)d * P_ + p] ^ 0x8000u;
  }
}

__device__ __forceinline__ float gelu_tanh(float x) {
  float u = 0.7978845608028654f * (x + 0.044715f * x * x * x);
  return 0.5f * x * (1.0f + tanhf(u));
}

constexpr int G1_BK = 64;
constexpr int G1_AT = 256 * G1_BK * 2;
constexpr int G1_WT = 288 * G1_BK * 2;
constexpr int G1_STAGE = G1_AT + G1_WT;

__global__ __launch_bounds__(512, 2) void k_gemm1(const unsigned short* __restrict__ xn, const unsigned short* __restrict__ w1t,
                                                  unsigned short* __restrict__ a2, float* __restrict__ bu) {
  extern __shared__ __attribute__((aligned(16))) char smem[];
  int bid = blockIdx.x;
  int xcd = bid & 7, loc = bid >> 3;
  int tn = xcd * 4 + (loc >> 3), tm = loc & 7;
  int tid = threadIdx.x, wid = tid >> 6, lane = tid & 63;
  int wr = wid >> 1, wc = wid & 1;
  int fr = lane & 15, fq = lane >> 4;
  const unsigned short* xg = xn + (size_t)tm * 256 * D_;
  const unsigned short* wg = w1t + (size_t)tn * 288 * D_;

  f32x4 acc[9][4];
#pragma unroll
  for (int i = 0; i < 9; ++i)
#pragma unroll
    for (int j = 0; j < 4; ++j) acc[i][j] = (f32x4){0.f, 0.f, 0.f, 0.f};

  auto stage = [&](int buf, int kt) {
    char* base = smem + buf * G1_STAGE;
#pragma unroll
    for (int i = 0; i < 4; ++i) {
      int ins = wid * 4 + i;
      int row = ins * 8 + (lane >> 3), c = (lane & 7) ^ (row & 7);
      __builtin_amdgcn_global_load_lds((const unsigned*)(xg + (size_t)row * D_ + kt * G1_BK + c * 8),
                                       (__attribute__((address_space(3))) unsigned*)(base + ins * 1024), 16, 0, 0);
    }
    int nW = (wid < 4) ? 5 : 4;
    int st = (wid < 4) ? wid * 5 : 20 + (wid - 4) * 4;
#pragma unroll
    for (int i = 0; i < 5; ++i) {
      if (i < nW) {
        int ins = st + i;
        int row = ins * 8 + (lane >> 3), c = (lane & 7) ^ (row & 7);
        __builtin_amdgcn_global_load_lds((const unsigned*)(wg + (size_t)row * D_ + kt * G1_BK + c * 8),
                                         (__attribute__((address_space(3))) unsigned*)(base + G1_AT + ins * 1024), 16, 0, 0);
      }
    }
  };

  const int nt = D_ / G1_BK;
  stage(0, 0);
  asm volatile("s_waitcnt vmcnt(0)" ::: "memory");
  __syncthreads();
  for (int t = 0; t < nt; ++t) {
    int cur = t & 1;
    if (t + 1 < nt) stage(cur ^ 1, t + 1);
    const char* xa = smem + cur * G1_STAGE;
    const char* wa = xa + G1_AT;
#pragma unroll
    for (int ks = 0; ks < 2; ++ks) {
      bf16x8 xf[4];
#pragma unroll
      for (int j = 0; j < 4; ++j) {
        int row = wr * 64 + j * 16 + fr;
        int c = (ks * 4 + fq) ^ (row & 7);
        xf[j] = *reinterpret_cast<const bf16x8*>(xa + row * 128 + c * 16);
      }
#pragma unroll
      for (int i = 0; i < 9; ++i) {
        int row = wc * 144 + i * 16 + fr;
        int c = (ks * 4 + fq) ^ (row & 7);
        bf16x8 wf = *reinterpret_cast<const bf16x8*>(wa + row * 128 + c * 16);
#pragma unroll
        for (int j = 0; j < 4; ++j) acc[i][j] = __builtin_amdgcn_mfma_f32_16x16x32_bf16(wf, xf[j], acc[i][j], 0, 0, 0);
      }
    }
    asm volatile("s_waitcnt vmcnt(0)" ::: "memory");
    __syncthreads();
  }

  int tbase = tm * 256 + wr * 64 + fr;
  if (wc == 0) {
#pragma unroll
    for (int i = 0; i < 4; ++i)
#pragma unroll
      for (int j = 0; j < 4; ++j) {
        u16x4 o;
#pragma unroll
        for (int r = 0; r < 4; ++r) {
          float g = rbf(gelu_tanh(rbf(acc[i][j][r])));
          float u = rbf(acc[i + 4][j][r]);
          o[r] = f2bf(g * u);
        }
        int t = tbase + j * 16;
        *reinterpret_cast<u16x4*>(a2 + (size_t)t * K2_ + tn * 112 + i * 16 + fq * 4) = o;
      }
#pragma unroll
    for (int j = 0; j < 4; ++j) {
      int t = tbase + j * 16;
      *reinterpret_cast<f32x4*>(bu + (size_t)t * 2048 + tn * 32 + fq * 4) = acc[8][j];
    }
  } else {
#pragma unroll
    for (int i = 0; i < 3; ++i)
#pragma unroll
      for (int j = 0; j < 4; ++j) {
        u16x4 o;
#pragma unroll
        for (int r = 0; r < 4; ++r) {
          float g = rbf(gelu_tanh(rbf(acc[i][j][r])));
          float u = rbf(acc[i + 3][j][r]);
          o[r] = f2bf(g * u);
        }
        int t = tbase + j * 16;
        *reinterpret_cast<u16x4*>(a2 + (size_t)t * K2_ + tn * 112 + 64 + i * 16 + fq * 4) = o;
      }
#pragma unroll
    for (int j = 0; j < 4; ++j) {
      int t = tbase + j * 16;
      *reinterpret_cast<f32x4*>(bu + (size_t)t * 2048 + tn * 32 + 16 + fq * 4) = acc[6][j];
      *reinterpret_cast<f32x4*>(bu + (size_t)t * 2048 + 1024 + tn * 32 + fq * 4) = acc[7][j];
      *reinterpret_cast<f32x4*>(bu + (size_t)t * 2048 + 1024 + tn * 32 + 16 + fq * 4) = acc[8][j];
    }
  }
}

__global__ __launch_bounds__(1024) void k_scan(const float* __restrict__ bu, const float2* __restrict__ lamv,
                                               unsigned short* __restrict__ a2) {
  __shared__ float2 agg[64][16];
  int tid = threadIdx.x;
  int ch = tid & 15, ck = tid >> 4;
  int p = blockIdx.x * 16 + ch;
  float2 lam = lamv[p];
  float hr[32], hi[32];
  float sr = 0.f, si = 0.f;
#pragma unroll
  for (int i = 0; i < 32; ++i) {
    int t = ck * 32 + i;
    float br = bu[(size_t)t * 2048 + p], bi = bu[(size_t)t * 2048 + 1024 + p];
    float nr = lam.x * sr - lam.y * si + br;
    float ni = lam.x * si + lam.y * sr + bi;
    sr = nr; si = ni; hr[i] = sr; hi[i] = si;
  }
  agg[ck][ch] = make_float2(sr, si);
  float2 l2 = lam;
#pragma unroll
  for (int i = 0; i < 5; ++i) l2 = make_float2(l2.x * l2.x - l2.y * l2.y, 2.f * l2.x * l2.y);
  __syncthreads();
  if (tid < 16) {
    float cr = 0.f, ci = 0.f;
    for (int k = 0; k < 64; ++k) {
      float2 a = agg[k][ch];
      agg[k][ch] = make_float2(cr, ci);
      float nr = l2.x * cr - l2.y * ci + a.x;
      float ni = l2.x * ci + l2.y * cr + a.y;
      cr = nr; ci = ni;
    }
  }
  __syncthreads();
  float2 c = agg[ck][ch];
  float pr = lam.x, pi = lam.y;
#pragma unroll
  for (int i = 0; i < 32; ++i) {
    int t = ck * 32 + i;
    float vr = hr[i] + pr * c.x - pi * c.y;
    float vi = hi[i] + pr * c.y + pi * c.x;
    a2[(size_t)t * K2_ + F_ + p] = f2bf(vr);
    a2[(size_t)t * K2_ + F_ + P_ + p] = f2bf(vi);
    float npr = pr * lam.x - pi * lam.y, npi = pr * lam.y + pi * lam.x;
    pr = npr; pi = npi;
  }
}

__global__ __launch_bounds__(256) void k_gemm2(const unsigned short* __restrict__ a2, const unsigned short* __restrict__ w2t,
                                               const unsigned short* __restrict__ x, const unsigned short* __restrict__ xn,
                                               const unsigned short* __restrict__ dskip, float* __restrict__ out) {
  __shared__ __attribute__((aligned(16))) char smem[2 * 32768];
  int tm = blockIdx.x, tn = blockIdx.y;
  int tid = threadIdx.x, wid = tid >> 6, lane = tid & 63;
  int wr = wid >> 1, wc = wid & 1, fr = lane & 15, fq = lane >> 4;
  const unsigned short* ag = a2 + (size_t)tm * 128 * K2_;
  const unsigned short* wg = w2t + (size_t)tn * 128 * K2_;
  f32x4 accf[4][4], accy[4][4];
#pragma unroll
  for (int i = 0; i < 4; ++i)
#pragma unroll
    for (int j = 0; j < 4; ++j) { accf[i][j] = (f32x4){0, 0, 0, 0}; accy[i][j] = (f32x4){0, 0, 0, 0}; }
  auto stage = [&](int buf, int kt) {
    char* base = smem + buf * 32768;
#pragma unroll
    for (int i = 0; i < 4; ++i) {
      int ins = wid * 4 + i;
      int row = ins * 8 + (lane >> 3), c = (lane & 7) ^ (row & 7);
      __builtin_amdgcn_global_load_lds((const unsigned*)(ag + (size_t)row * K2_ + kt * 64 + c * 8),
                                       (__attribute__((address_space(3))) unsigned*)(base + ins * 1024), 16, 0, 0);
      __builtin_amdgcn_global_load_lds((const unsigned*)(wg + (size_t)row * K2_ + kt * 64 + c * 8),
                                       (__attribute__((address_space(3))) unsigned*)(base + 16384 + ins * 1024), 16, 0, 0);
    }
  };
  const int nt = K2_ / 64, ntf = F_ / 64;
  stage(0, 0);
  asm volatile("s_waitcnt vmcnt(0)" ::: "memory");
  __syncthreads();
  for (int t = 0; t < nt; ++t) {
    int cur = t & 1;
    if (t + 1 < nt) stage(cur ^ 1, t + 1);
    const char* xa = smem + cur * 32768;
    const char* wa = xa + 16384;
    bool isf = t < ntf;
#pragma unroll
    for (int ks = 0; ks < 2; ++ks) {
      bf16x8 xf[4];
#pragma unroll
      for (int j = 0; j < 4; ++j) {
        int row = wr * 64 + j * 16 + fr;
        int c = (ks * 4 + fq) ^ (row & 7);
        xf[j] = *reinterpret_cast<const bf16x8*>(xa + row * 128 + c * 16);
      }
#pragma unroll
      for (int i = 0; i < 4; ++i) {
        int row = wc * 64 + i * 16 + fr;
        int c = (ks * 4 + fq) ^ (row & 7);
        bf16x8 wf = *reinterpret_cast<const bf16x8*>(wa + row * 128 + c * 16);
        if (isf) {
#pragma unroll
          for (int j = 0; j < 4; ++j) accf[i][j] = __builtin_amdgcn_mfma_f32_16x16x32_bf16(wf, xf[j], accf[i][j], 0, 0, 0);
        } else {
#pragma unroll
          for (int j = 0; j < 4; ++j) accy[i][j] = __builtin_amdgcn_mfma_f32_16x16x32_bf16(wf, xf[j], accy[i][j], 0, 0, 0);
        }
      }
    }
    asm volatile("s_waitcnt vmcnt(0)" ::: "memory");
    __syncthreads();
  }
#pragma unroll
  for (int i = 0; i < 4; ++i)
#pragma unroll
    for (int j = 0; j < 4; ++j) {
      int t = tm * 128 + wr * 64 + j * 16 + fr;
      int d = tn * 128 + wc * 64 + i * 16 + fq * 4;
      u16x4 xv = *reinterpret_cast<const u16x4*>(x + (size_t)t * D_ + d);
      u16x4 xnv = *reinterpret_cast<const u16x4*>(xn + (size_t)t * D_ + d);
      u16x4 dv = *reinterpret_cast<const u16x4*>(dskip + d);
      f32x4 o;
#pragma unroll
      for (int r = 0; r < 4; ++r) {
        float f = rbf(accf[i][j][r]);
        float g = rbf(f / (1.0f + expf(-f)));
        float s = 2.0f * accy[i][j][r] + rbf(bf2f(xnv[r]) * bf2f(dv[r]));
        o[r] = bf2f(xv[r]) + s * g;
      }
      *reinterpret_cast<f32x4*>(out + (size_t)t * D_ + d) = o;
    }
}

extern "C" void kernel_launch(void* const* d_in, const int* in_sizes, int n_in, void* d_out, int out_size, void* d_ws,
                              size_t ws_size, hipStream_t stream) {
  const unsigned short* x = (const unsigned short*)d_in[0];
  const unsigned short* lre = (const unsigned short*)d_in[1];
  const unsigned short* lim = (const unsigned short*)d_in[2];
  const unsigned short* bre = (const unsigned short*)d_in[3];
  const unsigned short* bim = (const unsigned short*)d_in[4];
  const unsigned short* cre = (const unsigned short*)d_in[5];
  const unsigned short* cim = (const unsigned short*)d_in[6];
  const unsigned short* logdt = (const unsigned short*)d_in[7];
  const unsigned short* dskip = (const unsigned short*)d_in[8];
  const unsigned short* wgate = (const unsigned short*)d_in[10];
  const unsigned short* wup = (const unsigned short*)d_in[11];
  const unsigned short* wdown = (const unsigned short*)d_in[12];
  char* ws = (char*)d_ws;
  unsigned short* xn = (unsigned short*)(ws + OFF_XN);
  unsigned short* w1t = (unsigned short*)(ws + OFF_W1T);
  unsigned short* w2t = (unsigned short*)(ws + OFF_W2T);
  unsigned short* a2 = (unsigned short*)(ws + OFF_A2);
  float* bu = (float*)(ws + OFF_BU);
  float2* lam = (float2*)(ws + OFF_LAM);
  float* out = (float*)d_out;

  k_ln<<<S_ / 4, 256, 0, stream>>>(x, xn);
  k_tr<0><<<dim3(F_ / 64, D_ / 64), 256, 0, stream>>>(wgate, w1t, D_, F_);
  k_tr<1><<<dim3(F_ / 64, D_ / 64), 256, 0, stream>>>(wup, w1t, D_, F_);
  k_tr<2><<<dim3(D_ / 64, F_ / 64), 256, 0, stream>>>(wdown, w2t, F_, D_);
  k_bbar<<<P_, 256, 0, stream>>>(lre, lim, bre, bim, logdt, w1t, lam);
  k_cprep<<<D_, 256, 0, stream>>>(cre, cim, w2t);
  hipFuncSetAttribute((const void*)k_gemm1, hipFuncAttributeMaxDynamicSharedMemorySize, 2 * G1_STAGE);
  k_gemm1<<<256, 512, 2 * G1_STAGE, stream>>>(xn, w1t, a2, bu);
  k_scan<<<64, 1024, 0, stream>>>(bu, lam, a2);
  k_gemm2<<<dim3(16, 8), 256, 0, stream>>>(a2, w2t, x, xn, dskip, out);
}
```

```cpp
#include <hip/hip_runtime.h>
#include <hip/hip_bf16.h>
#include <stdint.h>

typedef __attribute__((ext_vector_type(8))) short bf16x8;
typedef __attribute__((ext_vector_type(4))) float f32x4;
typedef __attribute__((ext_vector_type(4))) unsigned short u16x4;

constexpr int S_ = 2048;
constexpr int D_ = 1024;
constexpr int P_ = 1024;
constexpr int F_ = 3584;
constexpr int N1_ = 9216;
constexpr int K2_ = 5632;

constexpr size_t OFF_XN = 0;
constexpr size_t OFF_W1T = OFF_XN + (size_t)S_ * D_ * 2;
constexpr size_t OFF_W2T = OFF_W1T + (size_t)N1_ * D_ * 2;
constexpr size_t OFF_A2 = OFF_W2T + (size_t)D_ * K2_ * 2;
constexpr size_t OFF_BU = OFF_A2 + (size_t)S_ * K2_ * 2;
constexpr size_t OFF_LAM = OFF_BU + (size_t)S_ * 2048 * 4;
constexpr size_t OFF_CNT = OFF_LAM + 8192;
constexpr size_t OFF_SLAB = OFF_CNT + 128 * 32 * 4;

__device__ __forceinline__ float bf2f(unsigned short v) { return __uint_as_float(((unsigned)v) << 16); }
__device__ __forceinline__ unsigned short f2bf(float f) {
  unsigned u = __float_as_uint(f);
  unsigned r = 0x7FFFu + ((u >> 16) & 1u);
  return (unsigned short)((u + r) >> 16);
}
__device__ __forceinline__ float rbf(float f) { return bf2f(f2bf(f)); }

__global__ __launch_bounds__(256) void k_ln(const unsigned short* __restrict__ x, unsigned short* __restrict__ xn) {
  int wave = threadIdx.x >> 6, lane = threadIdx.x & 63;
  int row = blockIdx.x * 4 + wave;
  const bf16x8* src = reinterpret_cast<const bf16x8*>(x + (size_t)row * D_);
  bf16x8 v0 = src[lane], v1 = src[64 + lane];
  float f[16];
#pragma unroll
  for (int j = 0; j < 8; ++j) { f[j] = bf2f((unsigned short)v0[j]); f[8 + j] = bf2f((unsigned short)v1[j]); }
  float s = 0.f;
#pragma unroll
  for (int j = 0; j < 16; ++j) s += f[j];
#pragma unroll
  for (int o = 32; o >= 1; o >>= 1) s += __shfl_xor(s, o);
  float mean = s * (1.0f / D_);
  float q = 0.f;
#pragma unroll
  for (int j = 0; j < 16; ++j) { float d = f[j] - mean; q += d * d; }
#pragma unroll
  for (int o = 32; o >= 1; o >>= 1) q += __shfl_xor(q, o);
  float var = q * (1.0f / D_);
  float mb = rbf(mean), vb = rbf(var);
  float den = rbf(sqrtf(rbf(vb + 1e-5f)));
  bf16x8 o0, o1;
#pragma unroll
  for (int j = 0; j < 8; ++j) {
    o0[j] = (short)f2bf(rbf(f[j] - mb) / den);
    o1[j] = (short)f2bf(rbf(f[8 + j] - mb) / den);
  }
  bf16x8* dst = reinterpret_cast<bf16x8*>(xn + (size_t)row * D_);
  dst[lane] = o0; dst[64 + lane] = o1;
}

__device__ __forceinline__ int w1_row_gate(int col) { int tn = col / 112, c = col % 112; return tn * 288 + (c < 64 ? c : 144 + (c - 64)); }
__device__ __forceinline__ int w1_row_up(int col)   { int tn = col / 112, c = col % 112; return tn * 288 + (c < 64 ? 64 + c : 192 + (c - 64)); }
__device__ __forceinline__ int w1_row_bre(int p)    { int tn = p >> 5, c = p & 31; return tn * 288 + (c < 16 ? 128 + c : 240 + (c - 16)); }
__device__ __forceinline__ int w1_row_bim(int p)    { int tn = p >> 5, c = p & 31; return tn * 288 + 256 + c; }

template <int MODE>
__global__ __launch_bounds__(256) void k_tr(const unsigned short* __restrict__ src, unsigned short* __restrict__ dst, int R, int C) {
  __shared__ unsigned short tile[64][66];
  int r0 = blockIdx.y * 64, c0 = blockIdx.x * 64;
  int tid = threadIdx.x;
  for (int i = tid; i < 64 * 64; i += 256) { int r = i >> 6, c = i & 63; tile[r][c] = src[(size_t)(r0 + r) * C + c0 + c]; }
  __syncthreads();
  for (int i = tid; i < 64 * 64; i += 256) {
    int c = i >> 6, r = i & 63;
    int col = c0 + c;
    size_t drow;
    if (MODE == 0) drow = (size_t)w1_row_gate(col) * D_;
    else if (MODE == 1) drow = (size_t)w1_row_up(col) * D_;
    else drow = (size_t)col * K2_;
    dst[drow + r0 + r] = tile[r][c];
  }
}

__global__ __launch_bounds__(256) void k_bbar(const unsigned short* __restrict__ lre, const unsigned short* __restrict__ lim,
                                              const unsigned short* __restrict__ bre, const unsigned short* __restrict__ bim,
                                              const unsigned short* __restrict__ logdt, unsigned short* __restrict__ w1t,
                                              float2* __restrict__ lam) {
  int p = blockIdx.x;
  float lr = bf2f(lre[p]), li = bf2f(lim[p]);
  float dt = rbf(expf(bf2f(logdt[p])));
  float zr = dt * lr, zi = dt * li;
  float e = expf(zr);
  float br_ = e * cosf(zi), bi_ = e * sinf(zi);
  float nr = br_ - 1.0f, ni = bi_;
  float den = lr * lr + li * li;
  float cr = (nr * lr + ni * li) / den, ci = (ni * lr - nr * li) / den;
  if (threadIdx.x == 0) lam[p] = make_float2(br_, bi_);
  size_t rr = (size_t)w1_row_bre(p) * D_, ri = (size_t)w1_row_bim(p) * D_;
  for (int d = threadIdx.x; d < D_; d += 256) {
    float a = bf2f(bre[(size_t)p * D_ + d]), b = bf2f(bim[(size_t)p * D_ + d]);
    w1t[rr + d] = f2bf(cr * a - ci * b);
    w1t[ri + d] = f2bf(cr * b + ci * a);
  }
}

__global__ __launch_bounds__(256) void k_cprep(const unsigned short* __restrict__ cre, const unsigned short* __restrict__ cim,
                                               unsigned short* __restrict__ w2t) {
  int d = blockIdx.x;
  for (int p = threadIdx.x; p < P_; p += 256) {
    w2t[(size_t)d * K2_ + F_ + p] = cre[(size_t)d * P_ + p];
    w2t[(size_t)d * K2_ + F_ + P_ + p] = cim[(size_t)d * P_ + p] ^ 0x8000u;
  }
}

__device__ __forceinline__ float gelu_tanh(float x) {
  float u = 0.7978845608028654f * (x + 0.044715f * x * x * x);
  return 0.5f * x * (1.0f + tanhf(u));
}

constexpr int G1_AT = 256 * 64;
constexpr int G1_WT = 288 * 64;
constexpr int G1_STAGE = G1_AT + G1_WT;
constexpr int G1_NSLOT = 4;

#define WAIT_VM(n) asm volatile("s_waitcnt vmcnt(" #n ")" ::: "memory")

__global__ __launch_bounds__(512, 2) void k_gemm1(const unsigned short* __restrict__ xn, const unsigned short* __restrict__ w1t,
                                                  unsigned short* __restrict__ a2, float* __restrict__ bu) {
  extern __shared__ __attribute__((aligned(16))) char smem[];
  int bid = blockIdx.x;
  int xcd = bid & 7, loc = bid >> 3;
  int tn = xcd * 4 + (loc >> 3), tm = loc & 7;
  int tid = threadIdx.x, wid = __builtin_amdgcn_readfirstlane(tid >> 6), lane = tid & 63;
  int wr = wid >> 1, wc = wid & 1;
  int fr = lane & 15, fq = lane >> 4;
  const unsigned short* xg = xn + (size_t)tm * 256 * D_;
  const unsigned short* wg = w1t + (size_t)tn * 288 * D_;

  f32x4 acc[9][4];
#pragma unroll
  for (int i = 0; i < 9; ++i)
#pragma unroll
    for (int j = 0; j < 4; ++j) acc[i][j] = (f32x4){0.f, 0.f, 0.f, 0.f};

  int srow = lane >> 2;
  int schunk = (lane & 3) ^ (((srow >> 3) & 1) << 1);
  auto stage = [&](int slot, int ks) {
    char* base = smem + slot * G1_STAGE;
#pragma unroll
    for (int i = 0; i < 2; ++i) {
      int ins = wid * 2 + i;
      __builtin_amdgcn_global_load_lds((const unsigned*)(xg + (size_t)(ins * 16 + srow) * D_ + ks * 32 + schunk * 8),
                                       (__attribute__((address_space(3))) unsigned*)(base + ins * 1024), 16, 0, 0);
    }
#pragma unroll
    for (int i = 0; i < 2; ++i) {
      int ins = wid * 2 + i;
      __builtin_amdgcn_global_load_lds((const unsigned*)(wg + (size_t)(ins * 16 + srow) * D_ + ks * 32 + schunk * 8),
                                       (__attribute__((address_space(3))) unsigned*)(base + G1_AT + ins * 1024), 16, 0, 0);
    }
    if (wid < 2) {
      int ins = 16 + wid;
      __builtin_amdgcn_global_load_lds((const unsigned*)(wg + (size_t)(ins * 16 + srow) * D_ + ks * 32 + schunk * 8),
                                       (__attribute__((address_space(3))) unsigned*)(base + G1_AT + ins * 1024), 16, 0, 0);
    }
  };

  const int ns = D_ / 32;
  stage(0, 0); stage(1, 1); stage(2, 2); stage(3, 3);
  int rchunk = fq ^ (((fr >> 3) & 1) << 1);
  const int xoff = (wr * 64 + fr) * 64 + rchunk * 16;
  const int woff = G1_AT + (wc * 144 + fr) * 64 + rchunk * 16;
  bf16x8 xa_[4], xb_[4], wf[9];
  if (wid < 2) WAIT_VM(15); else WAIT_VM(12);
  __builtin_amdgcn_s_barrier();
#pragma unroll
  for (int j = 0; j < 4; ++j) xa_[j] = *reinterpret_cast<const bf16x8*>(smem + xoff + j * 1024);
#pragma unroll
  for (int i = 0; i < 9; ++i) wf[i] = *reinterpret_cast<const bf16x8*>(smem + woff + i * 1024);

  auto body = [&](int s, bf16x8 (&xc)[4], bf16x8 (&xnx)[4]) {
    if (s < ns - 3) { if (wid < 2) WAIT_VM(10); else WAIT_VM(8); }
    else if (s == ns - 3) { if (wid < 2) WAIT_VM(5); else WAIT_VM(4); }
    else WAIT_VM(0);
    __builtin_amdgcn_s_barrier();
    if (s + 4 < ns) stage(s & 3, s + 4);
    const char* nb = smem + ((s + 1) & 3) * G1_STAGE;
    bool more = (s + 1 < ns);
#pragma unroll
    for (int i = 0; i < 9; ++i) {
#pragma unroll
      for (int j = 0; j < 4; ++j) acc[i][j] = __builtin_amdgcn_mfma_f32_16x16x32_bf16(wf[i], xc[j], acc[i][j], 0, 0, 0);
      if (more) wf[i] = *reinterpret_cast<const bf16x8*>(nb + woff + i * 1024);
      if (more && i < 4) xnx[i] = *reinterpret_cast<const bf16x8*>(nb + xoff + i * 1024);
    }
#pragma unroll
    for (int i = 0; i < 4; ++i) {
      __builtin_amdgcn_sched_group_barrier(0x008, 4, 0);
      __builtin_amdgcn_sched_group_barrier(0x100, 2, 0);
    }
#pragma unroll
    for (int i = 0; i < 5; ++i) {
      __builtin_amdgcn_sched_group_barrier(0x008, 4, 0);
      __builtin_amdgcn_sched_group_barrier(0x100, 1, 0);
    }
  };
  for (int s = 0; s < ns; s += 2) {
    body(s, xa_, xb_);
    body(s + 1, xb_, xa_);
  }

  int tbase = tm * 256 + wr * 64 + fr;
  if (wc == 0) {
#pragma unroll
    for (int i = 0; i < 4; ++i)
#pragma unroll
      for (int j = 0; j < 4; ++j) {
        u16x4 o;
#pragma unroll
        for (int r = 0; r < 4; ++r) {
          float g = rbf(gelu_tanh(rbf(acc[i][j][r])));
          float u = rbf(acc[i + 4][j][r]);
          o[r] = f2bf(g * u);
        }
        int t = tbase + j * 16;
        *reinterpret_cast<u16x4*>(a2 + (size_t)t * K2_ + tn * 112 + i * 16 + fq * 4) = o;
      }
#pragma unroll
    for (int j = 0; j < 4; ++j) {
      int t = tbase + j * 16;
      *reinterpret_cast<f32x4*>(bu + (size_t)t * 2048 + tn * 32 + fq * 4) = acc[8][j];
    }
  } else {
#pragma unroll
    for (int i = 0; i < 3; ++i)
#pragma unroll
      for (int j = 0; j < 4; ++j) {
        u16x4 o;
#pragma unroll
        for (int r = 0; r < 4; ++r) {
          float g = rbf(gelu_tanh(rbf(acc[i][j][r])));
          float u = rbf(acc[i + 3][j][r]);
          o[r] = f2bf(g * u);
        }
        int t = tbase + j * 16;
        *reinterpret_cast<u16x4*>(a2 + (size_t)t * K2_ + tn * 112 + 64 + i * 16 + fq * 4) = o;
      }
#pragma unroll
    for (int j = 0; j < 4; ++j) {
      int t = tbase + j * 16;
      *reinterpret_cast<f32x4*>(bu + (size_t)t * 2048 + tn * 32 + 16 + fq * 4) = acc[6][j];
      *reinterpret_cast<f32x4*>(bu + (size_t)t * 2048 + 1024 + tn * 32 + fq * 4) = acc[7][j];
      *reinterpret_cast<f32x4*>(bu + (size_t)t * 2048 + 1024 + tn * 32 + 16 + fq * 4) = acc[8][j];
    }
  }
}

__global__ __launch_bounds__(1024) void k_scan(const float* __restrict__ bu, const float2* __restrict__ lamv,
                                               unsigned short* __restrict__ a2) {
  __shared__ float2 agg[64][16];
  int tid = threadIdx.x;
  int ch = tid & 15, ck = tid >> 4;
  int p = blockIdx.x * 16 + ch;
  float2 lam = lamv[p];
  float hr[32], hi[32];
  float sr = 0.f, si = 0.f;
#pragma unroll
  for (int i = 0; i < 32; ++i) {
    int t = ck * 32 + i;
    float br = bu[(size_t)t * 2048 + p], bi = bu[(size_t)t * 2048 + 1024 + p];
    float nr = lam.x * sr - lam.y * si + br;
    float ni = lam.x * si + lam.y * sr + bi;
    sr = nr; si = ni; hr[i] = sr; hi[i] = si;
  }
  agg[ck][ch] = make_float2(sr, si);
  float2 l2 = lam;
#pragma unroll
  for (int i = 0; i < 5; ++i) l2 = make_float2(l2.x * l2.x - l2.y * l2.y, 2.f * l2.x * l2.y);
  __syncthreads();
  if (tid < 16) {
    float cr = 0.f, ci = 0.f;
    for (int k = 0; k < 64; ++k) {
      float2 a = agg[k][ch];
      agg[k][ch] = make_float2(cr, ci);
      float nr = l2.x * cr - l2.y * ci + a.x;
      float ni = l2.x * ci + l2.y * cr + a.y;
      cr = nr; ci = ni;
    }
  }
  __syncthreads();
  float2 c = agg[ck][ch];
  float pr = lam.x, pi = lam.y;
#pragma unroll
  for (int i = 0; i < 32; ++i) {
    int t = ck * 32 + i;
    float vr = hr[i] + pr * c.x - pi * c.y;
    float vi = hi[i] + pr * c.y + pi * c.x;
    a2[(size_t)t * K2_ + F_ + p] = f2bf(vr);
    a2[(size_t)t * K2_ + F_ + P_ + p] = f2bf(vi);
    float npr = pr * lam.x - pi * lam.y, npi = pr * lam.y + pi * lam.x;
    pr = npr; pi = npi;
  }
}

constexpr int G2_STAGE = 32768;
constexpr int G2_NSLOT = 4;
__global__ __launch_bounds__(512, 2) void k_gemm2(const unsigned short* __restrict__ a2, const unsigned short* __restrict__ w2t,
                                                  const unsigned short* __restrict__ x, const unsigned short* __restrict__ xn,
                                                  const unsigned short* __restrict__ dskip, float* __restrict__ out,
                                                  f32x4* __restrict__ slab, unsigned* __restrict__ cnt) {
  extern __shared__ __attribute__((aligned(16))) char smem[];
  int bid = blockIdx.x;
  int xcd = bid & 7, loc = bid >> 3;
  int tm = xcd * 2 + (loc >> 4), tn = (loc >> 1) & 7, kz = loc & 1;
  int tile = tm * 8 + tn;
  int tid = threadIdx.x, wid = __builtin_amdgcn_readfirstlane(tid >> 6), lane = tid & 63;
  int kg = wid >> 2, wr = (wid >> 1) & 1, wc = wid & 1, fr = lane & 15, fq = lane >> 4;
  const unsigned short* ag = a2 + (size_t)tm * 128 * K2_ + kz * 2816;
  const unsigned short* wg = w2t + (size_t)tn * 128 * K2_ + kz * 2816;
  f32x4 acc[4][4], accf[4][4];
#pragma unroll
  for (int i = 0; i < 4; ++i)
#pragma unroll
    for (int j = 0; j < 4; ++j) { acc[i][j] = (f32x4){0, 0, 0, 0}; accf[i][j] = (f32x4){0, 0, 0, 0}; }
  int srow = lane >> 3, schunk = (lane & 7) ^ (srow & 7);
  auto stage = [&](int slot, int kt) {
    char* base = smem + slot * G2_STAGE;
#pragma unroll
    for (int i = 0; i < 2; ++i) {
      int ins = wid * 2 + i;
      __builtin_amdgcn_global_load_lds((const unsigned*)(ag + (size_t)(ins * 8 + srow) * K2_ + kt * 64 + schunk * 8),
                                       (__attribute__((address_space(3))) unsigned*)(base + ins * 1024), 16, 0, 0);
      __builtin_amdgcn_global_load_lds((const unsigned*)(wg + (size_t)(ins * 8 + srow) * K2_ + kt * 64 + schunk * 8),
                                       (__attribute__((address_space(3))) unsigned*)(base + 16384 + ins * 1024), 16, 0, 0);
    }
  };
  const int ns = 44;
  const int nsf = kz ? 12 : 44;
  stage(0, 0); stage(1, 1); stage(2, 2); stage(3, 3);
  int rch = ((kg * 4 + fq) ^ (fr & 7)) * 16;
  const int xoff = (wr * 64 + fr) * 128 + rch;
  const int woff = 16384 + (wc * 64 + fr) * 128 + rch;
  bf16x8 xa_[4], xb_[4], wa_[4], wb_[4];
  WAIT_VM(12);
  __builtin_amdgcn_s_barrier();
#pragma unroll
  for (int j = 0; j < 4; ++j) {
    xa_[j] = *reinterpret_cast<const bf16x8*>(smem + xoff + j * 2048);
    wa_[j] = *reinterpret_cast<const bf16x8*>(smem + woff + j * 2048);
  }
  auto body = [&](int s, bf16x8 (&xc)[4], bf16x8 (&wcur)[4], bf16x8 (&xnx)[4], bf16x8 (&wnx)[4]) {
    if (s < ns - 3) WAIT_VM(8);
    else if (s == ns - 3) WAIT_VM(4);
    else WAIT_VM(0);
    __builtin_amdgcn_s_barrier();
    if (s + 4 < ns) stage(s & 3, s + 4);
    const char* nb = smem + ((s + 1) & 3) * G2_STAGE;
    bool more = (s + 1 < ns);
#pragma unroll
    for (int i = 0; i < 4; ++i) {
#pragma unroll
      for (int j = 0; j < 4; ++j) acc[i][j] = __builtin_amdgcn_mfma_f32_16x16x32_bf16(wcur[i], xc[j], acc[i][j], 0, 0, 0);
      if (more) {
        wnx[i] = *reinterpret_cast<const bf16x8*>(nb + woff + i * 2048);
        xnx[i] = *reinterpret_cast<const bf16x8*>(nb + xoff + i * 2048);
      }
    }
#pragma unroll
    for (int i = 0; i < 4; ++i) {
      __builtin_amdgcn_sched_group_barrier(0x008, 4, 0);
      __builtin_amdgcn_sched_group_barrier(0x100, 2, 0);
    }
    if (s + 1 == nsf) {
#pragma unroll
      for (int i = 0; i < 4; ++i)
#pragma unroll
        for (int j = 0; j < 4; ++j) { accf[i][j] = acc[i][j]; acc[i][j] = (f32x4){0, 0, 0, 0}; }
    }
  };
  for (int s = 0; s < ns; s += 2) {
    body(s, xa_, wa_, xb_, wb_);
    body(s + 1, xb_, wb_, xa_, wa_);
  }
  __syncthreads();
  f32x4* ex = reinterpret_cast<f32x4*>(smem);
  int wq = wid & 3;
#pragma unroll
  for (int ii = 0; ii < 2; ++ii)
#pragma unroll
    for (int j = 0; j < 4; ++j) {
      f32x4 sf = kg ? accf[ii][j] : accf[2 + ii][j];
      f32x4 sy = kg ? acc[ii][j] : acc[2 + ii][j];
      ex[(((0 * 2 + kg) * 4 + wq) * 8 + ii * 4 + j) * 64 + lane] = sf;
      if (kz) ex[(((1 * 2 + kg) * 4 + wq) * 8 + ii * 4 + j) * 64 + lane] = sy;
    }
  __syncthreads();
  f32x4 mf[2][4], my[2][4];
#pragma unroll
  for (int ii = 0; ii < 2; ++ii)
#pragma unroll
    for (int j = 0; j < 4; ++j) {
      f32x4 ownf = kg ? accf[2 + ii][j] : accf[ii][j];
      f32x4 owny = kg ? acc[2 + ii][j] : acc[ii][j];
      mf[ii][j] = ownf + ex[(((0 * 2 + (kg ^ 1)) * 4 + wq) * 8 + ii * 4 + j) * 64 + lane];
      my[ii][j] = owny;
      if (kz) my[ii][j] += ex[(((1 * 2 + (kg ^ 1)) * 4 + wq) * 8 + ii * 4 + j) * 64 + lane];
    }
  unsigned* role_s = reinterpret_cast<unsigned*>(smem + G2_NSLOT * G2_STAGE);
  if (tid == 0) *role_s = __hip_atomic_fetch_add(&cnt[tile * 32], 1u, __ATOMIC_RELAXED, __HIP_MEMORY_SCOPE_AGENT);
  __syncthreads();
  unsigned role = *role_s;
  f32x4* sl = slab + (size_t)tile * 8192;
  if (role == 0) {
#pragma unroll
    for (int ii = 0; ii < 2; ++ii)
#pragma unroll
      for (int j = 0; j < 4; ++j) {
        sl[(ii * 4 + j) * 512 + tid] = mf[ii][j];
        if (kz) sl[4096 + (ii * 4 + j) * 512 + tid] = my[ii][j];
      }
    asm volatile("s_waitcnt vmcnt(0)" ::: "memory");
    __syncthreads();
    if (tid == 0) {
      __builtin_amdgcn_fence(__ATOMIC_RELEASE, "agent");
      asm volatile("s_waitcnt vmcnt(0)" ::: "memory");
      __hip_atomic_store(&cnt[tile * 32 + 16], 1u, __ATOMIC_RELAXED, __HIP_MEMORY_SCOPE_AGENT);
    }
    return;
  }
  if (tid == 0) {
    while (__hip_atomic_load(&cnt[tile * 32 + 16], __ATOMIC_RELAXED, __HIP_MEMORY_SCOPE_AGENT) == 0u) __builtin_amdgcn_s_sleep(1);
    __builtin_amdgcn_fence(__ATOMIC_ACQUIRE, "agent");
    asm volatile("s_waitcnt vmcnt(0)" ::: "memory");
  }
  __syncthreads();
#pragma unroll
  for (int ii = 0; ii < 2; ++ii)
#pragma unroll
    for (int j = 0; j < 4; ++j) {
      mf[ii][j] += sl[(ii * 4 + j) * 512 + tid];
      if (!kz) my[ii][j] = sl[4096 + (ii * 4 + j) * 512 + tid];
    }
#pragma unroll
  for (int ii = 0; ii < 2; ++ii)
#pragma unroll
    for (int j = 0; j < 4; ++j) {
      int t = tm * 128 + wr * 64 + j * 16 + fr;
      int d = tn * 128 + wc * 64 + (kg * 2 + ii) * 16 + fq * 4;
      u16x4 xv = *reinterpret_cast<const u16x4*>(x + (size_t)t * D_ + d);
      u16x4 xnv = *reinterpret_cast<const u16x4*>(xn + (size_t)t * D_ + d);
      u16x4 dv = *reinterpret_cast<const u16x4*>(dskip + d);
      f32x4 o;
#pragma unroll
      for (int r = 0; r < 4; ++r) {
        float f = rbf(mf[ii][j][r]);
        float g = rbf(f / (1.0f + __expf(-f)));
        float s = 2.0f * my[ii][j][r] + rbf(bf2f(xnv[r]) * bf2f(dv[r]));
        o[r] = bf2f(xv[r]) + s * g;
      }
      *reinterpret_cast<f32x4*>(out + (size_t)t * D_ + d) = o;
    }
}

extern "C" void kernel_launch(void* const* d_in, const int* in_sizes, int n_in, void* d_out, int out_size, void* d_ws,
                              size_t ws_size, hipStream_t stream) {
  const unsigned short* x = (const unsigned short*)d_in[0];
  const unsigned short* lre = (const unsigned short*)d_in[1];
  const unsigned short* lim = (const unsigned short*)d_in[2];
  const unsigned short* bre = (const unsigned short*)d_in[3];
  const unsigned short* bim = (const unsigned short*)d_in[4];
  const unsigned short* cre = (const unsigned short*)d_in[5];
  const unsigned short* cim = (const unsigned short*)d_in[6];
  const unsigned short* logdt = (const unsigned short*)d_in[7];
  const unsigned short* dskip = (const unsigned short*)d_in[8];
  const unsigned short* wgate = (const unsigned short*)d_in[10];
  const unsigned short* wup = (const unsigned short*)d_in[11];
  const unsigned short* wdown = (const unsigned short*)d_in[12];
  char* ws = (char*)d_ws;
  unsigned short* xn = (unsigned short*)(ws + OFF_XN);
  unsigned short* w1t = (unsigned short*)(ws + OFF_W1T);
  unsigned short* w2t = (unsigned short*)(ws + OFF_W2T);
  unsigned short* a2 = (unsigned short*)(ws + OFF_A2);
  float* bu = (float*)(ws + OFF_BU);
  float2* lam = (float2*)(ws + OFF_LAM);
  float* out = (float*)d_out;

  hipMemsetAsync(ws + OFF_CNT, 0, 128 * 32 * 4, stream);
  k_ln<<<S_ / 4, 256, 0, stream>>>(x, xn);
  k_tr<0><<<dim3(F_ / 64, D_ / 64), 256, 0, stream>>>(wgate, w1t, D_, F_);
  k_tr<1><<<dim3(F_ / 64, D_ / 64), 256, 0, stream>>>(wup, w1t, D_, F_);
  k_tr<2><<<dim3(D_ / 64, F_ / 64), 256, 0, stream>>>(wdown, w2t, F_, D_);
  k_bbar<<<P_, 256, 0, stream>>>(lre, lim, bre, bim, logdt, w1t, lam);
  k_cprep<<<D_, 256, 0, stream>>>(cre, cim, w2t);
  hipFuncSetAttribute((const void*)k_gemm1, hipFuncAttributeMaxDynamicSharedMemorySize, G1_NSLOT * G1_STAGE);
  k_gemm1<<<256, 512, G1_NSLOT * G1_STAGE, stream>>>(xn, w1t, a2, bu);
  k_scan<<<64, 1024, 0, stream>>>(bu, lam, a2);
  hipFuncSetAttribute((const void*)k_gemm2, hipFuncAttributeMaxDynamicSharedMemorySize, G2_NSLOT * G2_STAGE + 16);
  k_gemm2<<<256, 512, G2_NSLOT * G2_STAGE + 16, stream>>>(a2, w2t, x, xn, dskip, out, (f32x4*)(ws + OFF_SLAB), (unsigned*)(ws + OFF_CNT));
}
```

```cpp
#include <hip/hip_runtime.h>
#include <hip/hip_bf16.h>
#include <stdint.h>

typedef __attribute__((ext_vector_type(8))) short bf16x8;
typedef __attribute__((ext_vector_type(4))) float f32x4;
typedef __attribute__((ext_vector_type(4))) unsigned short u16x4;
typedef __attribute__((ext_vector_type(4))) unsigned u32x4;

constexpr int S_ = 2048;
constexpr int D_ = 1024;
constexpr int P_ = 1024;
constexpr int F_ = 3584;
constexpr int N1_ = 9216;
constexpr int K2_ = 5632;

constexpr size_t OFF_TICK = 0;
constexpr size_t OFF_AGG = 1024;
constexpr size_t OFF_CNT = OFF_AGG + 32 * 8 * 32 * 2 * 8;
constexpr size_t SYNC_BYTES = OFF_CNT + 128 * 32 * 4;
constexpr size_t OFF_LAM = SYNC_BYTES;
constexpr size_t OFF_XN = OFF_LAM + 8192;
constexpr size_t OFF_W1T = OFF_XN + (size_t)S_ * D_ * 2;
constexpr size_t OFF_W2T = OFF_W1T + (size_t)N1_ * D_ * 2;
constexpr size_t OFF_A2 = OFF_W2T + (size_t)D_ * K2_ * 2;
constexpr size_t OFF_SLAB = OFF_A2 + (size_t)S_ * K2_ * 2;

__device__ __forceinline__ float bf2f(unsigned short v) { return __uint_as_float(((unsigned)v) << 16); }
__device__ __forceinline__ unsigned short f2bf(float f) {
  unsigned u = __float_as_uint(f);
  unsigned r = 0x7FFFu + ((u >> 16) & 1u);
  return (unsigned short)((u + r) >> 16);
}
__device__ __forceinline__ float rbf(float f) { return bf2f(f2bf(f)); }
__device__ __forceinline__ float2 cmul(float2 a, float2 b) { return make_float2(a.x * b.x - a.y * b.y, a.x * b.y + a.y * b.x); }

#define WAIT_VM(n) asm volatile("s_waitcnt vmcnt(" #n ")" ::: "memory")
#define LDS_PTR(p) ((__attribute__((address_space(3))) unsigned*)(p))

__device__ __forceinline__ int w1_row_gate(int col) { int tn = col / 112, c = col % 112; return tn * 288 + (c < 64 ? c : 144 + (c - 64)); }
__device__ __forceinline__ int w1_row_up(int col)   { int tn = col / 112, c = col % 112; return tn * 288 + (c < 64 ? 64 + c : 192 + (c - 64)); }
__device__ __forceinline__ int w1_row_bre(int p)    { int tn = p >> 5, c = p & 31; return tn * 288 + (c < 16 ? 128 + c : 240 + (c - 16)); }
__device__ __forceinline__ int w1_row_bim(int p)    { int tn = p >> 5, c = p & 31; return tn * 288 + 256 + c; }

constexpr int PB_LN = 512, PB_TR = 896, PB_BB = 256, PB_CP = 256;
constexpr int PB_TOTAL = PB_LN + 3 * PB_TR + PB_BB + PB_CP;

struct PrepArgs {
  const unsigned short *x, *lre, *lim, *bre, *bim, *cre, *cim, *logdt, *wgate, *wup, *wdown;
  char* ws;
};

__global__ __launch_bounds__(256) void k_prep(PrepArgs a) {
  __shared__ __attribute__((aligned(16))) unsigned short tile[64][72];
  int b = blockIdx.x, tid = threadIdx.x, wave = tid >> 6, lane = tid & 63;
  unsigned short* xn = (unsigned short*)(a.ws + OFF_XN);
  unsigned short* w1t = (unsigned short*)(a.ws + OFF_W1T);
  unsigned short* w2t = (unsigned short*)(a.ws + OFF_W2T);
  if (b < PB_LN) {
    for (int i = b * 256 + tid; i < (int)(SYNC_BYTES / 16); i += PB_LN * 256)
      reinterpret_cast<u32x4*>(a.ws)[i] = (u32x4){0u, 0u, 0u, 0u};
    int row = b * 4 + wave;
    const bf16x8* src = reinterpret_cast<const bf16x8*>(a.x + (size_t)row * D_);
    bf16x8 v0 = src[lane], v1 = src[64 + lane];
    float f[16];
#pragma unroll
    for (int j = 0; j < 8; ++j) { f[j] = bf2f((unsigned short)v0[j]); f[8 + j] = bf2f((unsigned short)v1[j]); }
    float s = 0.f;
#pragma unroll
    for (int j = 0; j < 16; ++j) s += f[j];
#pragma unroll
    for (int o = 32; o >= 1; o >>= 1) s += __shfl_xor(s, o);
    float mean = s * (1.0f / D_);
    float q = 0.f;
#pragma unroll
    for (int j = 0; j < 16; ++j) { float d = f[j] - mean; q += d * d; }
#pragma unroll
    for (int o = 32; o >= 1; o >>= 1) q += __shfl_xor(q, o);
    float var = q * (1.0f / D_);
    float mb = rbf(mean), vb = rbf(var);
    float den = rbf(sqrtf(rbf(vb + 1e-5f)));
    bf16x8 o0, o1;
#pragma unroll
    for (int j = 0; j < 8; ++j) {
      o0[j] = (short)f2bf(rbf(f[j] - mb) / den);
      o1[j] = (short)f2bf(rbf(f[8 + j] - mb) / den);
    }
    bf16x8* dst = reinterpret_cast<bf16x8*>(xn + (size_t)row * D_);
    dst[lane] = o0; dst[64 + lane] = o1;
    return;
  }
  b -= PB_LN;
  if (b < 3 * PB_TR) {
    int mode = b / PB_TR; b -= mode * PB_TR;
    const unsigned short* src; int C, tr, tc;
    if (mode < 2) { src = mode ? a.wup : a.wgate; C = F_; tc = b % 56; tr = b / 56; }
    else { src = a.wdown; C = D_; tc = b & 15; tr = b >> 4; }
    int r0 = tr * 64, c0 = tc * 64;
#pragma unroll
    for (int i = 0; i < 2; ++i) {
      int idx = i * 256 + tid; int r = idx >> 3, ch = idx & 7;
      *reinterpret_cast<bf16x8*>(&tile[r][ch * 8]) = *reinterpret_cast<const bf16x8*>(src + (size_t)(r0 + r) * C + c0 + ch * 8);
    }
    __syncthreads();
#pragma unroll
    for (int i = 0; i < 2; ++i) {
      int idx = i * 256 + tid; int c = idx >> 3, rc = idx & 7;
      bf16x8 v;
#pragma unroll
      for (int j = 0; j < 8; ++j) v[j] = (short)tile[rc * 8 + j][c];
      int col = c0 + c;
      size_t drow;
      unsigned short* dst;
      if (mode == 0) { drow = (size_t)w1_row_gate(col) * D_; dst = w1t; }
      else if (mode == 1) { drow = (size_t)w1_row_up(col) * D_; dst = w1t; }
      else { drow = (size_t)col * K2_; dst = w2t; }
      *reinterpret_cast<bf16x8*>(dst + drow + r0 + rc * 8) = v;
    }
    return;
  }
  b -= 3 * PB_TR;
  if (b < PB_BB) {
    int p = b * 4 + wave;
    float lr = bf2f(a.lre[p]), li = bf2f(a.lim[p]);
    float dt = rbf(expf(bf2f(a.logdt[p])));
    float zr = dt * lr, zi = dt * li;
    float e = expf(zr);
    float br_ = e * cosf(zi), bi_ = e * sinf(zi);
    float nr = br_ - 1.0f, ni = bi_;
    float den = lr * lr + li * li;
    float cr = (nr * lr + ni * li) / den, ci = (ni * lr - nr * li) / den;
    if (lane == 0) reinterpret_cast<float2*>(a.ws + OFF_LAM)[p] = make_float2(br_, bi_);
    size_t rr = (size_t)w1_row_bre(p) * D_, ri = (size_t)w1_row_bim(p) * D_;
#pragma unroll
    for (int i = 0; i < 2; ++i) {
      int d0 = (i * 64 + lane) * 8;
      bf16x8 va = *reinterpret_cast<const bf16x8*>(a.bre + (size_t)p * D_ + d0);
      bf16x8 vb = *reinterpret_cast<const bf16x8*>(a.bim + (size_t)p * D_ + d0);
      bf16x8 o1, o2;
#pragma unroll
      for (int j = 0; j < 8; ++j) {
        float x1 = bf2f((unsigned short)va[j]), x2 = bf2f((unsigned short)vb[j]);
        o1[j] = (short)f2bf(cr * x1 - ci * x2);
        o2[j] = (short)f2bf(cr * x2 + ci * x1);
      }
      *reinterpret_cast<bf16x8*>(w1t + rr + d0) = o1;
      *reinterpret_cast<bf16x8*>(w1t + ri + d0) = o2;
    }
    return;
  }
  b -= PB_BB;
  {
    int d = b * 4 + wave;
#pragma unroll
    for (int i = 0; i < 2; ++i) {
      int p0 = (i * 64 + lane) * 8;
      bf16x8 v1 = *reinterpret_cast<const bf16x8*>(a.cre + (size_t)d * P_ + p0);
      bf16x8 v2 = *reinterpret_cast<const bf16x8*>(a.cim + (size_t)d * P_ + p0);
#pragma unroll
      for (int j = 0; j < 8; ++j) v2[j] = (short)(v2[j] ^ (short)0x8000);
      *reinterpret_cast<bf16x8*>(w2t + (size_t)d * K2_ + F_ + p0) = v1;
      *reinterpret_cast<bf16x8*>(w2t + (size_t)d * K2_ + F_ + P_ + p0) = v2;
    }
  }
}

__device__ __forceinline__ float gelu_tanh(float x) {
  float u2 = 1.5957691216057308f * (x + 0.044715f * x * x * x);
  return x * __frcp_rn(1.0f + __expf(-u2));
}

constexpr int G1_AT = 256 * 64;
constexpr int G1_WT = 288 * 64;
constexpr int G1_STAGE = G1_AT + G1_WT;
constexpr int G1_NSLOT = 4;
constexpr int G1_LDS = G1_NSLOT * G1_STAGE;
constexpr int E_BUL = 0;
constexpr int E_ACT = 69632;
constexpr int E_CAG = 131072;
constexpr int E_TERM = 135168;
constexpr int E_MISC = 137216;

__global__ __launch_bounds__(512, 2) void k_gemm1(const unsigned short* __restrict__ xn, const unsigned short* __restrict__ w1t,
                                                  unsigned short* __restrict__ a2, const float2* __restrict__ lamv,
                                                  unsigned* __restrict__ tick, unsigned long long* __restrict__ agg) {
  extern __shared__ __attribute__((aligned(16))) char smem[];
  int tid = threadIdx.x, wid = __builtin_amdgcn_readfirstlane(tid >> 6), lane = tid & 63;
  int xl = blockIdx.x & 7;
  {
    unsigned* mw = reinterpret_cast<unsigned*>(smem + E_MISC);
    if (tid == 0) mw[0] = __hip_atomic_fetch_add(&tick[xl * 32], 1u, __ATOMIC_RELAXED, __HIP_MEMORY_SCOPE_AGENT);
    __syncthreads();
  }
  int loc = __builtin_amdgcn_readfirstlane(*reinterpret_cast<volatile unsigned*>(smem + E_MISC));
  __syncthreads();
  int tn = xl * 4 + (loc >> 3), tm = loc & 7;
  int wr = wid >> 1, wc = wid & 1;
  int fr = lane & 15, fq = lane >> 4;
  const unsigned short* xg = xn + (size_t)tm * 256 * D_;
  const unsigned short* wg = w1t + (size_t)tn * 288 * D_;

  f32x4 acc[9][4];
#pragma unroll
  for (int i = 0; i < 9; ++i)
#pragma unroll
    for (int j = 0; j < 4; ++j) acc[i][j] = (f32x4){0.f, 0.f, 0.f, 0.f};

  int srow = lane >> 2;
  int schunk = (lane & 3) ^ (((srow >> 3) & 1) << 1);
  auto stage = [&](int slot, int ks) {
    char* base = smem + slot * G1_STAGE;
#pragma unroll
    for (int i = 0; i < 2; ++i) {
      int ins = wid * 2 + i;
      __builtin_amdgcn_global_load_lds((const unsigned*)(xg + (size_t)(ins * 16 + srow) * D_ + ks * 32 + schunk * 8),
                                       LDS_PTR(base + ins * 1024), 16, 0, 0);
    }
#pragma unroll
    for (int i = 0; i < 2; ++i) {
      int ins = wid * 2 + i;
      __builtin_amdgcn_global_load_lds((const unsigned*)(wg + (size_t)(ins * 16 + srow) * D_ + ks * 32 + schunk * 8),
                                       LDS_PTR(base + G1_AT + ins * 1024), 16, 0, 0);
    }
    if (wid < 2) {
      int ins = 16 + wid;
      __builtin_amdgcn_global_load_lds((const unsigned*)(wg + (size_t)(ins * 16 + srow) * D_ + ks * 32 + schunk * 8),
                                       LDS_PTR(base + G1_AT + ins * 1024), 16, 0, 0);
    }
  };

  const int ns = D_ / 32;
  stage(0, 0); stage(1, 1); stage(2, 2); stage(3, 3);
  int rchunk = fq ^ (((fr >> 3) & 1) << 1);
  const int xoff = (wr * 64 + fr) * 64 + rchunk * 16;
  const int woff = G1_AT + (wc * 144 + fr) * 64 + rchunk * 16;
  bf16x8 xa_[4], xb_[4], wf[9];
  if (wid < 2) WAIT_VM(15); else WAIT_VM(12);
  __builtin_amdgcn_s_barrier();
#pragma unroll
  for (int j = 0; j < 4; ++j) xa_[j] = *reinterpret_cast<const bf16x8*>(smem + xoff + j * 1024);
#pragma unroll
  for (int i = 0; i < 9; ++i) wf[i] = *reinterpret_cast<const bf16x8*>(smem + woff + i * 1024);

  auto body = [&](int s, bf16x8 (&xc)[4], bf16x8 (&xnx)[4]) {
    if (s < ns - 3) { if (wid < 2) WAIT_VM(10); else WAIT_VM(8); }
    else if (s == ns - 3) { if (wid < 2) WAIT_VM(5); else WAIT_VM(4); }
    else WAIT_VM(0);
    __builtin_amdgcn_s_barrier();
    if (s + 4 < ns) stage(s & 3, s + 4);
    const char* nb = smem + ((s + 1) & 3) * G1_STAGE;
    bool more = (s + 1 < ns);
#pragma unroll
    for (int i = 0; i < 9; ++i) {
#pragma unroll
      for (int j = 0; j < 4; ++j) acc[i][j] = __builtin_amdgcn_mfma_f32_16x16x32_bf16(wf[i], xc[j], acc[i][j], 0, 0, 0);
      if (more) wf[i] = *reinterpret_cast<const bf16x8*>(nb + woff + i * 1024);
      if (more && i < 4) xnx[i] = *reinterpret_cast<const bf16x8*>(nb + xoff + i * 1024);
    }
#pragma unroll
    for (int i = 0; i < 4; ++i) {
      __builtin_amdgcn_sched_group_barrier(0x008, 4, 0);
      __builtin_amdgcn_sched_group_barrier(0x100, 2, 0);
    }
#pragma unroll
    for (int i = 0; i < 5; ++i) {
      __builtin_amdgcn_sched_group_barrier(0x008, 4, 0);
      __builtin_amdgcn_sched_group_barrier(0x100, 1, 0);
    }
  };
  for (int s = 0; s < ns; s += 2) {
    body(s, xa_, xb_);
    body(s + 1, xb_, xa_);
  }

  __syncthreads();
  float* bul = reinterpret_cast<float*>(smem + E_BUL);
  unsigned short* actl = reinterpret_cast<unsigned short*>(smem + E_ACT);
  float2* cag = reinterpret_cast<float2*>(smem + E_CAG);
  float2* term = reinterpret_cast<float2*>(smem + E_TERM);
  int tl = wr * 64 + fr;
  if (wc == 0) {
#pragma unroll
    for (int j = 0; j < 4; ++j) *reinterpret_cast<f32x4*>(bul + (tl + j * 16) * 68 + fq * 4) = acc[8][j];
  } else {
#pragma unroll
    for (int j = 0; j < 4; ++j) {
      *reinterpret_cast<f32x4*>(bul + (tl + j * 16) * 68 + 16 + fq * 4) = acc[6][j];
      *reinterpret_cast<f32x4*>(bul + (tl + j * 16) * 68 + 32 + fq * 4) = acc[7][j];
      *reinterpret_cast<f32x4*>(bul + (tl + j * 16) * 68 + 48 + fq * 4) = acc[8][j];
    }
  }
  __syncthreads();
  int ch = tid & 31, ck = tid >> 5;
  int p = tn * 32 + ch;
  float2 lam = lamv[p];
  float hr[16], hi[16];
  {
    float sr = 0.f, si = 0.f;
#pragma unroll
    for (int i = 0; i < 16; ++i) {
      float br = bul[(ck * 16 + i) * 68 + ch], bi = bul[(ck * 16 + i) * 68 + 32 + ch];
      float nr = lam.x * sr - lam.y * si + br;
      float ni = lam.x * si + lam.y * sr + bi;
      sr = nr; si = ni; hr[i] = sr; hi[i] = si;
    }
    cag[ck * 32 + ch] = make_float2(sr, si);
  }
  float2 l16 = lam;
#pragma unroll
  for (int i = 0; i < 4; ++i) l16 = cmul(l16, l16);
  float2 l256 = l16;
#pragma unroll
  for (int i = 0; i < 4; ++i) l256 = cmul(l256, l256);
  __syncthreads();
  if (tid < 32) {
    float2 c = make_float2(0.f, 0.f);
#pragma unroll
    for (int k = 0; k < 16; ++k) {
      float2 aa = cag[k * 32 + ch];
      cag[k * 32 + ch] = c;
      float2 t2 = cmul(l16, c);
      c = make_float2(t2.x + aa.x, t2.y + aa.y);
    }
    if (tm < 7) {
      unsigned long long* g = agg + ((size_t)(tn * 8 + tm) * 32 + ch) * 2;
      __hip_atomic_store(g, (1ull << 32) | (unsigned long long)__float_as_uint(c.x), __ATOMIC_RELAXED, __HIP_MEMORY_SCOPE_AGENT);
      __hip_atomic_store(g + 1, (1ull << 32) | (unsigned long long)__float_as_uint(c.y), __ATOMIC_RELAXED, __HIP_MEMORY_SCOPE_AGENT);
    }
  }
  if (wc == 0) {
#pragma unroll
    for (int i = 0; i < 4; ++i)
#pragma unroll
      for (int j = 0; j < 4; ++j) {
        u16x4 o;
#pragma unroll
        for (int r = 0; r < 4; ++r) {
          float g = rbf(gelu_tanh(rbf(acc[i][j][r])));
          float u = rbf(acc[i + 4][j][r]);
          o[r] = f2bf(g * u);
        }
        *reinterpret_cast<u16x4*>(actl + (tl + j * 16) * 120 + i * 16 + fq * 4) = o;
      }
  } else {
#pragma unroll
    for (int i = 0; i < 3; ++i)
#pragma unroll
      for (int j = 0; j < 4; ++j) {
        u16x4 o;
#pragma unroll
        for (int r = 0; r < 4; ++r) {
          float g = rbf(gelu_tanh(rbf(acc[i][j][r])));
          float u = rbf(acc[i + 3][j][r]);
          o[r] = f2bf(g * u);
        }
        *reinterpret_cast<u16x4*>(actl + (tl + j * 16) * 120 + 64 + i * 16 + fq * 4) = o;
      }
  }
  __syncthreads();
#pragma unroll
  for (int it = 0; it < 7; ++it) {
    int idx = it * 512 + tid;
    int row = idx / 14, c = idx - row * 14;
    bf16x8 v = *reinterpret_cast<const bf16x8*>(actl + row * 120 + c * 8);
    *reinterpret_cast<bf16x8*>(a2 + (size_t)(tm * 256 + row) * K2_ + tn * 112 + c * 8) = v;
  }
  if (ck < tm) {
    const unsigned long long* g = agg + ((size_t)(tn * 8 + ck) * 32 + ch) * 2;
    unsigned long long v0, v1;
    for (;;) {
      v0 = __hip_atomic_load(g, __ATOMIC_RELAXED, __HIP_MEMORY_SCOPE_AGENT);
      v1 = __hip_atomic_load(g + 1, __ATOMIC_RELAXED, __HIP_MEMORY_SCOPE_AGENT);
      if ((v0 >> 32) == 1ull && (v1 >> 32) == 1ull) break;
      __builtin_amdgcn_s_sleep(1);
    }
    float2 av = make_float2(__uint_as_float((unsigned)v0), __uint_as_float((unsigned)v1));
    for (int e = tm - 1 - ck; e > 0; --e) av = cmul(av, l256);
    term[ck * 32 + ch] = av;
  }
  __syncthreads();
  float2 bc = make_float2(0.f, 0.f);
  for (int j = 0; j < tm; ++j) { float2 tv = term[j * 32 + ch]; bc.x += tv.x; bc.y += tv.y; }
  float2 pw = make_float2(1.f, 0.f);
  for (int e = 0; e < ck; ++e) pw = cmul(pw, l16);
  float2 c0 = cag[ck * 32 + ch];
  float2 t3 = cmul(pw, bc);
  c0.x += t3.x; c0.y += t3.y;
  unsigned short* hl = reinterpret_cast<unsigned short*>(smem + E_BUL);
  {
    float2 pq = lam;
#pragma unroll
    for (int i = 0; i < 16; ++i) {
      float vr = hr[i] + pq.x * c0.x - pq.y * c0.y;
      float vi = hi[i] + pq.x * c0.y + pq.y * c0.x;
      hl[(ck * 16 + i) * 72 + ch] = f2bf(vr);
      hl[(ck * 16 + i) * 72 + 32 + ch] = f2bf(vi);
      pq = cmul(pq, lam);
    }
  }
  __syncthreads();
#pragma unroll
  for (int it = 0; it < 4; ++it) {
    int idx = it * 512 + tid;
    int row = idx >> 3, c = idx & 7;
    bf16x8 v = *reinterpret_cast<const bf16x8*>(hl + row * 72 + c * 8);
    *reinterpret_cast<bf16x8*>(a2 + (size_t)(tm * 256 + row) * K2_ + F_ + (c >> 2) * P_ + tn * 32 + (c & 3) * 8) = v;
  }
}

constexpr int G2_STAGE = 32768;
constexpr int G2_NSLOT = 4;
constexpr int G2_LDS = G2_NSLOT * G2_STAGE + 16;
__global__ __launch_bounds__(512, 2) void k_gemm2(const unsigned short* __restrict__ a2, const unsigned short* __restrict__ w2t,
                                                  const unsigned short* __restrict__ x, const unsigned short* __restrict__ xn,
                                                  const unsigned short* __restrict__ dskip, float* __restrict__ out,
                                                  f32x4* __restrict__ slab, unsigned* __restrict__ cnt) {
  extern __shared__ __attribute__((aligned(16))) char smem[];
  int bid = blockIdx.x;
  int xcd = bid & 7, loc = bid >> 3;
  int tm = (xcd >> 1) * 4 + ((loc >> 3) & 3), tn = (xcd & 1) * 4 + ((loc >> 1) & 3), kz = loc & 1;
  int tile = tm * 8 + tn;
  int tid = threadIdx.x, wid = __builtin_amdgcn_readfirstlane(tid >> 6), lane = tid & 63;
  int kg = wid >> 2, wr = (wid >> 1) & 1, wc = wid & 1, fr = lane & 15, fq = lane >> 4;
  const unsigned short* ag = a2 + (size_t)tm * 128 * K2_ + kz * 2816;
  const unsigned short* wg = w2t + (size_t)tn * 128 * K2_ + kz * 2816;
  f32x4 acc[4][4], accf[4][4];
#pragma unroll
  for (int i = 0; i < 4; ++i)
#pragma unroll
    for (int j = 0; j < 4; ++j) { acc[i][j] = (f32x4){0, 0, 0, 0}; accf[i][j] = (f32x4){0, 0, 0, 0}; }
  int srow = lane >> 3, schunk = (lane & 7) ^ (srow & 7);
  auto stage = [&](int slot, int kt) {
    char* base = smem + slot * G2_STAGE;
#pragma unroll
    for (int i = 0; i < 2; ++i) {
      int ins = wid * 2 + i;
      __builtin_amdgcn_global_load_lds((const unsigned*)(ag + (size_t)(ins * 8 + srow) * K2_ + kt * 64 + schunk * 8),
                                       LDS_PTR(base + ins * 1024), 16, 0, 0);
      __builtin_amdgcn_global_load_lds((const unsigned*)(wg + (size_t)(ins * 8 + srow) * K2_ + kt * 64 + schunk * 8),
                                       LDS_PTR(base + 16384 + ins * 1024), 16, 0, 0);
    }
  };
  const int ns = 44;
  const int nsf = kz ? 12 : 44;
  stage(0, 0); stage(1, 1); stage(2, 2); stage(3, 3);
  int rch = ((kg * 4 + fq) ^ (fr & 7)) * 16;
  const int xoff = (wr * 64 + fr) * 128 + rch;
  const int woff = 16384 + (wc * 64 + fr) * 128 + rch;
  bf16x8 xa_[4], xb_[4], wa_[4], wb_[4];
  WAIT_VM(12);
  __builtin_amdgcn_s_barrier();
#pragma unroll
  for (int j = 0; j < 4; ++j) {
    xa_[j] = *reinterpret_cast<const bf16x8*>(smem + xoff + j * 2048);
    wa_[j] = *reinterpret_cast<const bf16x8*>(smem + woff + j * 2048);
  }
  auto body = [&](int s, bf16x8 (&xc)[4], bf16x8 (&wcur)[4], bf16x8 (&xnx)[4], bf16x8 (&wnx)[4]) {
    if (s < ns - 3) WAIT_VM(8);
    else if (s == ns - 3) WAIT_VM(4);
    else WAIT_VM(0);
    __builtin_amdgcn_s_barrier();
    if (s + 4 < ns) stage(s & 3, s + 4);
    const char* nb = smem + ((s + 1) & 3) * G2_STAGE;
    bool more = (s + 1 < ns);
#pragma unroll
    for (int i = 0; i < 4; ++i) {
#pragma unroll
      for (int j = 0; j < 4; ++j) acc[i][j] = __builtin_amdgcn_mfma_f32_16x16x32_bf16(wcur[i], xc[j], acc[i][j], 0, 0, 0);
      if (more) {
        wnx[i] = *reinterpret_cast<const bf16x8*>(nb + woff + i * 2048);
        xnx[i] = *reinterpret_cast<const bf16x8*>(nb + xoff + i * 2048);
      }
    }
#pragma unroll
    for (int i = 0; i < 4; ++i) {
      __builtin_amdgcn_sched_group_barrier(0x008, 4, 0);
      __builtin_amdgcn_sched_group_barrier(0x100, 2, 0);
    }
    if (s + 1 == nsf) {
#pragma unroll
      for (int i = 0; i < 4; ++i)
#pragma unroll
        for (int j = 0; j < 4; ++j) { accf[i][j] = acc[i][j]; acc[i][j] = (f32x4){0, 0, 0, 0}; }
    }
  };
  for (int s = 0; s < ns; s += 2) {
    body(s, xa_, wa_, xb_, wb_);
    body(s + 1, xb_, wb_, xa_, wa_);
  }
  __syncthreads();
  f32x4* ex = reinterpret_cast<f32x4*>(smem);
  int wq = wid & 3;
#pragma unroll
  for (int ii = 0; ii < 2; ++ii)
#pragma unroll
    for (int j = 0; j < 4; ++j) {
      f32x4 sf = kg ? accf[ii][j] : accf[2 + ii][j];
      f32x4 sy = kg ? acc[ii][j] : acc[2 + ii][j];
      ex[(((0 * 2 + kg) * 4 + wq) * 8 + ii * 4 + j) * 64 + lane] = sf;
      if (kz) ex[(((1 * 2 + kg) * 4 + wq) * 8 + ii * 4 + j) * 64 + lane] = sy;
    }
  unsigned* role_s = reinterpret_cast<unsigned*>(smem + G2_NSLOT * G2_STAGE);
  if (tid == 0) *role_s = __hip_atomic_fetch_add(&cnt[tile * 32], 1u, __ATOMIC_RELAXED, __HIP_MEMORY_SCOPE_AGENT);
  __syncthreads();
  f32x4 mf[2][4], my[2][4];
#pragma unroll
  for (int ii = 0; ii < 2; ++ii)
#pragma unroll
    for (int j = 0; j < 4; ++j) {
      f32x4 ownf = kg ? accf[2 + ii][j] : accf[ii][j];
      f32x4 owny = kg ? acc[2 + ii][j] : acc[ii][j];
      mf[ii][j] = ownf + ex[(((0 * 2 + (kg ^ 1)) * 4 + wq) * 8 + ii * 4 + j) * 64 + lane];
      my[ii][j] = owny;
      if (kz) my[ii][j] += ex[(((1 * 2 + (kg ^ 1)) * 4 + wq) * 8 + ii * 4 + j) * 64 + lane];
    }
  unsigned role = *role_s;
  f32x4* sl = slab + (size_t)tile * 8192;
  if (role == 0) {
    __amdgpu_buffer_rsrc_t rs = __builtin_amdgcn_make_buffer_rsrc((void*)sl, 0, 8192 * 16, 0x00020000);
#pragma unroll
    for (int ii = 0; ii < 2; ++ii)
#pragma unroll
      for (int j = 0; j < 4; ++j) {
        u32x4 v = __builtin_bit_cast(u32x4, mf[ii][j]);
        __builtin_amdgcn_raw_buffer_store_b128(v, rs, ((ii * 4 + j) * 512 + tid) * 16, 0, 16);
        if (kz) {
          u32x4 w = __builtin_bit_cast(u32x4, my[ii][j]);
          __builtin_amdgcn_raw_buffer_store_b128(w, rs, (4096 + (ii * 4 + j) * 512 + tid) * 16, 0, 16);
        }
      }
    asm volatile("s_waitcnt vmcnt(0)" ::: "memory");
    __syncthreads();
    if (tid == 0) __hip_atomic_store(&cnt[tile * 32 + 16], 1u, __ATOMIC_RELAXED, __HIP_MEMORY_SCOPE_AGENT);
    return;
  }
  if (tid == 0) {
    while (__hip_atomic_load(&cnt[tile * 32 + 16], __ATOMIC_RELAXED, __HIP_MEMORY_SCOPE_AGENT) == 0u) __builtin_amdgcn_s_sleep(1);
    __builtin_amdgcn_fence(__ATOMIC_ACQUIRE, "agent");
    asm volatile("s_waitcnt vmcnt(0)" ::: "memory");
  }
  __syncthreads();
#pragma unroll
  for (int ii = 0; ii < 2; ++ii)
#pragma unroll
    for (int j = 0; j < 4; ++j) {
      mf[ii][j] += sl[(ii * 4 + j) * 512 + tid];
      if (!kz) my[ii][j] = sl[4096 + (ii * 4 + j) * 512 + tid];
    }
#pragma unroll
  for (int ii = 0; ii < 2; ++ii)
#pragma unroll
    for (int j = 0; j < 4; ++j) {
      int t = tm * 128 + wr * 64 + j * 16 + fr;
      int d = tn * 128 + wc * 64 + (kg * 2 + ii) * 16 + fq * 4;
      u16x4 xv = *reinterpret_cast<const u16x4*>(x + (size_t)t * D_ + d);
      u16x4 xnv = *reinterpret_cast<const u16x4*>(xn + (size_t)t * D_ + d);
      u16x4 dv = *reinterpret_cast<const u16x4*>(dskip + d);
      f32x4 o;
#pragma unroll
      for (int r = 0; r < 4; ++r) {
        float f = rbf(mf[ii][j][r]);
        float g = rbf(f * __frcp_rn(1.0f + __expf(-f)));
        float s = 2.0f * my[ii][j][r] + rbf(bf2f(xnv[r]) * bf2f(dv[r]));
        o[r] = bf2f(xv[r]) + s * g;
      }
      *reinterpret_cast<f32x4*>(out + (size_t)t * D_ + d) = o;
    }
}

extern "C" void kernel_launch(void* const* d_in, const int* in_sizes, int n_in, void* d_out, int out_size, void* d_ws,
                              size_t ws_size, hipStream_t stream) {
  PrepArgs pa;
  pa.x = (const unsigned short*)d_in[0];
  pa.lre = (const unsigned short*)d_in[1];
  pa.lim = (const unsigned short*)d_in[2];
  pa.bre = (const unsigned short*)d_in[3];
  pa.bim = (const unsigned short*)d_in[4];
  pa.cre = (const unsigned short*)d_in[5];
  pa.cim = (const unsigned short*)d_in[6];
  pa.logdt = (const unsigned short*)d_in[7];
  const unsigned short* dskip = (const unsigned short*)d_in[8];
  pa.wgate = (const unsigned short*)d_in[10];
  pa.wup = (const unsigned short*)d_in[11];
  pa.wdown = (const unsigned short*)d_in[12];
  char* ws = (char*)d_ws;
  pa.ws = ws;
  unsigned short* xn = (unsigned short*)(ws + OFF_XN);
  unsigned short* w1t = (unsigned short*)(ws + OFF_W1T);
  unsigned short* w2t = (unsigned short*)(ws + OFF_W2T);
  unsigned short* a2 = (unsigned short*)(ws + OFF_A2);
  float* out = (float*)d_out;

  k_prep<<<PB_TOTAL, 256, 0, stream>>>(pa);
  (void)hipFuncSetAttribute((const void*)k_gemm1, hipFuncAttributeMaxDynamicSharedMemorySize, G1_LDS);
  k_gemm1<<<256, 512, G1_LDS, stream>>>(xn, w1t, a2, (const float2*)(ws + OFF_LAM), (unsigned*)(ws + OFF_TICK),
                                        (unsigned long long*)(ws + OFF_AGG));
  (void)hipFuncSetAttribute((const void*)k_gemm2, hipFuncAttributeMaxDynamicSharedMemorySize, G2_LDS);
  k_gemm2<<<256, 512, G2_LDS, stream>>>(a2, w2t, pa.x, xn, dskip, out, (f32x4*)(ws + OFF_SLAB), (unsigned*)(ws + OFF_CNT));
}
```

```cpp
#include <hip/hip_runtime.h>
#include <hip/hip_bf16.h>
#include <stdint.h>

typedef __attribute__((ext_vector_type(8))) short bf16x8;
typedef __attribute__((ext_vector_type(4))) float f32x4;
typedef __attribute__((ext_vector_type(4))) unsigned short u16x4;
typedef __attribute__((ext_vector_type(4))) unsigned u32x4;

constexpr int S_ = 2048;
constexpr int D_ = 1024;
constexpr int P_ = 1024;
constexpr int F_ = 3584;
constexpr int N1_ = 9216;
constexpr int K2_ = 5632;

constexpr size_t OFF_TICK = 0;
constexpr size_t OFF_AGG = 1024;
constexpr size_t OFF_CNT = OFF_AGG + 32 * 8 * 32 * 2 * 8;
constexpr size_t SYNC_BYTES = OFF_CNT + 128 * 32 * 4;
constexpr size_t OFF_LAM = SYNC_BYTES;
constexpr size_t OFF_XN = OFF_LAM + 8192;
constexpr size_t OFF_BBT = OFF_XN + (size_t)S_ * D_ * 2;
constexpr size_t OFF_C2 = OFF_BBT + (size_t)D_ * 2048 * 2;
constexpr size_t OFF_A2 = OFF_C2 + (size_t)2048 * D_ * 2;
constexpr size_t OFF_SLAB = OFF_A2 + (size_t)S_ * K2_ * 2;

__device__ __forceinline__ float bf2f(unsigned short v) { return __uint_as_float(((unsigned)v) << 16); }
__device__ __forceinline__ unsigned short f2bf(float f) {
  __bf16 b = (__bf16)f;
  return __builtin_bit_cast(unsigned short, b);
}
typedef __attribute__((ext_vector_type(2))) __bf16 bf16x2n;
typedef __attribute__((ext_vector_type(2))) float f32x2;
__device__ __forceinline__ unsigned pk2bf(float a, float b) {
  f32x2 v = {a, b};
  bf16x2n r = __builtin_convertvector(v, bf16x2n);
  return __builtin_bit_cast(unsigned, r);
}
__device__ __forceinline__ float lo2f(unsigned u) { return __uint_as_float(u << 16); }
__device__ __forceinline__ float hi2f(unsigned u) { return __uint_as_float(u & 0xFFFF0000u); }
__device__ __forceinline__ float rbf(float f) { return bf2f(f2bf(f)); }
__device__ __forceinline__ float2 cmul(float2 a, float2 b) { return make_float2(a.x * b.x - a.y * b.y, a.x * b.y + a.y * b.x); }

#define STAMP(k, i)
#define WAIT_VM(n) asm volatile("s_waitcnt vmcnt(" #n ")" ::: "memory")
#define LDS_PTR(p) ((__attribute__((address_space(3))) unsigned*)(p))

constexpr int PB_LN = 512, PB_BB = 256, PB_CP = 512;
constexpr int PB_TOTAL = PB_LN + PB_BB + PB_CP;

struct PrepArgs {
  const unsigned short *x, *lre, *lim, *bre, *bim, *cre, *cim, *logdt;
  char* ws;
};

__global__ __launch_bounds__(256) void k_prep(PrepArgs a) {
  __shared__ __attribute__((aligned(16))) unsigned short tile[2][64][72];
  __shared__ float2 coef[64];
  int b = blockIdx.x, tid = threadIdx.x, wave = tid >> 6, lane = tid & 63;
  if (b < PB_LN) {
    unsigned short* xn = (unsigned short*)(a.ws + OFF_XN);
    for (int i = b * 256 + tid; i < (int)(SYNC_BYTES / 16); i += PB_LN * 256)
      reinterpret_cast<u32x4*>(a.ws)[i] = (u32x4){0u, 0u, 0u, 0u};
    int row = b * 4 + wave;
    const bf16x8* src = reinterpret_cast<const bf16x8*>(a.x + (size_t)row * D_);
    bf16x8 v0 = src[lane], v1 = src[64 + lane];
    float f[16];
#pragma unroll
    for (int j = 0; j < 8; ++j) { f[j] = bf2f((unsigned short)v0[j]); f[8 + j] = bf2f((unsigned short)v1[j]); }
    float s = 0.f;
#pragma unroll
    for (int j = 0; j < 16; ++j) s += f[j];
#pragma unroll
    for (int o = 32; o >= 1; o >>= 1) s += __shfl_xor(s, o);
    float mean = s * (1.0f / D_);
    float q = 0.f;
#pragma unroll
    for (int j = 0; j < 16; ++j) { float d = f[j] - mean; q += d * d; }
#pragma unroll
    for (int o = 32; o >= 1; o >>= 1) q += __shfl_xor(q, o);
    float var = q * (1.0f / D_);
    float mb = rbf(mean), vb = rbf(var);
    float den = rbf(sqrtf(rbf(vb + 1e-5f)));
    bf16x8 o0, o1;
#pragma unroll
    for (int j = 0; j < 8; ++j) {
      o0[j] = (short)f2bf(rbf(f[j] - mb) / den);
      o1[j] = (short)f2bf(rbf(f[8 + j] - mb) / den);
    }
    bf16x8* dst = reinterpret_cast<bf16x8*>(xn + (size_t)row * D_);
    dst[lane] = o0; dst[64 + lane] = o1;
    return;
  }
  b -= PB_LN;
  if (b < PB_BB) {
    int tp = b >> 4, td = b & 15;
    int p0 = tp * 64, d0 = td * 64;
    if (tid < 64) {
      int p = p0 + tid;
      float lr = bf2f(a.lre[p]), li = bf2f(a.lim[p]);
      float dt = rbf(expf(bf2f(a.logdt[p])));
      float zr = dt * lr, zi = dt * li;
      float e = expf(zr);
      float br_ = e * cosf(zi), bi_ = e * sinf(zi);
      float nr = br_ - 1.0f, ni = bi_;
      float den = lr * lr + li * li;
      coef[tid] = make_float2((nr * lr + ni * li) / den, (ni * lr - nr * li) / den);
      if (td == 0) reinterpret_cast<float2*>(a.ws + OFF_LAM)[p] = make_float2(br_, bi_);
    }
#pragma unroll
    for (int i = 0; i < 2; ++i) {
      int idx = i * 256 + tid; int r = idx >> 3, ch = idx & 7;
      *reinterpret_cast<bf16x8*>(&tile[0][r][ch * 8]) = *reinterpret_cast<const bf16x8*>(a.bre + (size_t)(p0 + r) * D_ + d0 + ch * 8);
      *reinterpret_cast<bf16x8*>(&tile[1][r][ch * 8]) = *reinterpret_cast<const bf16x8*>(a.bim + (size_t)(p0 + r) * D_ + d0 + ch * 8);
    }
    __syncthreads();
    unsigned short* bbt = (unsigned short*)(a.ws + OFF_BBT);
#pragma unroll
    for (int i = 0; i < 2; ++i) {
      int idx = i * 256 + tid; int c = idx >> 3, rc = idx & 7;
      bf16x8 vr, vi;
#pragma unroll
      for (int j = 0; j < 8; ++j) {
        float x1 = bf2f(tile[0][rc * 8 + j][c]), x2 = bf2f(tile[1][rc * 8 + j][c]);
        float2 cf = coef[rc * 8 + j];
        vr[j] = (short)f2bf(cf.x * x1 - cf.y * x2);
        vi[j] = (short)f2bf(cf.x * x2 + cf.y * x1);
      }
      *reinterpret_cast<bf16x8*>(bbt + (size_t)(d0 + c) * 2048 + p0 + rc * 8) = vr;
      *reinterpret_cast<bf16x8*>(bbt + (size_t)(d0 + c) * 2048 + 1024 + p0 + rc * 8) = vi;
    }
    return;
  }
  b -= PB_BB;
  {
    int im = b >> 8; b &= 255;
    int td = b >> 4, tp = b & 15;
    int d0 = td * 64, p0 = tp * 64;
    const unsigned short* src = im ? a.cim : a.cre;
#pragma unroll
    for (int i = 0; i < 2; ++i) {
      int idx = i * 256 + tid; int r = idx >> 3, ch = idx & 7;
      *reinterpret_cast<bf16x8*>(&tile[0][r][ch * 8]) = *reinterpret_cast<const bf16x8*>(src + (size_t)(d0 + r) * P_ + p0 + ch * 8);
    }
    __syncthreads();
    unsigned short* c2 = (unsigned short*)(a.ws + OFF_C2);
    unsigned short sg = im ? 0x8000u : 0u;
#pragma unroll
    for (int i = 0; i < 2; ++i) {
      int idx = i * 256 + tid; int c = idx >> 3, rc = idx & 7;
      bf16x8 v;
#pragma unroll
      for (int j = 0; j < 8; ++j) v[j] = (short)(tile[0][rc * 8 + j][c] ^ sg);
      *reinterpret_cast<bf16x8*>(c2 + (size_t)(im * 1024 + p0 + c) * D_ + d0 + rc * 8) = v;
    }
  }
}

typedef __attribute__((ext_vector_type(4))) short s16x4;
__device__ __forceinline__ bf16x8 tr_frag(const char* p) {
  s16x4 lo = __builtin_amdgcn_ds_read_tr16_b64_v4i16((__attribute__((address_space(3))) s16x4*)(p));
  s16x4 hi = __builtin_amdgcn_ds_read_tr16_b64_v4i16((__attribute__((address_space(3))) s16x4*)(p + 512));
  return __builtin_shufflevector(lo, hi, 0, 1, 2, 3, 4, 5, 6, 7);
}

__device__ __forceinline__ float gelu_tanh(float x) {
  float w = x * __builtin_fmaf(x * x, -0.10294323970650479f, -2.3022081985952363f);
  return x * __builtin_amdgcn_rcpf(1.0f + __builtin_amdgcn_exp2f(w));
}

constexpr int G1_AT = 256 * 64;
constexpr int G1_WT = 288 * 64;
constexpr int G1_STAGE = G1_AT + G1_WT;
constexpr int G1_NSLOT = 4;
constexpr int G1_LDS = G1_NSLOT * G1_STAGE;
constexpr int E_BUL = 0;
constexpr int E_ACT = 69632;
constexpr int E_CAG = 131072;
constexpr int E_TERM = 135168;
constexpr int E_MISC = 137216;

__global__ __launch_bounds__(512, 2) void k_gemm1(const unsigned short* __restrict__ xn, const unsigned short* __restrict__ wgate,
                                                  const unsigned short* __restrict__ wup, const unsigned short* __restrict__ bbt,
                                                  unsigned short* __restrict__ a2, const float2* __restrict__ lamv,
                                                  unsigned* __restrict__ tick, unsigned long long* __restrict__ agg) {
  extern __shared__ __attribute__((aligned(16))) char smem[];
  int tid = threadIdx.x, wid = __builtin_amdgcn_readfirstlane(tid >> 6), lane = tid & 63;
  int xl = blockIdx.x & 7;
  STAMP(0, 0);
  {
    unsigned* mw = reinterpret_cast<unsigned*>(smem + E_MISC);
    if (tid == 0) mw[0] = __hip_atomic_fetch_add(&tick[xl * 32], 1u, __ATOMIC_RELAXED, __HIP_MEMORY_SCOPE_AGENT);
    __syncthreads();
  }
  int loc = __builtin_amdgcn_readfirstlane(*reinterpret_cast<volatile unsigned*>(smem + E_MISC));
  __syncthreads();
  int tn = xl * 4 + (loc >> 3), tm = loc & 7;
  int wr = wid >> 1, wc = wid & 1;
  int fr = lane & 15, fq = lane >> 4;
  const unsigned short* xg = xn + (size_t)tm * 256 * D_;

  f32x4 acc[9][4];
#pragma unroll
  for (int i = 0; i < 9; ++i)
#pragma unroll
    for (int j = 0; j < 4; ++j) acc[i][j] = (f32x4){0.f, 0.f, 0.f, 0.f};

  int srow = lane >> 2;
  int schunk = (lane & 3) ^ (((srow >> 3) & 1) << 1);
  const char* wsrc[3]; int wstr[3];
#pragma unroll
  for (int i = 0; i < 3; ++i) {
    int reg = (i < 2) ? wid * 2 + i : 16 + (wid & 1);
    const unsigned short* mat; int col, k, ld;
    if (reg < 16) {
      int Q = reg >> 2, kb = reg & 3;
      int pi = lane ^ ((kb & 1) << 3);
      int r = ((pi >> 5) << 2) | ((pi >> 1) & 3), Gl = (pi >> 3) & 3, h = pi & 1;
      k = kb * 8 + r;
      int G = Q * 4 + Gl;
      if (G < 4) { mat = wgate; col = tn * 112 + G * 16; ld = F_; }
      else if (G < 8) { mat = wup; col = tn * 112 + (G - 4) * 16; ld = F_; }
      else if (G == 8) { mat = bbt; col = tn * 32; ld = 2048; }
      else if (G < 12) { mat = wgate; col = tn * 112 + 64 + (G - 9) * 16; ld = F_; }
      else if (G < 15) { mat = wup; col = tn * 112 + 64 + (G - 12) * 16; ld = F_; }
      else { mat = bbt; col = tn * 32 + 16; ld = 2048; }
      col += h * 8;
    } else {
      int unit = (lane >> 3) & 3, gl = unit >> 1, kbl = unit & 1;
      int r = ((lane >> 5) << 2) | ((lane >> 1) & 3), h = lane & 1;
      k = ((reg - 16) * 2 + kbl) * 8 + r;
      mat = bbt; col = 1024 + tn * 32 + gl * 16 + h * 8; ld = 2048;
    }
    wsrc[i] = (const char*)(mat + (size_t)k * ld + col);
    wstr[i] = ld * 64;
  }
  auto stage = [&](int slot, int ks) {
    char* base = smem + slot * G1_STAGE;
#pragma unroll
    for (int i = 0; i < 2; ++i) {
      int ins = wid * 2 + i;
      __builtin_amdgcn_global_load_lds((const unsigned*)(xg + (size_t)(ins * 16 + srow) * D_ + ks * 32 + schunk * 8),
                                       LDS_PTR(base + ins * 1024), 16, 0, 0);
    }
#pragma unroll
    for (int i = 0; i < 2; ++i)
      __builtin_amdgcn_global_load_lds((const unsigned*)(wsrc[i] + (size_t)ks * wstr[i]), LDS_PTR(base + G1_AT + (wid * 2 + i) * 1024), 16, 0, 0);
    if (wid < 2)
      __builtin_amdgcn_global_load_lds((const unsigned*)(wsrc[2] + (size_t)ks * wstr[2]), LDS_PTR(base + G1_AT + (16 + wid) * 1024), 16, 0, 0);
  };

  const int ns = D_ / 32;
  stage(0, 0); stage(1, 1); stage(2, 2); stage(3, 3);
  int rchunk = fq ^ (((fr >> 3) & 1) << 1);
  const int xoff = (wr * 64 + fr) * 64 + rchunk * 16;
  int wofs[9];
  {
    int kb = fq, q = (lane >> 2) & 3, pp = lane & 3;
    int inner = (q * 2 + (pp >> 1)) * 16 + (pp & 1) * 8;
#pragma unroll
    for (int i = 0; i < 9; ++i) {
      int G = wc * 9 + i;
      if (G < 16) wofs[i] = G1_AT + ((G >> 2) * 4 + kb) * 1024 + (((G & 3) ^ (kb & 1)) * 128) + inner;
      else wofs[i] = G1_AT + (16 + (kb >> 1)) * 1024 + (((G - 16) * 2 + (kb & 1)) * 128) + inner;
    }
  }
  bf16x8 xa_[4], xb_[4], wf[9];
  if (wid < 2) WAIT_VM(15); else WAIT_VM(12);
  __builtin_amdgcn_s_barrier();
#pragma unroll
  for (int j = 0; j < 4; ++j) xa_[j] = *reinterpret_cast<const bf16x8*>(smem + xoff + j * 1024);
#pragma unroll
  for (int i = 0; i < 9; ++i) wf[i] = tr_frag(smem + wofs[i]);

  auto body = [&](int s, bf16x8 (&xc)[4], bf16x8 (&xnx)[4]) {
    if (s < ns - 3) { if (wid < 2) WAIT_VM(10); else WAIT_VM(8); }
    else if (s == ns - 3) { if (wid < 2) WAIT_VM(5); else WAIT_VM(4); }
    else WAIT_VM(0);
    __builtin_amdgcn_s_barrier();
    if (s + 4 < ns) stage(s & 3, s + 4);
    const char* nb = smem + ((s + 1) & 3) * G1_STAGE;
    bool more = (s + 1 < ns);
#pragma unroll
    for (int i = 0; i < 9; ++i) {
#pragma unroll
      for (int j = 0; j < 4; ++j) acc[i][j] = __builtin_amdgcn_mfma_f32_16x16x32_bf16(wf[i], xc[j], acc[i][j], 0, 0, 0);
      if (more) wf[i] = tr_frag(nb + wofs[i]);
      if (more && i < 4) xnx[i] = *reinterpret_cast<const bf16x8*>(nb + xoff + i * 1024);
    }
#pragma unroll
    for (int i = 0; i < 4; ++i) {
      __builtin_amdgcn_sched_group_barrier(0x008, 4, 0);
      __builtin_amdgcn_sched_group_barrier(0x100, 3, 0);
    }
#pragma unroll
    for (int i = 0; i < 5; ++i) {
      __builtin_amdgcn_sched_group_barrier(0x008, 4, 0);
      __builtin_amdgcn_sched_group_barrier(0x100, 2, 0);
    }
  };
  for (int s = 0; s < ns; s += 2) {
    body(s, xa_, xb_);
    body(s + 1, xb_, xa_);
  }

  __syncthreads();
  STAMP(0, 1);
  float* bul = reinterpret_cast<float*>(smem + E_BUL);
  unsigned short* actl = reinterpret_cast<unsigned short*>(smem + E_ACT);
  float2* cag = reinterpret_cast<float2*>(smem + E_CAG);
  float2* term = reinterpret_cast<float2*>(smem + E_TERM);
  int tl = wr * 64 + fr;
  if (wc == 0) {
#pragma unroll
    for (int j = 0; j < 4; ++j) *reinterpret_cast<f32x4*>(bul + (tl + j * 16) * 68 + fq * 4) = acc[8][j];
  } else {
#pragma unroll
    for (int j = 0; j < 4; ++j) {
      *reinterpret_cast<f32x4*>(bul + (tl + j * 16) * 68 + 16 + fq * 4) = acc[6][j];
      *reinterpret_cast<f32x4*>(bul + (tl + j * 16) * 68 + 32 + fq * 4) = acc[7][j];
      *reinterpret_cast<f32x4*>(bul + (tl + j * 16) * 68 + 48 + fq * 4) = acc[8][j];
    }
  }
  __syncthreads();
  int ch = tid & 31, ck = tid >> 5;
  int p = tn * 32 + ch;
  float2 lam = lamv[p];
  float hr[16], hi[16];
  {
    float sr = 0.f, si = 0.f;
#pragma unroll
    for (int i = 0; i < 16; ++i) {
      float br = bul[(ck * 16 + i) * 68 + ch], bi = bul[(ck * 16 + i) * 68 + 32 + ch];
      float nr = lam.x * sr - lam.y * si + br;
      float ni = lam.x * si + lam.y * sr + bi;
      sr = nr; si = ni; hr[i] = sr; hi[i] = si;
    }
    cag[ck * 32 + ch] = make_float2(sr, si);
  }
  float2 l16 = lam;
#pragma unroll
  for (int i = 0; i < 4; ++i) l16 = cmul(l16, l16);
  float2 l256 = l16;
#pragma unroll
  for (int i = 0; i < 4; ++i) l256 = cmul(l256, l256);
  __syncthreads();
  if (tid < 32) {
    float2 c = make_float2(0.f, 0.f);
#pragma unroll
    for (int k = 0; k < 16; ++k) {
      float2 aa = cag[k * 32 + ch];
      cag[k * 32 + ch] = c;
      float2 t2 = cmul(l16, c);
      c = make_float2(t2.x + aa.x, t2.y + aa.y);
    }
    if (tm < 7) {
      unsigned long long* g = agg + ((size_t)(tn * 8 + tm) * 32 + ch) * 2;
      __hip_atomic_store(g, (1ull << 32) | (unsigned long long)__float_as_uint(c.x), __ATOMIC_RELAXED, __HIP_MEMORY_SCOPE_AGENT);
      __hip_atomic_store(g + 1, (1ull << 32) | (unsigned long long)__float_as_uint(c.y), __ATOMIC_RELAXED, __HIP_MEMORY_SCOPE_AGENT);
    }
  }
  STAMP(0, 2);
  auto geglu4 = [&](const f32x4& gt, const f32x4& up) -> uint2 {
    unsigned g01 = pk2bf(gt[0], gt[1]), g23 = pk2bf(gt[2], gt[3]);
    unsigned u01 = pk2bf(up[0], up[1]), u23 = pk2bf(up[2], up[3]);
    unsigned a01 = pk2bf(gelu_tanh(lo2f(g01)), gelu_tanh(hi2f(g01)));
    unsigned a23 = pk2bf(gelu_tanh(lo2f(g23)), gelu_tanh(hi2f(g23)));
    uint2 o;
    o.x = pk2bf(lo2f(a01) * lo2f(u01), hi2f(a01) * hi2f(u01));
    o.y = pk2bf(lo2f(a23) * lo2f(u23), hi2f(a23) * hi2f(u23));
    return o;
  };
  if (wc == 0) {
#pragma unroll
    for (int i = 0; i < 4; ++i)
#pragma unroll
      for (int j = 0; j < 4; ++j)
        *reinterpret_cast<uint2*>(actl + (tl + j * 16) * 120 + i * 16 + fq * 4) = geglu4(acc[i][j], acc[i + 4][j]);
  } else {
#pragma unroll
    for (int i = 0; i < 3; ++i)
#pragma unroll
      for (int j = 0; j < 4; ++j)
        *reinterpret_cast<uint2*>(actl + (tl + j * 16) * 120 + 64 + i * 16 + fq * 4) = geglu4(acc[i][j], acc[i + 3][j]);
  }
  __syncthreads();
  STAMP(0, 3);
#pragma unroll
  for (int it = 0; it < 7; ++it) {
    int idx = it * 512 + tid;
    int row = idx / 14, c = idx - row * 14;
    bf16x8 v = *reinterpret_cast<const bf16x8*>(actl + row * 120 + c * 8);
    *reinterpret_cast<bf16x8*>(a2 + (size_t)(tm * 256 + row) * K2_ + tn * 112 + c * 8) = v;
  }
  STAMP(0, 4);
  if (ck < tm) {
    const unsigned long long* g = agg + ((size_t)(tn * 8 + ck) * 32 + ch) * 2;
    unsigned long long v0, v1;
    for (;;) {
      v0 = __hip_atomic_load(g, __ATOMIC_RELAXED, __HIP_MEMORY_SCOPE_AGENT);
      v1 = __hip_atomic_load(g + 1, __ATOMIC_RELAXED, __HIP_MEMORY_SCOPE_AGENT);
      if ((v0 >> 32) == 1ull && (v1 >> 32) == 1ull) break;
      __builtin_amdgcn_s_sleep(1);
    }
    float2 av = make_float2(__uint_as_float((unsigned)v0), __uint_as_float((unsigned)v1));
    for (int e = tm - 1 - ck; e > 0; --e) av = cmul(av, l256);
    term[ck * 32 + ch] = av;
  }
  __syncthreads();
  STAMP(0, 5);
  float2 bc = make_float2(0.f, 0.f);
  for (int j = 0; j < tm; ++j) { float2 tv = term[j * 32 + ch]; bc.x += tv.x; bc.y += tv.y; }
  float2 pw = make_float2(1.f, 0.f);
  for (int e = 0; e < ck; ++e) pw = cmul(pw, l16);
  float2 c0 = cag[ck * 32 + ch];
  float2 t3 = cmul(pw, bc);
  c0.x += t3.x; c0.y += t3.y;
  unsigned short* hl = reinterpret_cast<unsigned short*>(smem + E_BUL);
  {
    float2 pq = lam;
#pragma unroll
    for (int i = 0; i < 16; ++i) {
      float vr = hr[i] + pq.x * c0.x - pq.y * c0.y;
      float vi = hi[i] + pq.x * c0.y + pq.y * c0.x;
      hl[(ck * 16 + i) * 72 + ch] = f2bf(vr);
      hl[(ck * 16 + i) * 72 + 32 + ch] = f2bf(vi);
      pq = cmul(pq, lam);
    }
  }
  __syncthreads();
#pragma unroll
  for (int it = 0; it < 4; ++it) {
    int idx = it * 512 + tid;
    int row = idx >> 3, c = idx & 7;
    bf16x8 v = *reinterpret_cast<const bf16x8*>(hl + row * 72 + c * 8);
    *reinterpret_cast<bf16x8*>(a2 + (size_t)(tm * 256 + row) * K2_ + F_ + (c >> 2) * P_ + tn * 32 + (c & 3) * 8) = v;
  }
  STAMP(0, 6);
}

constexpr int G2_STAGE = 32768;
constexpr int G2_NSLOT = 4;
constexpr int G2_LDS = G2_NSLOT * G2_STAGE + 16;
__global__ __launch_bounds__(512, 2) void k_gemm2(const unsigned short* __restrict__ a2, const unsigned short* __restrict__ wdown,
                                                  const unsigned short* __restrict__ c2, const unsigned short* __restrict__ x,
                                                  const unsigned short* __restrict__ xn, const unsigned short* __restrict__ dskip,
                                                  float* __restrict__ out,
                                                  f32x4* __restrict__ slab, unsigned* __restrict__ cnt) {
  extern __shared__ __attribute__((aligned(16))) char smem[];
  int bid = blockIdx.x;
  int xcd = bid & 7, loc = bid >> 3;
  int tm = (xcd >> 1) * 4 + ((loc >> 3) & 3), tn = (xcd & 1) * 4 + ((loc >> 1) & 3), kz = loc & 1;
  int tile = tm * 8 + tn;
  STAMP(1, 0);
  int tid = threadIdx.x, wid = __builtin_amdgcn_readfirstlane(tid >> 6), lane = tid & 63;
  int kg = wid >> 2, wr = (wid >> 1) & 1, wc = wid & 1, fr = lane & 15, fq = lane >> 4;
  const unsigned short* ag = a2 + (size_t)tm * 128 * K2_ + kz * 2560;
  f32x4 acc[4][4], accf[4][4];
#pragma unroll
  for (int i = 0; i < 4; ++i)
#pragma unroll
    for (int j = 0; j < 4; ++j) { acc[i][j] = (f32x4){0, 0, 0, 0}; accf[i][j] = (f32x4){0, 0, 0, 0}; }
  int srow = lane >> 3, schunk = (lane & 7) ^ (srow & 7);
  int wlofs[2];
#pragma unroll
  for (int i = 0; i < 2; ++i) {
    int reg = wid * 2 + i, Q = reg >> 3, kb = reg & 7;
    int pi = lane ^ ((kb & 1) << 3);
    int r = ((pi >> 5) << 2) | ((pi >> 1) & 3), Gl = (pi >> 3) & 3, h = pi & 1;
    wlofs[i] = (kb * 8 + r) * D_ + tn * 128 + Q * 64 + Gl * 16 + h * 8;
  }
  auto stage = [&](int slot, int kt) {
    char* base = smem + slot * G2_STAGE;
    int kabs = kz * 2560 + kt * 64;
    const unsigned short* wb = (kabs < F_) ? wdown + (size_t)kabs * D_ : c2 + (size_t)(kabs - F_) * D_;
#pragma unroll
    for (int i = 0; i < 2; ++i) {
      int ins = wid * 2 + i;
      __builtin_amdgcn_global_load_lds((const unsigned*)(ag + (size_t)(ins * 8 + srow) * K2_ + kt * 64 + schunk * 8),
                                       LDS_PTR(base + ins * 1024), 16, 0, 0);
      __builtin_amdgcn_global_load_lds((const unsigned*)(wb + wlofs[i]), LDS_PTR(base + 16384 + ins * 1024), 16, 0, 0);
    }
  };
  const int ns = kz ? 48 : 40;
  const int nsf = kz ? 16 : 40;
  stage(0, 0); stage(1, 1); stage(2, 2); stage(3, 3);
  int rch = ((kg * 4 + fq) ^ (fr & 7)) * 16;
  const int xoff = (wr * 64 + fr) * 128 + rch;
  int wofs[4];
  {
    int kb = kg * 4 + fq, q = (lane >> 2) & 3, pp = lane & 3;
    int inner = (q * 2 + (pp >> 1)) * 16 + (pp & 1) * 8;
#pragma unroll
    for (int i = 0; i < 4; ++i) wofs[i] = 16384 + (wc * 8 + kb) * 1024 + ((i ^ (kb & 1)) * 128) + inner;
  }
  bf16x8 xa_[4], xb_[4], wa_[4], wb_[4];
  WAIT_VM(12);
  __builtin_amdgcn_s_barrier();
#pragma unroll
  for (int j = 0; j < 4; ++j) {
    xa_[j] = *reinterpret_cast<const bf16x8*>(smem + xoff + j * 2048);
    wa_[j] = tr_frag(smem + wofs[j]);
  }
  auto body = [&](int s, bf16x8 (&xc)[4], bf16x8 (&wcur)[4], bf16x8 (&xnx)[4], bf16x8 (&wnx)[4]) {
    if (s < ns - 3) WAIT_VM(8);
    else if (s == ns - 3) WAIT_VM(4);
    else WAIT_VM(0);
    __builtin_amdgcn_s_barrier();
    if (s + 4 < ns) stage(s & 3, s + 4);
    const char* nb = smem + ((s + 1) & 3) * G2_STAGE;
    bool more = (s + 1 < ns);
#pragma unroll
    for (int i = 0; i < 4; ++i) {
#pragma unroll
      for (int j = 0; j < 4; ++j) acc[i][j] = __builtin_amdgcn_mfma_f32_16x16x32_bf16(wcur[i], xc[j], acc[i][j], 0, 0, 0);
      if (more) {
        wnx[i] = tr_frag(nb + wofs[i]);
        xnx[i] = *reinterpret_cast<const bf16x8*>(nb + xoff + i * 2048);
      }
    }
#pragma unroll
    for (int i = 0; i < 4; ++i) {
      __builtin_amdgcn_sched_group_barrier(0x008, 4, 0);
      __builtin_amdgcn_sched_group_barrier(0x100, 3, 0);
    }
    if (s + 1 == nsf) {
#pragma unroll
      for (int i = 0; i < 4; ++i)
#pragma unroll
        for (int j = 0; j < 4; ++j) { accf[i][j] = acc[i][j]; acc[i][j] = (f32x4){0, 0, 0, 0}; }
    }
  };
  for (int s = 0; s < ns; s += 2) {
    body(s, xa_, wa_, xb_, wb_);
    body(s + 1, xb_, wb_, xa_, wa_);
  }
  __syncthreads();
  STAMP(1, 1);
  f32x4* ex = reinterpret_cast<f32x4*>(smem);
  int wq = wid & 3;
#pragma unroll
  for (int ii = 0; ii < 2; ++ii)
#pragma unroll
    for (int j = 0; j < 4; ++j) {
      f32x4 sf = kg ? accf[ii][j] : accf[2 + ii][j];
      f32x4 sy = kg ? acc[ii][j] : acc[2 + ii][j];
      ex[(((0 * 2 + kg) * 4 + wq) * 8 + ii * 4 + j) * 64 + lane] = sf;
      if (kz) ex[(((1 * 2 + kg) * 4 + wq) * 8 + ii * 4 + j) * 64 + lane] = sy;
    }
  unsigned* role_s = reinterpret_cast<unsigned*>(smem + G2_NSLOT * G2_STAGE);
  if (tid == 0) *role_s = __hip_atomic_fetch_add(&cnt[tile * 32], 1u, __ATOMIC_RELAXED, __HIP_MEMORY_SCOPE_AGENT);
  __syncthreads();
  f32x4 mf[2][4], my[2][4];
#pragma unroll
  for (int ii = 0; ii < 2; ++ii)
#pragma unroll
    for (int j = 0; j < 4; ++j) {
      f32x4 ownf = kg ? accf[2 + ii][j] : accf[ii][j];
      f32x4 owny = kg ? acc[2 + ii][j] : acc[ii][j];
      mf[ii][j] = ownf + ex[(((0 * 2 + (kg ^ 1)) * 4 + wq) * 8 + ii * 4 + j) * 64 + lane];
      my[ii][j] = owny;
      if (kz) my[ii][j] += ex[(((1 * 2 + (kg ^ 1)) * 4 + wq) * 8 + ii * 4 + j) * 64 + lane];
    }
  unsigned role = *role_s;
  STAMP(1, 2);
  f32x4* sl = slab + (size_t)tile * 8192;
  if (role == 0) {
    __amdgpu_buffer_rsrc_t rs = __builtin_amdgcn_make_buffer_rsrc((void*)sl, 0, 8192 * 16, 0x00020000);
#pragma unroll
    for (int ii = 0; ii < 2; ++ii)
#pragma unroll
      for (int j = 0; j < 4; ++j) {
        u32x4 v = __builtin_bit_cast(u32x4, mf[ii][j]);
        __builtin_amdgcn_raw_buffer_store_b128(v, rs, ((ii * 4 + j) * 512 + tid) * 16, 0, 16);
        if (kz) {
          u32x4 w = __builtin_bit_cast(u32x4, my[ii][j]);
          __builtin_amdgcn_raw_buffer_store_b128(w, rs, (4096 + (ii * 4 + j) * 512 + tid) * 16, 0, 16);
        }
      }
    asm volatile("s_waitcnt vmcnt(0)" ::: "memory");
    __syncthreads();
    if (tid == 0) __hip_atomic_store(&cnt[tile * 32 + 16], 1u, __ATOMIC_RELAXED, __HIP_MEMORY_SCOPE_AGENT);
    STAMP(1, 3);
    return;
  }
  u16x4 xv[2][4], xnv[2][4], dv[2];
#pragma unroll
  for (int ii = 0; ii < 2; ++ii) {
    int d = tn * 128 + wc * 64 + (kg * 2 + ii) * 16 + fq * 4;
    dv[ii] = *reinterpret_cast<const u16x4*>(dskip + d);
#pragma unroll
    for (int j = 0; j < 4; ++j) {
      int t = tm * 128 + wr * 64 + j * 16 + fr;
      xv[ii][j] = *reinterpret_cast<const u16x4*>(x + (size_t)t * D_ + d);
      xnv[ii][j] = *reinterpret_cast<const u16x4*>(xn + (size_t)t * D_ + d);
    }
  }
  if (tid == 0) {
    while (__hip_atomic_load(&cnt[tile * 32 + 16], __ATOMIC_RELAXED, __HIP_MEMORY_SCOPE_AGENT) == 0u) __builtin_amdgcn_s_sleep(1);
  }
  __syncthreads();
  __builtin_amdgcn_fence(__ATOMIC_ACQUIRE, "wavefront");
  STAMP(1, 3);
  {
    __amdgpu_buffer_rsrc_t rs = __builtin_amdgcn_make_buffer_rsrc((void*)sl, 0, 8192 * 16, 0x00020000);
#pragma unroll
    for (int ii = 0; ii < 2; ++ii)
#pragma unroll
      for (int j = 0; j < 4; ++j) {
        u32x4 v = __builtin_amdgcn_raw_buffer_load_b128(rs, ((ii * 4 + j) * 512 + tid) * 16, 0, 16);
        mf[ii][j] += __builtin_bit_cast(f32x4, v);
        if (!kz) {
          u32x4 w = __builtin_amdgcn_raw_buffer_load_b128(rs, (4096 + (ii * 4 + j) * 512 + tid) * 16, 0, 16);
          my[ii][j] = __builtin_bit_cast(f32x4, w);
        }
      }
  }
#pragma unroll
  for (int ii = 0; ii < 2; ++ii)
#pragma unroll
    for (int j = 0; j < 4; ++j) {
      int t = tm * 128 + wr * 64 + j * 16 + fr;
      int d = tn * 128 + wc * 64 + (kg * 2 + ii) * 16 + fq * 4;
      f32x4 o;
#pragma unroll
      for (int r = 0; r < 4; ++r) {
        float f = rbf(mf[ii][j][r]);
        float g = rbf(f * __builtin_amdgcn_rcpf(1.0f + __expf(-f)));
        float s = 2.0f * my[ii][j][r] + rbf(bf2f(xnv[ii][j][r]) * bf2f(dv[ii][r]));
        o[r] = bf2f(xv[ii][j][r]) + s * g;
      }
      *reinterpret_cast<f32x4*>(out + (size_t)t * D_ + d) = o;
    }
  STAMP(1, 4);
}

extern "C" void kernel_launch(void* const* d_in, const int* in_sizes, int n_in, void* d_out, int out_size, void* d_ws,
                              size_t ws_size, hipStream_t stream) {
  PrepArgs pa;
  pa.x = (const unsigned short*)d_in[0];
  pa.lre = (const unsigned short*)d_in[1];
  pa.lim = (const unsigned short*)d_in[2];
  pa.bre = (const unsigned short*)d_in[3];
  pa.bim = (const unsigned short*)d_in[4];
  pa.cre = (const unsigned short*)d_in[5];
  pa.cim = (const unsigned short*)d_in[6];
  pa.logdt = (const unsigned short*)d_in[7];
  const unsigned short* dskip = (const unsigned short*)d_in[8];
  const unsigned short* wgate = (const unsigned short*)d_in[10];
  const unsigned short* wup = (const unsigned short*)d_in[11];
  const unsigned short* wdown = (const unsigned short*)d_in[12];
  char* ws = (char*)d_ws;
  pa.ws = ws;
  unsigned short* xn = (unsigned short*)(ws + OFF_XN);
  unsigned short* a2 = (unsigned short*)(ws + OFF_A2);
  float* out = (float*)d_out;

  k_prep<<<PB_TOTAL, 256, 0, stream>>>(pa);
  (void)hipFuncSetAttribute((const void*)k_gemm1, hipFuncAttributeMaxDynamicSharedMemorySize, G1_LDS);
  k_gemm1<<<256, 512, G1_LDS, stream>>>(xn, wgate, wup, (const unsigned short*)(ws + OFF_BBT), a2, (const float2*)(ws + OFF_LAM),
                                        (unsigned*)(ws + OFF_TICK), (unsigned long long*)(ws + OFF_AGG));
  (void)hipFuncSetAttribute((const void*)k_gemm2, hipFuncAttributeMaxDynamicSharedMemorySize, G2_LDS);
  k_gemm2<<<256, 512, G2_LDS, stream>>>(a2, wdown, (const unsigned short*)(ws + OFF_C2), pa.x, xn, dskip, out,
                                        (f32x4*)(ws + OFF_SLAB), (unsigned*)(ws + OFF_CNT));
}
```

```cpp
#include <hip/hip_runtime.h>
#include <hip/hip_bf16.h>
#include <stdint.h>

typedef __attribute__((ext_vector_type(8))) short bf16x8;
typedef __attribute__((ext_vector_type(4))) float f32x4;
typedef __attribute__((ext_vector_type(4))) unsigned short u16x4;
typedef __attribute__((ext_vector_type(4))) unsigned u32x4;

constexpr int S_ = 2048;
constexpr int D_ = 1024;
constexpr int P_ = 1024;
constexpr int F_ = 3584;
constexpr int N1_ = 9216;
constexpr int K2_ = 5632;

constexpr size_t OFF_TICK = 0;
constexpr size_t OFF_AGG = 1024;
constexpr size_t OFF_CNT = OFF_AGG + 32 * 8 * 32 * 2 * 8;
constexpr size_t SYNC_BYTES = OFF_CNT + 128 * 32 * 4;
constexpr size_t OFF_LAM = SYNC_BYTES;
constexpr size_t OFF_XN = OFF_LAM + 8192;
constexpr size_t OFF_BBT = OFF_XN + (size_t)S_ * D_ * 2;
constexpr size_t OFF_C2 = OFF_BBT + (size_t)D_ * 2048 * 2;
constexpr size_t OFF_A2 = OFF_C2 + (size_t)2048 * D_ * 2;
constexpr size_t OFF_SLAB = OFF_A2 + (size_t)S_ * K2_ * 2;

__device__ __forceinline__ float bf2f(unsigned short v) { return __uint_as_float(((unsigned)v) << 16); }
__device__ __forceinline__ unsigned short f2bf(float f) {
  __bf16 b = (__bf16)f;
  return __builtin_bit_cast(unsigned short, b);
}
typedef __attribute__((ext_vector_type(2))) __bf16 bf16x2n;
typedef __attribute__((ext_vector_type(2))) float f32x2;
__device__ __forceinline__ unsigned pk2bf(float a, float b) {
  f32x2 v = {a, b};
  bf16x2n r = __builtin_convertvector(v, bf16x2n);
  return __builtin_bit_cast(unsigned, r);
}
__device__ __forceinline__ float lo2f(unsigned u) { return __uint_as_float(u << 16); }
__device__ __forceinline__ float hi2f(unsigned u) { return __uint_as_float(u & 0xFFFF0000u); }
__device__ __forceinline__ float rbf(float f) { return bf2f(f2bf(f)); }
__device__ __forceinline__ float2 cmul(float2 a, float2 b) { return make_float2(a.x * b.x - a.y * b.y, a.x * b.y + a.y * b.x); }

#define STAMP(k, i)
#define WAIT_VM(n) asm volatile("s_waitcnt vmcnt(" #n ")" ::: "memory")
#define LDS_PTR(p) ((__attribute__((address_space(3))) unsigned*)(p))

constexpr int PB_LN = 512, PB_BB = 256, PB_CP = 512;
constexpr int PB_TOTAL = PB_LN + PB_BB + PB_CP;

struct PrepArgs {
  const unsigned short *x, *lre, *lim, *bre, *bim, *cre, *cim, *logdt;
  char* ws;
};

__global__ __launch_bounds__(256) void k_prep(PrepArgs a) {
  __shared__ __attribute__((aligned(16))) unsigned short tile[2][64][72];
  __shared__ float2 coef[64];
  int b = blockIdx.x, tid = threadIdx.x, wave = tid >> 6, lane = tid & 63;
  if (b < PB_LN) {
    unsigned short* xn = (unsigned short*)(a.ws + OFF_XN);
    for (int i = b * 256 + tid; i < (int)(SYNC_BYTES / 16); i += PB_LN * 256)
      reinterpret_cast<u32x4*>(a.ws)[i] = (u32x4){0u, 0u, 0u, 0u};
    int row = b * 4 + wave;
    const bf16x8* src = reinterpret_cast<const bf16x8*>(a.x + (size_t)row * D_);
    bf16x8 v0 = src[lane], v1 = src[64 + lane];
    float f[16];
#pragma unroll
    for (int j = 0; j < 8; ++j) { f[j] = bf2f((unsigned short)v0[j]); f[8 + j] = bf2f((unsigned short)v1[j]); }
    float s = 0.f;
#pragma unroll
    for (int j = 0; j < 16; ++j) s += f[j];
#pragma unroll
    for (int o = 32; o >= 1; o >>= 1) s += __shfl_xor(s, o);
    float mean = s * (1.0f / D_);
    float q = 0.f;
#pragma unroll
    for (int j = 0; j < 16; ++j) { float d = f[j] - mean; q += d * d; }
#pragma unroll
    for (int o = 32; o >= 1; o >>= 1) q += __shfl_xor(q, o);
    float var = q * (1.0f / D_);
    float mb = rbf(mean), vb = rbf(var);
    float den = rbf(sqrtf(rbf(vb + 1e-5f)));
    bf16x8 o0, o1;
#pragma unroll
    for (int j = 0; j < 8; ++j) {
      o0[j] = (short)f2bf(rbf(f[j] - mb) / den);
      o1[j] = (short)f2bf(rbf(f[8 + j] - mb) / den);
    }
    bf16x8* dst = reinterpret_cast<bf16x8*>(xn + (size_t)row * D_);
    dst[lane] = o0; dst[64 + lane] = o1;
    return;
  }
  b -= PB_LN;
  if (b < PB_BB) {
    int tp = b >> 4, td = b & 15;
    int p0 = tp * 64, d0 = td * 64;
    if (tid < 64) {
      int p = p0 + tid;
      float lr = bf2f(a.lre[p]), li = bf2f(a.lim[p]);
      float dt = rbf(expf(bf2f(a.logdt[p])));
      float zr = dt * lr, zi = dt * li;
      float e = expf(zr);
      float br_ = e * cosf(zi), bi_ = e * sinf(zi);
      float nr = br_ - 1.0f, ni = bi_;
      float den = lr * lr + li * li;
      coef[tid] = make_float2((nr * lr + ni * li) / den, (ni * lr - nr * li) / den);
      if (td == 0) reinterpret_cast<float2*>(a.ws + OFF_LAM)[p] = make_float2(br_, bi_);
    }
#pragma unroll
    for (int i = 0; i < 2; ++i) {
      int idx = i * 256 + tid; int r = idx >> 3, ch = idx & 7;
      *reinterpret_cast<bf16x8*>(&tile[0][r][ch * 8]) = *reinterpret_cast<const bf16x8*>(a.bre + (size_t)(p0 + r) * D_ + d0 + ch * 8);
      *reinterpret_cast<bf16x8*>(&tile[1][r][ch * 8]) = *reinterpret_cast<const bf16x8*>(a.bim + (size_t)(p0 + r) * D_ + d0 + ch * 8);
    }
    __syncthreads();
    unsigned short* bbt = (unsigned short*)(a.ws + OFF_BBT);
#pragma unroll
    for (int i = 0; i < 2; ++i) {
      int idx = i * 256 + tid; int c = idx >> 3, rc = idx & 7;
      bf16x8 vr, vi;
#pragma unroll
      for (int j = 0; j < 8; ++j) {
        float x1 = bf2f(tile[0][rc * 8 + j][c]), x2 = bf2f(tile[1][rc * 8 + j][c]);
        float2 cf = coef[rc * 8 + j];
        vr[j] = (short)f2bf(cf.x * x1 - cf.y * x2);
        vi[j] = (short)f2bf(cf.x * x2 + cf.y * x1);
      }
      *reinterpret_cast<bf16x8*>(bbt + (size_t)(d0 + c) * 2048 + p0 + rc * 8) = vr;
      *reinterpret_cast<bf16x8*>(bbt + (size_t)(d0 + c) * 2048 + 1024 + p0 + rc * 8) = vi;
    }
    return;
  }
  b -= PB_BB;
  {
    int im = b >> 8; b &= 255;
    int td = b >> 4, tp = b & 15;
    int d0 = td * 64, p0 = tp * 64;
    const unsigned short* src = im ? a.cim : a.cre;
#pragma unroll
    for (int i = 0; i < 2; ++i) {
      int idx = i * 256 + tid; int r = idx >> 3, ch = idx & 7;
      *reinterpret_cast<bf16x8*>(&tile[0][r][ch * 8]) = *reinterpret_cast<const bf16x8*>(src + (size_t)(d0 + r) * P_ + p0 + ch * 8);
    }
    __syncthreads();
    unsigned short* c2 = (unsigned short*)(a.ws + OFF_C2);
    unsigned short sg = im ? 0x8000u : 0u;
#pragma unroll
    for (int i = 0; i < 2; ++i) {
      int idx = i * 256 + tid; int c = idx >> 3, rc = idx & 7;
      bf16x8 v;
#pragma unroll
      for (int j = 0; j < 8; ++j) v[j] = (short)(tile[0][rc * 8 + j][c] ^ sg);
      *reinterpret_cast<bf16x8*>(c2 + (size_t)(im * 1024 + p0 + c) * D_ + d0 + rc * 8) = v;
    }
  }
}

typedef __attribute__((ext_vector_type(4))) short s16x4;
template <int OFF2>
__device__ __forceinline__ bf16x8 tr_frag(const char* p) {
  s16x4 lo = __builtin_amdgcn_ds_read_tr16_b64_v4i16((__attribute__((address_space(3))) s16x4*)(p));
  s16x4 hi = __builtin_amdgcn_ds_read_tr16_b64_v4i16((__attribute__((address_space(3))) s16x4*)(p + OFF2));
  return __builtin_shufflevector(lo, hi, 0, 1, 2, 3, 4, 5, 6, 7);
}

__device__ __forceinline__ float gelu_tanh(float x) {
  float w = x * __builtin_fmaf(x * x, -0.10294323970650479f, -2.3022081985952363f);
  return x * __builtin_amdgcn_rcpf(1.0f + __builtin_amdgcn_exp2f(w));
}

constexpr int G1_AT = 256 * 64;
constexpr int G1_WT = 288 * 64;
constexpr int G1_STAGE = G1_AT + G1_WT;
constexpr int G1_NSLOT = 4;
constexpr int G1_LDS = G1_NSLOT * G1_STAGE;
constexpr int E_BUL = 0;
constexpr int E_ACT = 69632;
constexpr int E_CAG = 131072;
constexpr int E_TERM = 135168;
constexpr int E_MISC = 137216;

__global__ __launch_bounds__(512, 2) void k_gemm1(const unsigned short* __restrict__ xn, const unsigned short* __restrict__ wgate,
                                                  const unsigned short* __restrict__ wup, const unsigned short* __restrict__ bbt,
                                                  unsigned short* __restrict__ a2, const float2* __restrict__ lamv,
                                                  unsigned* __restrict__ tick, unsigned long long* __restrict__ agg) {
  extern __shared__ __attribute__((aligned(16))) char smem[];
  int tid = threadIdx.x, wid = __builtin_amdgcn_readfirstlane(tid >> 6), lane = tid & 63;
  int xl = blockIdx.x & 7;
  STAMP(0, 0);
  {
    unsigned* mw = reinterpret_cast<unsigned*>(smem + E_MISC);
    if (tid == 0) mw[0] = __hip_atomic_fetch_add(&tick[xl * 32], 1u, __ATOMIC_RELAXED, __HIP_MEMORY_SCOPE_AGENT);
    __syncthreads();
  }
  int loc = __builtin_amdgcn_readfirstlane(*reinterpret_cast<volatile unsigned*>(smem + E_MISC));
  __syncthreads();
  int tn = xl * 4 + (loc >> 3), tm = loc & 7;
  int wr = wid >> 1, wc = wid & 1;
  int fr = lane & 15, fq = lane >> 4;
  const unsigned short* xg = xn + (size_t)tm * 256 * D_;

  f32x4 acc[9][4];
#pragma unroll
  for (int i = 0; i < 9; ++i)
#pragma unroll
    for (int j = 0; j < 4; ++j) acc[i][j] = (f32x4){0.f, 0.f, 0.f, 0.f};

  int srow = lane >> 2;
  int schunk = (lane & 3) ^ (((srow >> 3) & 1) << 1);
  const char* wsrc[3]; int wstr[3];
#pragma unroll
  for (int i = 0; i < 3; ++i) {
    int reg = (i < 2) ? wid * 2 + i : 16 + (wid & 1);
    const unsigned short* mat; int col, k, ld;
    if (reg < 16) {
      int Q = reg >> 2, kb = reg & 3;
      int r = lane >> 3, c = (lane & 7) ^ (((r >> 1) & 1) << 1) ^ ((kb & 1) << 2);
      int Gl = c >> 1, h = c & 1;
      k = kb * 8 + r;
      int G = Q * 4 + Gl;
      if (G < 4) { mat = wgate; col = tn * 112 + G * 16; ld = F_; }
      else if (G < 8) { mat = wup; col = tn * 112 + (G - 4) * 16; ld = F_; }
      else if (G == 8) { mat = bbt; col = tn * 32; ld = 2048; }
      else if (G < 12) { mat = wgate; col = tn * 112 + 64 + (G - 9) * 16; ld = F_; }
      else if (G < 15) { mat = wup; col = tn * 112 + 64 + (G - 12) * 16; ld = F_; }
      else { mat = bbt; col = tn * 32 + 16; ld = 2048; }
      col += h * 8;
    } else {
      int row16 = lane >> 2, kbl = row16 >> 3, c = (lane & 3) ^ (kbl << 1);
      k = (reg - 16) * 16 + row16;
      mat = bbt; col = 1024 + tn * 32 + c * 8; ld = 2048;
    }
    wsrc[i] = (const char*)(mat + (size_t)k * ld + col);
    wstr[i] = ld * 64;
  }
  auto stage = [&](int slot, int ks) {
    char* base = smem + slot * G1_STAGE;
#pragma unroll
    for (int i = 0; i < 2; ++i) {
      int ins = wid * 2 + i;
      __builtin_amdgcn_global_load_lds((const unsigned*)(xg + (size_t)(ins * 16 + srow) * D_ + ks * 32 + schunk * 8),
                                       LDS_PTR(base + ins * 1024), 16, 0, 0);
    }
#pragma unroll
    for (int i = 0; i < 2; ++i)
      __builtin_amdgcn_global_load_lds((const unsigned*)(wsrc[i] + (size_t)ks * wstr[i]), LDS_PTR(base + G1_AT + (wid * 2 + i) * 1024), 16, 0, 0);
    if (wid < 2)
      __builtin_amdgcn_global_load_lds((const unsigned*)(wsrc[2] + (size_t)ks * wstr[2]), LDS_PTR(base + G1_AT + (16 + wid) * 1024), 16, 0, 0);
  };

  const int ns = D_ / 32;
  stage(0, 0); stage(1, 1); stage(2, 2); stage(3, 3);
  int rchunk = fq ^ (((fr >> 3) & 1) << 1);
  const int xoff = (wr * 64 + fr) * 64 + rchunk * 16;
  int wofs[9];
  {
    int kb = fq, q = (lane >> 2) & 3, pp = lane & 3;
    int sw = ((q >> 1) << 1) ^ ((kb & 1) << 2);
#pragma unroll
    for (int i = 0; i < 9; ++i) {
      int G = wc * 9 + i;
      if (G < 16) wofs[i] = G1_AT + ((G >> 2) * 4 + kb) * 1024 + q * 128 + ((((G & 3) * 2 + (pp >> 1)) ^ sw) * 16) + (pp & 1) * 8;
      else wofs[i] = G1_AT + (16 + (kb >> 1)) * 1024 + ((kb & 1) * 8 + q) * 64 + ((((G - 16) * 2 + (pp >> 1)) ^ ((kb & 1) << 1)) * 16) + (pp & 1) * 8;
    }
  }
  bf16x8 xa_[4], xb_[4], wf[9];
  if (wid < 2) WAIT_VM(15); else WAIT_VM(12);
  __builtin_amdgcn_s_barrier();
#pragma unroll
  for (int j = 0; j < 4; ++j) xa_[j] = *reinterpret_cast<const bf16x8*>(smem + xoff + j * 1024);
#pragma unroll
  for (int i = 0; i < 9; ++i) wf[i] = (wc * 9 + i < 16) ? tr_frag<512>(smem + wofs[i]) : tr_frag<256>(smem + wofs[i]);

  auto body = [&](int s, bf16x8 (&xc)[4], bf16x8 (&xnx)[4]) {
    if (s < ns - 3) { if (wid < 2) WAIT_VM(10); else WAIT_VM(8); }
    else if (s == ns - 3) { if (wid < 2) WAIT_VM(5); else WAIT_VM(4); }
    else WAIT_VM(0);
    __builtin_amdgcn_s_barrier();
    if (s + 4 < ns) stage(s & 3, s + 4);
    const char* nb = smem + ((s + 1) & 3) * G1_STAGE;
    bool more = (s + 1 < ns);
#pragma unroll
    for (int i = 0; i < 9; ++i) {
#pragma unroll
      for (int j = 0; j < 4; ++j) acc[i][j] = __builtin_amdgcn_mfma_f32_16x16x32_bf16(wf[i], xc[j], acc[i][j], 0, 0, 0);
      if (more) wf[i] = (wc * 9 + i < 16) ? tr_frag<512>(nb + wofs[i]) : tr_frag<256>(nb + wofs[i]);
      if (more && i < 4) xnx[i] = *reinterpret_cast<const bf16x8*>(nb + xoff + i * 1024);
    }
#pragma unroll
    for (int i = 0; i < 4; ++i) {
      __builtin_amdgcn_sched_group_barrier(0x008, 4, 0);
      __builtin_amdgcn_sched_group_barrier(0x100, 3, 0);
    }
#pragma unroll
    for (int i = 0; i < 5; ++i) {
      __builtin_amdgcn_sched_group_barrier(0x008, 4, 0);
      __builtin_amdgcn_sched_group_barrier(0x100, 2, 0);
    }
  };
  for (int s = 0; s < ns; s += 2) {
    body(s, xa_, xb_);
    body(s + 1, xb_, xa_);
  }

  __syncthreads();
  STAMP(0, 1);
  float* bul = reinterpret_cast<float*>(smem + E_BUL);
  unsigned short* actl = reinterpret_cast<unsigned short*>(smem + E_ACT);
  float2* cag = reinterpret_cast<float2*>(smem + E_CAG);
  float2* term = reinterpret_cast<float2*>(smem + E_TERM);
  int tl = wr * 64 + fr;
  if (wc == 0) {
#pragma unroll
    for (int j = 0; j < 4; ++j) *reinterpret_cast<f32x4*>(bul + (tl + j * 16) * 68 + fq * 4) = acc[8][j];
  } else {
#pragma unroll
    for (int j = 0; j < 4; ++j) {
      *reinterpret_cast<f32x4*>(bul + (tl + j * 16) * 68 + 16 + fq * 4) = acc[6][j];
      *reinterpret_cast<f32x4*>(bul + (tl + j * 16) * 68 + 32 + fq * 4) = acc[7][j];
      *reinterpret_cast<f32x4*>(bul + (tl + j * 16) * 68 + 48 + fq * 4) = acc[8][j];
    }
  }
  __syncthreads();
  int ch = tid & 31, ck = tid >> 5;
  int p = tn * 32 + ch;
  float2 lam = lamv[p];
  float hr[16], hi[16];
  {
    float sr = 0.f, si = 0.f;
#pragma unroll
    for (int i = 0; i < 16; ++i) {
      float br = bul[(ck * 16 + i) * 68 + ch], bi = bul[(ck * 16 + i) * 68 + 32 + ch];
      float nr = lam.x * sr - lam.y * si + br;
      float ni = lam.x * si + lam.y * sr + bi;
      sr = nr; si = ni; hr[i] = sr; hi[i] = si;
    }
    cag[ck * 32 + ch] = make_float2(sr, si);
  }
  float2 l16 = lam;
#pragma unroll
  for (int i = 0; i < 4; ++i) l16 = cmul(l16, l16);
  float2 l256 = l16;
#pragma unroll
  for (int i = 0; i < 4; ++i) l256 = cmul(l256, l256);
  __syncthreads();
  if (tid < 32) {
    float2 c = make_float2(0.f, 0.f);
#pragma unroll
    for (int k = 0; k < 16; ++k) {
      float2 aa = cag[k * 32 + ch];
      cag[k * 32 + ch] = c;
      float2 t2 = cmul(l16, c);
      c = make_float2(t2.x + aa.x, t2.y + aa.y);
    }
    if (tm < 7) {
      unsigned long long* g = agg + ((size_t)(tn * 8 + tm) * 32 + ch) * 2;
      __hip_atomic_store(g, (1ull << 32) | (unsigned long long)__float_as_uint(c.x), __ATOMIC_RELAXED, __HIP_MEMORY_SCOPE_AGENT);
      __hip_atomic_store(g + 1, (1ull << 32) | (unsigned long long)__float_as_uint(c.y), __ATOMIC_RELAXED, __HIP_MEMORY_SCOPE_AGENT);
    }
  }
  STAMP(0, 2);
  auto geglu4 = [&](const f32x4& gt, const f32x4& up) -> uint2 {
    unsigned g01 = pk2bf(gt[0], gt[1]), g23 = pk2bf(gt[2], gt[3]);
    unsigned u01 = pk2bf(up[0], up[1]), u23 = pk2bf(up[2], up[3]);
    unsigned a01 = pk2bf(gelu_tanh(lo2f(g01)), gelu_tanh(hi2f(g01)));
    unsigned a23 = pk2bf(gelu_tanh(lo2f(g23)), gelu_tanh(hi2f(g23)));
    uint2 o;
    o.x = pk2bf(lo2f(a01) * lo2f(u01), hi2f(a01) * hi2f(u01));
    o.y = pk2bf(lo2f(a23) * lo2f(u23), hi2f(a23) * hi2f(u23));
    return o;
  };
  if (wc == 0) {
#pragma unroll
    for (int i = 0; i < 4; ++i)
#pragma unroll
      for (int j = 0; j < 4; ++j)
        *reinterpret_cast<uint2*>(actl + (tl + j * 16) * 120 + i * 16 + fq * 4) = geglu4(acc[i][j], acc[i + 4][j]);
  } else {
#pragma unroll
    for (int i = 0; i < 3; ++i)
#pragma unroll
      for (int j = 0; j < 4; ++j)
        *reinterpret_cast<uint2*>(actl + (tl + j * 16) * 120 + 64 + i * 16 + fq * 4) = geglu4(acc[i][j], acc[i + 3][j]);
  }
  __syncthreads();
  STAMP(0, 3);
#pragma unroll
  for (int it = 0; it < 7; ++it) {
    int idx = it * 512 + tid;
    int row = idx / 14, c = idx - row * 14;
    bf16x8 v = *reinterpret_cast<const bf16x8*>(actl + row * 120 + c * 8);
    *reinterpret_cast<bf16x8*>(a2 + (size_t)(tm * 256 + row) * K2_ + tn * 112 + c * 8) = v;
  }
  STAMP(0, 4);
  if (ck < tm) {
    const unsigned long long* g = agg + ((size_t)(tn * 8 + ck) * 32 + ch) * 2;
    unsigned long long v0, v1;
    for (;;) {
      v0 = __hip_atomic_load(g, __ATOMIC_RELAXED, __HIP_MEMORY_SCOPE_AGENT);
      v1 = __hip_atomic_load(g + 1, __ATOMIC_RELAXED, __HIP_MEMORY_SCOPE_AGENT);
      if ((v0 >> 32) == 1ull && (v1 >> 32) == 1ull) break;
      __builtin_amdgcn_s_sleep(1);
    }
    float2 av = make_float2(__uint_as_float((unsigned)v0), __uint_as_float((unsigned)v1));
    for (int e = tm - 1 - ck; e > 0; --e) av = cmul(av, l256);
    term[ck * 32 + ch] = av;
  }
  __syncthreads();
  STAMP(0, 5);
  float2 bc = make_float2(0.f, 0.f);
  for (int j = 0; j < tm; ++j) { float2 tv = term[j * 32 + ch]; bc.x += tv.x; bc.y += tv.y; }
  float2 pw = make_float2(1.f, 0.f);
  for (int e = 0; e < ck; ++e) pw = cmul(pw, l16);
  float2 c0 = cag[ck * 32 + ch];
  float2 t3 = cmul(pw, bc);
  c0.x += t3.x; c0.y += t3.y;
  unsigned short* hl = reinterpret_cast<unsigned short*>(smem + E_BUL);
  {
    float2 pq = lam;
#pragma unroll
    for (int i = 0; i < 16; ++i) {
      float vr = hr[i] + pq.x * c0.x - pq.y * c0.y;
      float vi = hi[i] + pq.x * c0.y + pq.y * c0.x;
      hl[(ck * 16 + i) * 72 + ch] = f2bf(vr);
      hl[(ck * 16 + i) * 72 + 32 + ch] = f2bf(vi);
      pq = cmul(pq, lam);
    }
  }
  __syncthreads();
#pragma unroll
  for (int it = 0; it < 4; ++it) {
    int idx = it * 512 + tid;
    int row = idx >> 3, c = idx & 7;
    bf16x8 v = *reinterpret_cast<const bf16x8*>(hl + row * 72 + c * 8);
    *reinterpret_cast<bf16x8*>(a2 + (size_t)(tm * 256 + row) * K2_ + F_ + (c >> 2) * P_ + tn * 32 + (c & 3) * 8) = v;
  }
  STAMP(0, 6);
}

constexpr int G2_STAGE = 32768;
constexpr int G2_NSLOT = 4;
constexpr int G2_LDS = G2_NSLOT * G2_STAGE + 16;
__global__ __launch_bounds__(512, 2) void k_gemm2(const unsigned short* __restrict__ a2, const unsigned short* __restrict__ wdown,
                                                  const unsigned short* __restrict__ c2, const unsigned short* __restrict__ x,
                                                  const unsigned short* __restrict__ xn, const unsigned short* __restrict__ dskip,
                                                  float* __restrict__ out,
                                                  f32x4* __restrict__ slab, unsigned* __restrict__ cnt) {
  extern __shared__ __attribute__((aligned(16))) char smem[];
  int bid = blockIdx.x;
  int xcd = bid & 7, loc = bid >> 3;
  int tm = (xcd >> 1) * 4 + ((loc >> 3) & 3), tn = (xcd & 1) * 4 + ((loc >> 1) & 3), kz = loc & 1;
  int tile = tm * 8 + tn;
  STAMP(1, 0);
  int tid = threadIdx.x, wid = __builtin_amdgcn_readfirstlane(tid >> 6), lane = tid & 63;
  int kg = wid >> 2, wr = (wid >> 1) & 1, wc = wid & 1, fr = lane & 15, fq = lane >> 4;
  const unsigned short* ag = a2 + (size_t)tm * 128 * K2_ + kz * 2560;
  f32x4 acc[4][4], accf[4][4];
#pragma unroll
  for (int i = 0; i < 4; ++i)
#pragma unroll
    for (int j = 0; j < 4; ++j) { acc[i][j] = (f32x4){0, 0, 0, 0}; accf[i][j] = (f32x4){0, 0, 0, 0}; }
  int srow = lane >> 3, schunk = (lane & 7) ^ (srow & 7);
  int wlofs[2];
#pragma unroll
  for (int i = 0; i < 2; ++i) {
    int reg = wid * 2 + i, Q = reg >> 3, kb = reg & 7;
    int r = lane >> 3, c = (lane & 7) ^ (((r >> 1) & 1) << 1) ^ ((kb & 1) << 2);
    wlofs[i] = (kb * 8 + r) * D_ + tn * 128 + Q * 64 + c * 8;
  }
  auto stage = [&](int slot, int kt) {
    char* base = smem + slot * G2_STAGE;
    int kabs = kz * 2560 + kt * 64;
    const unsigned short* wb = (kabs < F_) ? wdown + (size_t)kabs * D_ : c2 + (size_t)(kabs - F_) * D_;
#pragma unroll
    for (int i = 0; i < 2; ++i) {
      int ins = wid * 2 + i;
      __builtin_amdgcn_global_load_lds((const unsigned*)(ag + (size_t)(ins * 8 + srow) * K2_ + kt * 64 + schunk * 8),
                                       LDS_PTR(base + ins * 1024), 16, 0, 0);
      __builtin_amdgcn_global_load_lds((const unsigned*)(wb + wlofs[i]), LDS_PTR(base + 16384 + ins * 1024), 16, 0, 0);
    }
  };
  const int ns = kz ? 48 : 40;
  const int nsf = kz ? 16 : 40;
  stage(0, 0); stage(1, 1); stage(2, 2); stage(3, 3);
  int rch = ((kg * 4 + fq) ^ (fr & 7)) * 16;
  const int xoff = (wr * 64 + fr) * 128 + rch;
  int wofs[4];
  {
    int kb = kg * 4 + fq, q = (lane >> 2) & 3, pp = lane & 3;
    int sw = ((q >> 1) << 1) ^ ((kb & 1) << 2);
#pragma unroll
    for (int i = 0; i < 4; ++i) wofs[i] = 16384 + (wc * 8 + kb) * 1024 + q * 128 + (((i * 2 + (pp >> 1)) ^ sw) * 16) + (pp & 1) * 8;
  }
  bf16x8 xa_[4], xb_[4], wa_[4], wb_[4];
  WAIT_VM(12);
  __builtin_amdgcn_s_barrier();
#pragma unroll
  for (int j = 0; j < 4; ++j) {
    xa_[j] = *reinterpret_cast<const bf16x8*>(smem + xoff + j * 2048);
    wa_[j] = tr_frag<512>(smem + wofs[j]);
  }
  auto body = [&](int s, bf16x8 (&xc)[4], bf16x8 (&wcur)[4], bf16x8 (&xnx)[4], bf16x8 (&wnx)[4]) {
    if (s < ns - 3) WAIT_VM(8);
    else if (s == ns - 3) WAIT_VM(4);
    else WAIT_VM(0);
    __builtin_amdgcn_s_barrier();
    if (s + 4 < ns) stage(s & 3, s + 4);
    const char* nb = smem + ((s + 1) & 3) * G2_STAGE;
    bool more = (s + 1 < ns);
#pragma unroll
    for (int i = 0; i < 4; ++i) {
#pragma unroll
      for (int j = 0; j < 4; ++j) acc[i][j] = __builtin_amdgcn_mfma_f32_16x16x32_bf16(wcur[i], xc[j], acc[i][j], 0, 0, 0);
      if (more) {
        wnx[i] = tr_frag<512>(nb + wofs[i]);
        xnx[i] = *reinterpret_cast<const bf16x8*>(nb + xoff + i * 2048);
      }
    }
#pragma unroll
    for (int i = 0; i < 4; ++i) {
      __builtin_amdgcn_sched_group_barrier(0x008, 4, 0);
      __builtin_amdgcn_sched_group_barrier(0x100, 3, 0);
    }
    if (s + 1 == nsf) {
#pragma unroll
      for (int i = 0; i < 4; ++i)
#pragma unroll
        for (int j = 0; j < 4; ++j) { accf[i][j] = acc[i][j]; acc[i][j] = (f32x4){0, 0, 0, 0}; }
    }
  };
  for (int s = 0; s < ns; s += 2) {
    body(s, xa_, wa_, xb_, wb_);
    body(s + 1, xb_, wb_, xa_, wa_);
  }
  __syncthreads();
  STAMP(1, 1);
  f32x4* ex = reinterpret_cast<f32x4*>(smem);
  int wq = wid & 3;
#pragma unroll
  for (int ii = 0; ii < 2; ++ii)
#pragma unroll
    for (int j = 0; j < 4; ++j) {
      f32x4 sf = kg ? accf[ii][j] : accf[2 + ii][j];
      f32x4 sy = kg ? acc[ii][j] : acc[2 + ii][j];
      ex[(((0 * 2 + kg) * 4 + wq) * 8 + ii * 4 + j) * 64 + lane] = sf;
      if (kz) ex[(((1 * 2 + kg) * 4 + wq) * 8 + ii * 4 + j) * 64 + lane] = sy;
    }
  unsigned* role_s = reinterpret_cast<unsigned*>(smem + G2_NSLOT * G2_STAGE);
  if (tid == 0) *role_s = __hip_atomic_fetch_add(&cnt[tile * 32], 1u, __ATOMIC_RELAXED, __HIP_MEMORY_SCOPE_AGENT);
  __syncthreads();
  f32x4 mf[2][4], my[2][4];
#pragma unroll
  for (int ii = 0; ii < 2; ++ii)
#pragma unroll
    for (int j = 0; j < 4; ++j) {
      f32x4 ownf = kg ? accf[2 + ii][j] : accf[ii][j];
      f32x4 owny = kg ? acc[2 + ii][j] : acc[ii][j];
      mf[ii][j] = ownf + ex[(((0 * 2 + (kg ^ 1)) * 4 + wq) * 8 + ii * 4 + j) * 64 + lane];
      my[ii][j] = owny;
      if (kz) my[ii][j] += ex[(((1 * 2 + (kg ^ 1)) * 4 + wq) * 8 + ii * 4 + j) * 64 + lane];
    }
  unsigned role = *role_s;
  STAMP(1, 2);
  f32x4* sl = slab + (size_t)tile * 8192;
  if (role == 0) {
    __amdgpu_buffer_rsrc_t rs = __builtin_amdgcn_make_buffer_rsrc((void*)sl, 0, 8192 * 16, 0x00020000);
#pragma unroll
    for (int ii = 0; ii < 2; ++ii)
#pragma unroll
      for (int j = 0; j < 4; ++j) {
        u32x4 v = __builtin_bit_cast(u32x4, mf[ii][j]);
        __builtin_amdgcn_raw_buffer_store_b128(v, rs, ((ii * 4 + j) * 512 + tid) * 16, 0, 16);
        if (kz) {
          u32x4 w = __builtin_bit_cast(u32x4, my[ii][j]);
          __builtin_amdgcn_raw_buffer_store_b128(w, rs, (4096 + (ii * 4 + j) * 512 + tid) * 16, 0, 16);
        }
      }
    asm volatile("s_waitcnt vmcnt(0)" ::: "memory");
    __syncthreads();
    if (tid == 0) __hip_atomic_store(&cnt[tile * 32 + 16], 1u, __ATOMIC_RELAXED, __HIP_MEMORY_SCOPE_AGENT);
    STAMP(1, 3);
    return;
  }
  u16x4 xv[2][4], xnv[2][4], dv[2];
#pragma unroll
  for (int ii = 0; ii < 2; ++ii) {
    int d = tn * 128 + wc * 64 + (kg * 2 + ii) * 16 + fq * 4;
    dv[ii] = *reinterpret_cast<const u16x4*>(dskip + d);
#pragma unroll
    for (int j = 0; j < 4; ++j) {
      int t = tm * 128 + wr * 64 + j * 16 + fr;
      xv[ii][j] = *reinterpret_cast<const u16x4*>(x + (size_t)t * D_ + d);
      xnv[ii][j] = *reinterpret_cast<const u16x4*>(xn + (size_t)t * D_ + d);
    }
  }
  if (tid == 0) {
    while (__hip_atomic_load(&cnt[tile * 32 + 16], __ATOMIC_RELAXED, __HIP_MEMORY_SCOPE_AGENT) == 0u) __builtin_amdgcn_s_sleep(1);
  }
  __syncthreads();
  __builtin_amdgcn_fence(__ATOMIC_ACQUIRE, "wavefront");
  STAMP(1, 3);
  {
    __amdgpu_buffer_rsrc_t rs = __builtin_amdgcn_make_buffer_rsrc((void*)sl, 0, 8192 * 16, 0x00020000);
#pragma unroll
    for (int ii = 0; ii < 2; ++ii)
#pragma unroll
      for (int j = 0; j < 4; ++j) {
        u32x4 v = __builtin_amdgcn_raw_buffer_load_b128(rs, ((ii * 4 + j) * 512 + tid) * 16, 0, 16);
        mf[ii][j] += __builtin_bit_cast(f32x4, v);
        if (!kz) {
          u32x4 w = __builtin_amdgcn_raw_buffer_load_b128(rs, (4096 + (ii * 4 + j) * 512 + tid) * 16, 0, 16);
          my[ii][j] = __builtin_bit_cast(f32x4, w);
        }
      }
  }
#pragma unroll
  for (int ii = 0; ii < 2; ++ii)
#pragma unroll
    for (int j = 0; j < 4; ++j) {
      int t = tm * 128 + wr * 64 + j * 16 + fr;
      int d = tn * 128 + wc * 64 + (kg * 2 + ii) * 16 + fq * 4;
      f32x4 o;
#pragma unroll
      for (int r = 0; r < 4; ++r) {
        float f = rbf(mf[ii][j][r]);
        float g = rbf(f * __builtin_amdgcn_rcpf(1.0f + __expf(-f)));
        float s = 2.0f * my[ii][j][r] + rbf(bf2f(xnv[ii][j][r]) * bf2f(dv[ii][r]));
        o[r] = bf2f(xv[ii][j][r]) + s * g;
      }
      *reinterpret_cast<f32x4*>(out + (size_t)t * D_ + d) = o;
    }
  STAMP(1, 4);
}

extern "C" void kernel_launch(void* const* d_in, const int* in_sizes, int n_in, void* d_out, int out_size, void* d_ws,
                              size_t ws_size, hipStream_t stream) {
  PrepArgs pa;
  pa.x = (const unsigned short*)d_in[0];
  pa.lre = (const unsigned short*)d_in[1];
  pa.lim = (const unsigned short*)d_in[2];
  pa.bre = (const unsigned short*)d_in[3];
  pa.bim = (const unsigned short*)d_in[4];
  pa.cre = (const unsigned short*)d_in[5];
  pa.cim = (const unsigned short*)d_in[6];
  pa.logdt = (const unsigned short*)d_in[7];
  const unsigned short* dskip = (const unsigned short*)d_in[8];
  const unsigned short* wgate = (const unsigned short*)d_in[10];
  const unsigned short* wup = (const unsigned short*)d_in[11];
  const unsigned short* wdown = (const unsigned short*)d_in[12];
  char* ws = (char*)d_ws;
  pa.ws = ws;
  unsigned short* xn = (unsigned short*)(ws + OFF_XN);
  unsigned short* a2 = (unsigned short*)(ws + OFF_A2);
  float* out = (float*)d_out;

  k_prep<<<PB_TOTAL, 256, 0, stream>>>(pa);
  (void)hipFuncSetAttribute((const void*)k_gemm1, hipFuncAttributeMaxDynamicSharedMemorySize, G1_LDS);
  k_gemm1<<<256, 512, G1_LDS, stream>>>(xn, wgate, wup, (const unsigned short*)(ws + OFF_BBT), a2, (const float2*)(ws + OFF_LAM),
                                        (unsigned*)(ws + OFF_TICK), (unsigned long long*)(ws + OFF_AGG));
  (void)hipFuncSetAttribute((const void*)k_gemm2, hipFuncAttributeMaxDynamicSharedMemorySize, G2_LDS);
  k_gemm2<<<256, 512, G2_LDS, stream>>>(a2, wdown, (const unsigned short*)(ws + OFF_C2), pa.x, xn, dskip, out,
                                        (f32x4*)(ws + OFF_SLAB), (unsigned*)(ws + OFF_CNT));
}
```

```cpp
#include <hip/hip_runtime.h>
#include <hip/hip_bf16.h>
#include <stdint.h>

typedef __attribute__((ext_vector_type(8))) short bf16x8;
typedef __attribute__((ext_vector_type(4))) float f32x4;
typedef __attribute__((ext_vector_type(4))) unsigned short u16x4;
typedef __attribute__((ext_vector_type(4))) unsigned u32x4;

constexpr int S_ = 2048;
constexpr int D_ = 1024;
constexpr int P_ = 1024;
constexpr int F_ = 3584;
constexpr int N1_ = 9216;
constexpr int K2_ = 5632;

constexpr size_t OFF_TICK = 0;
constexpr size_t OFF_AGG = 1024;
constexpr size_t OFF_CNT = OFF_AGG + 32 * 8 * 32 * 2 * 8;
constexpr size_t SYNC_BYTES = OFF_CNT + 128 * 32 * 4;
constexpr size_t OFF_LAM = SYNC_BYTES;
constexpr size_t OFF_XN = OFF_LAM + 8192;
constexpr size_t OFF_BBT = OFF_XN + (size_t)S_ * D_ * 2;
constexpr size_t OFF_C2 = OFF_BBT + (size_t)D_ * 2048 * 2;
constexpr size_t OFF_A2 = OFF_C2 + (size_t)2048 * D_ * 2;
constexpr size_t OFF_SLAB = OFF_A2 + (size_t)S_ * K2_ * 2;

__device__ __forceinline__ float bf2f(unsigned short v) { return __uint_as_float(((unsigned)v) << 16); }
__device__ __forceinline__ unsigned short f2bf(float f) {
  __bf16 b = (__bf16)f;
  return __builtin_bit_cast(unsigned short, b);
}
typedef __attribute__((ext_vector_type(2))) __bf16 bf16x2n;
typedef __attribute__((ext_vector_type(2))) float f32x2;
__device__ __forceinline__ unsigned pk2bf(float a, float b) {
  f32x2 v = {a, b};
  bf16x2n r = __builtin_convertvector(v, bf16x2n);
  return __builtin_bit_cast(unsigned, r);
}
__device__ __forceinline__ float lo2f(unsigned u) { return __uint_as_float(u << 16); }
__device__ __forceinline__ float hi2f(unsigned u) { return __uint_as_float(u & 0xFFFF0000u); }
__device__ __forceinline__ float rbf(float f) { return bf2f(f2bf(f)); }
__device__ __forceinline__ float2 cmul(float2 a, float2 b) { return make_float2(a.x * b.x - a.y * b.y, a.x * b.y + a.y * b.x); }

#define STAMP(k, i)
#define WAIT_VM(n) asm volatile("s_waitcnt vmcnt(" #n ")" ::: "memory")
#define LDS_PTR(p) ((__attribute__((address_space(3))) unsigned*)(p))

constexpr int PB_LN = 512, PB_BB = 256, PB_CP = 512;
constexpr int PB_TOTAL = PB_LN + PB_BB + PB_CP;

struct PrepArgs {
  const unsigned short *x, *lre, *lim, *bre, *bim, *cre, *cim, *logdt;
  char* ws;
};

__global__ __launch_bounds__(256) void k_prep(PrepArgs a) {
  __shared__ __attribute__((aligned(16))) unsigned short tile[2][64][72];
  __shared__ float2 coef[64];
  int b = blockIdx.x, tid = threadIdx.x, wave = tid >> 6, lane = tid & 63;
  if (b < PB_LN) {
    unsigned short* xn = (unsigned short*)(a.ws + OFF_XN);
    for (int i = b * 256 + tid; i < (int)(SYNC_BYTES / 16); i += PB_LN * 256)
      reinterpret_cast<u32x4*>(a.ws)[i] = (u32x4){0u, 0u, 0u, 0u};
    int row = b * 4 + wave;
    const bf16x8* src = reinterpret_cast<const bf16x8*>(a.x + (size_t)row * D_);
    bf16x8 v0 = src[lane], v1 = src[64 + lane];
    float f[16];
#pragma unroll
    for (int j = 0; j < 8; ++j) { f[j] = bf2f((unsigned short)v0[j]); f[8 + j] = bf2f((unsigned short)v1[j]); }
    float s = 0.f;
#pragma unroll
    for (int j = 0; j < 16; ++j) s += f[j];
#pragma unroll
    for (int o = 32; o >= 1; o >>= 1) s += __shfl_xor(s, o);
    float mean = s * (1.0f / D_);
    float q = 0.f;
#pragma unroll
    for (int j = 0; j < 16; ++j) { float d = f[j] - mean; q += d * d; }
#pragma unroll
    for (int o = 32; o >= 1; o >>= 1) q += __shfl_xor(q, o);
    float var = q * (1.0f / D_);
    float mb = rbf(mean), vb = rbf(var);
    float den = rbf(sqrtf(rbf(vb + 1e-5f)));
    bf16x8 o0, o1;
#pragma unroll
    for (int j = 0; j < 8; ++j) {
      o0[j] = (short)f2bf(rbf(f[j] - mb) / den);
      o1[j] = (short)f2bf(rbf(f[8 + j] - mb) / den);
    }
    bf16x8* dst = reinterpret_cast<bf16x8*>(xn + (size_t)row * D_);
    dst[lane] = o0; dst[64 + lane] = o1;
    return;
  }
  b -= PB_LN;
  if (b < PB_BB) {
    int tp = b >> 4, td = b & 15;
    int p0 = tp * 64, d0 = td * 64;
    if (tid < 64) {
      int p = p0 + tid;
      float lr = bf2f(a.lre[p]), li = bf2f(a.lim[p]);
      float dt = rbf(expf(bf2f(a.logdt[p])));
      float zr = dt * lr, zi = dt * li;
      float e = expf(zr);
      float br_ = e * cosf(zi), bi_ = e * sinf(zi);
      float nr = br_ - 1.0f, ni = bi_;
      float den = lr * lr + li * li;
      coef[tid] = make_float2((nr * lr + ni * li) / den, (ni * lr - nr * li) / den);
      if (td == 0) reinterpret_cast<float2*>(a.ws + OFF_LAM)[p] = make_float2(br_, bi_);
    }
#pragma unroll
    for (int i = 0; i < 2; ++i) {
      int idx = i * 256 + tid; int r = idx >> 3, ch = idx & 7;
      *reinterpret_cast<bf16x8*>(&tile[0][r][ch * 8]) = *reinterpret_cast<const bf16x8*>(a.bre + (size_t)(p0 + r) * D_ + d0 + ch * 8);
      *reinterpret_cast<bf16x8*>(&tile[1][r][ch * 8]) = *reinterpret_cast<const bf16x8*>(a.bim + (size_t)(p0 + r) * D_ + d0 + ch * 8);
    }
    __syncthreads();
    unsigned short* bbt = (unsigned short*)(a.ws + OFF_BBT);
#pragma unroll
    for (int i = 0; i < 2; ++i) {
      int idx = i * 256 + tid; int c = idx >> 3, rc = idx & 7;
      bf16x8 vr, vi;
#pragma unroll
      for (int j = 0; j < 8; ++j) {
        float x1 = bf2f(tile[0][rc * 8 + j][c]), x2 = bf2f(tile[1][rc * 8 + j][c]);
        float2 cf = coef[rc * 8 + j];
        vr[j] = (short)f2bf(cf.x * x1 - cf.y * x2);
        vi[j] = (short)f2bf(cf.x * x2 + cf.y * x1);
      }
      *reinterpret_cast<bf16x8*>(bbt + (size_t)(d0 + c) * 2048 + p0 + rc * 8) = vr;
      *reinterpret_cast<bf16x8*>(bbt + (size_t)(d0 + c) * 2048 + 1024 + p0 + rc * 8) = vi;
    }
    return;
  }
  b -= PB_BB;
  {
    int im = b >> 8; b &= 255;
    int td = b >> 4, tp = b & 15;
    int d0 = td * 64, p0 = tp * 64;
    const unsigned short* src = im ? a.cim : a.cre;
#pragma unroll
    for (int i = 0; i < 2; ++i) {
      int idx = i * 256 + tid; int r = idx >> 3, ch = idx & 7;
      *reinterpret_cast<bf16x8*>(&tile[0][r][ch * 8]) = *reinterpret_cast<const bf16x8*>(src + (size_t)(d0 + r) * P_ + p0 + ch * 8);
    }
    __syncthreads();
    unsigned short* c2 = (unsigned short*)(a.ws + OFF_C2);
    unsigned short sg = im ? 0x8000u : 0u;
#pragma unroll
    for (int i = 0; i < 2; ++i) {
      int idx = i * 256 + tid; int c = idx >> 3, rc = idx & 7;
      bf16x8 v;
#pragma unroll
      for (int j = 0; j < 8; ++j) v[j] = (short)(tile[0][rc * 8 + j][c] ^ sg);
      *reinterpret_cast<bf16x8*>(c2 + (size_t)(im * 1024 + p0 + c) * D_ + d0 + rc * 8) = v;
    }
  }
}

typedef __attribute__((ext_vector_type(4))) short s16x4;
template <int OFF2>
__device__ __forceinline__ bf16x8 tr_frag(unsigned lds_addr) {
  s16x4 lo, hi;
  asm volatile("ds_read_b64_tr_b16 %0, %2\n\tds_read_b64_tr_b16 %1, %2 offset:%c3"
               : "=&v"(lo), "=&v"(hi) : "v"(lds_addr), "i"(OFF2));
  return __builtin_shufflevector(lo, hi, 0, 1, 2, 3, 4, 5, 6, 7);
}
#define TR_WAIT() do { asm volatile("s_waitcnt lgkmcnt(0)" ::: "memory"); __builtin_amdgcn_sched_barrier(0); } while (0)
__device__ __forceinline__ unsigned lds_u32(const void* p) {
  return (unsigned)(uintptr_t)(__attribute__((address_space(3))) const char*)p;
}

__device__ __forceinline__ float gelu_tanh(float x) {
  float w = x * __builtin_fmaf(x * x, -0.10294323970650479f, -2.3022081985952363f);
  return x * __builtin_amdgcn_rcpf(1.0f + __builtin_amdgcn_exp2f(w));
}

constexpr int G1_AT = 256 * 64;
constexpr int G1_WT = 288 * 64;
constexpr int G1_STAGE = G1_AT + G1_WT;
constexpr int G1_NSLOT = 4;
constexpr int G1_LDS = G1_NSLOT * G1_STAGE;
constexpr int E_BUL = 0;
constexpr int E_ACT = 69632;
constexpr int E_CAG = 131072;
constexpr int E_TERM = 135168;
constexpr int E_MISC = 137216;

__global__ __launch_bounds__(512, 2) void k_gemm1(const unsigned short* __restrict__ xn, const unsigned short* __restrict__ wgate,
                                                  const unsigned short* __restrict__ wup, const unsigned short* __restrict__ bbt,
                                                  unsigned short* __restrict__ a2, const float2* __restrict__ lamv,
                                                  unsigned* __restrict__ tick, unsigned long long* __restrict__ agg) {
  extern __shared__ __attribute__((aligned(16))) char smem[];
  int tid = threadIdx.x, wid = __builtin_amdgcn_readfirstlane(tid >> 6), lane = tid & 63;
  int xl = blockIdx.x & 7;
  STAMP(0, 0);
  {
    unsigned* mw = reinterpret_cast<unsigned*>(smem + E_MISC);
    if (tid == 0) mw[0] = __hip_atomic_fetch_add(&tick[xl * 32], 1u, __ATOMIC_RELAXED, __HIP_MEMORY_SCOPE_AGENT);
    __syncthreads();
  }
  int loc = __builtin_amdgcn_readfirstlane(*reinterpret_cast<volatile unsigned*>(smem + E_MISC));
  __syncthreads();
  int tn = xl * 4 + (loc >> 3), tm = loc & 7;
  int wr = wid >> 1, wc = wid & 1;
  int fr = lane & 15, fq = lane >> 4;
  const unsigned short* xg = xn + (size_t)tm * 256 * D_;

  f32x4 acc[9][4];
#pragma unroll
  for (int i = 0; i < 9; ++i)
#pragma unroll
    for (int j = 0; j < 4; ++j) acc[i][j] = (f32x4){0.f, 0.f, 0.f, 0.f};

  int srow = lane >> 2;
  int schunk = (lane & 3) ^ (((srow >> 3) & 1) << 1);
  const char* wsrc[3]; int wstr[3];
#pragma unroll
  for (int i = 0; i < 3; ++i) {
    int reg = (i < 2) ? wid * 2 + i : 16 + (wid & 1);
    const unsigned short* mat; int col, k, ld;
    if (reg < 16) {
      int Q = reg >> 2, kb = reg & 3;
      int r = lane >> 3, c = (lane & 7) ^ (((r >> 1) & 1) << 1) ^ ((kb & 1) << 2);
      int Gl = c >> 1, h = c & 1;
      k = kb * 8 + r;
      int G = Q * 4 + Gl;
      if (G < 4) { mat = wgate; col = tn * 112 + G * 16; ld = F_; }
      else if (G < 8) { mat = wup; col = tn * 112 + (G - 4) * 16; ld = F_; }
      else if (G == 8) { mat = bbt; col = tn * 32; ld = 2048; }
      else if (G < 12) { mat = wgate; col = tn * 112 + 64 + (G - 9) * 16; ld = F_; }
      else if (G < 15) { mat = wup; col = tn * 112 + 64 + (G - 12) * 16; ld = F_; }
      else { mat = bbt; col = tn * 32 + 16; ld = 2048; }
      col += h * 8;
    } else {
      int r = lane >> 3, c = (lane & 7) ^ (((r >> 1) & 1) << 1);
      k = ((reg - 16) * 2 + (c >> 2)) * 8 + r;
      mat = bbt; col = 1024 + tn * 32 + (c & 3) * 8; ld = 2048;
    }
    wsrc[i] = (const char*)(mat + (size_t)k * ld + col);
    wstr[i] = ld * 64;
  }
  auto stage = [&](int slot, int ks) {
    char* base = smem + slot * G1_STAGE;
#pragma unroll
    for (int i = 0; i < 2; ++i) {
      int ins = wid * 2 + i;
      __builtin_amdgcn_global_load_lds((const unsigned*)(xg + (size_t)(ins * 16 + srow) * D_ + ks * 32 + schunk * 8),
                                       LDS_PTR(base + ins * 1024), 16, 0, 0);
    }
#pragma unroll
    for (int i = 0; i < 2; ++i)
      __builtin_amdgcn_global_load_lds((const unsigned*)(wsrc[i] + (size_t)ks * wstr[i]), LDS_PTR(base + G1_AT + (wid * 2 + i) * 1024), 16, 0, 0);
    if (wid < 2)
      __builtin_amdgcn_global_load_lds((const unsigned*)(wsrc[2] + (size_t)ks * wstr[2]), LDS_PTR(base + G1_AT + (16 + wid) * 1024), 16, 0, 0);
  };

  const int ns = D_ / 32;
  stage(0, 0); stage(1, 1); stage(2, 2); stage(3, 3);
  int rchunk = fq ^ (((fr >> 3) & 1) << 1);
  const int xoff = (wr * 64 + fr) * 64 + rchunk * 16;
  int wofs[9];
  {
    int kb = fq, q = (lane >> 2) & 3, pp = lane & 3;
    int sw = ((q >> 1) << 1) ^ ((kb & 1) << 2);
#pragma unroll
    for (int i = 0; i < 9; ++i) {
      int G = wc * 9 + i;
      if (G < 16) wofs[i] = G1_AT + ((G >> 2) * 4 + kb) * 1024 + q * 128 + ((((G & 3) * 2 + (pp >> 1)) ^ sw) * 16) + (pp & 1) * 8;
      else wofs[i] = G1_AT + (16 + (kb >> 1)) * 1024 + q * 128 + ((((kb & 1) * 4 + (G - 16) * 2 + (pp >> 1)) ^ ((q >> 1) << 1)) * 16) + (pp & 1) * 8;
    }
  }
  unsigned wpk[5];
#pragma unroll
  for (int i = 0; i < 4; ++i) wpk[i] = (unsigned)wofs[2 * i] | ((unsigned)wofs[2 * i + 1] << 16);
  wpk[4] = (unsigned)wofs[8];
#define WOFS(i) (((i) & 1) ? (wpk[(i) >> 1] >> 16) : (wpk[(i) >> 1] & 0xFFFFu))
  bf16x8 xa_[4], xb_[4], wf[9];
  const unsigned sbase = lds_u32(smem);
  if (wid < 2) WAIT_VM(15); else WAIT_VM(12);
  __builtin_amdgcn_s_barrier();
#pragma unroll
  for (int j = 0; j < 4; ++j) xa_[j] = *reinterpret_cast<const bf16x8*>(smem + xoff + j * 1024);
#pragma unroll
  for (int i = 0; i < 9; ++i) wf[i] = tr_frag<512>(sbase + WOFS(i));

  auto body = [&](int s, bf16x8 (&xc)[4], bf16x8 (&xnx)[4]) {
    if (s < ns - 3) { if (wid < 2) WAIT_VM(10); else WAIT_VM(8); }
    else if (s == ns - 3) { if (wid < 2) WAIT_VM(5); else WAIT_VM(4); }
    else WAIT_VM(0);
    __builtin_amdgcn_s_barrier();
    if (s + 4 < ns) stage(s & 3, s + 4);
    const char* nb = smem + ((s + 1) & 3) * G1_STAGE;
    unsigned nbu = sbase + ((s + 1) & 3) * G1_STAGE;
    TR_WAIT();
#pragma unroll
    for (int i = 0; i < 9; ++i) {
#pragma unroll
      for (int j = 0; j < 4; ++j) acc[i][j] = __builtin_amdgcn_mfma_f32_16x16x32_bf16(wf[i], xc[j], acc[i][j], 0, 0, 0);
      wf[i] = tr_frag<512>(nbu + WOFS(i));
      if (i < 4) xnx[i] = *reinterpret_cast<const bf16x8*>(nb + xoff + i * 1024);
    }
#pragma unroll
    for (int i = 0; i < 4; ++i) {
      __builtin_amdgcn_sched_group_barrier(0x008, 4, 0);
      __builtin_amdgcn_sched_group_barrier(0x100, 3, 0);
    }
#pragma unroll
    for (int i = 0; i < 5; ++i) {
      __builtin_amdgcn_sched_group_barrier(0x008, 4, 0);
      __builtin_amdgcn_sched_group_barrier(0x100, 2, 0);
    }
  };
  for (int s = 0; s < ns; s += 2) {
    body(s, xa_, xb_);
    body(s + 1, xb_, xa_);
  }

  __syncthreads();
  STAMP(0, 1);
  float* bul = reinterpret_cast<float*>(smem + E_BUL);
  unsigned short* actl = reinterpret_cast<unsigned short*>(smem + E_ACT);
  float2* cag = reinterpret_cast<float2*>(smem + E_CAG);
  float2* term = reinterpret_cast<float2*>(smem + E_TERM);
  int tl = wr * 64 + fr;
  if (wc == 0) {
#pragma unroll
    for (int j = 0; j < 4; ++j) *reinterpret_cast<f32x4*>(bul + (tl + j * 16) * 68 + fq * 4) = acc[8][j];
  } else {
#pragma unroll
    for (int j = 0; j < 4; ++j) {
      *reinterpret_cast<f32x4*>(bul + (tl + j * 16) * 68 + 16 + fq * 4) = acc[6][j];
      *reinterpret_cast<f32x4*>(bul + (tl + j * 16) * 68 + 32 + fq * 4) = acc[7][j];
      *reinterpret_cast<f32x4*>(bul + (tl + j * 16) * 68 + 48 + fq * 4) = acc[8][j];
    }
  }
  __syncthreads();
  int ch = tid & 31, ck = tid >> 5;
  int p = tn * 32 + ch;
  float2 lam = lamv[p];
  float hr[16], hi[16];
  {
    float sr = 0.f, si = 0.f;
#pragma unroll
    for (int i = 0; i < 16; ++i) {
      float br = bul[(ck * 16 + i) * 68 + ch], bi = bul[(ck * 16 + i) * 68 + 32 + ch];
      float nr = lam.x * sr - lam.y * si + br;
      float ni = lam.x * si + lam.y * sr + bi;
      sr = nr; si = ni; hr[i] = sr; hi[i] = si;
    }
    cag[ck * 32 + ch] = make_float2(sr, si);
  }
  float2 l16 = lam;
#pragma unroll
  for (int i = 0; i < 4; ++i) l16 = cmul(l16, l16);
  float2 l256 = l16;
#pragma unroll
  for (int i = 0; i < 4; ++i) l256 = cmul(l256, l256);
  __syncthreads();
  if (tid < 32) {
    float2 c = make_float2(0.f, 0.f);
#pragma unroll
    for (int k = 0; k < 16; ++k) {
      float2 aa = cag[k * 32 + ch];
      cag[k * 32 + ch] = c;
      float2 t2 = cmul(l16, c);
      c = make_float2(t2.x + aa.x, t2.y + aa.y);
    }
    if (tm < 7) {
      unsigned long long* g = agg + ((size_t)(tn * 8 + tm) * 32 + ch) * 2;
      __hip_atomic_store(g, (1ull << 32) | (unsigned long long)__float_as_uint(c.x), __ATOMIC_RELAXED, __HIP_MEMORY_SCOPE_AGENT);
      __hip_atomic_store(g + 1, (1ull << 32) | (unsigned long long)__float_as_uint(c.y), __ATOMIC_RELAXED, __HIP_MEMORY_SCOPE_AGENT);
    }
  }
  STAMP(0, 2);
  auto geglu4 = [&](const f32x4& gt, const f32x4& up) -> uint2 {
    unsigned g01 = pk2bf(gt[0], gt[1]), g23 = pk2bf(gt[2], gt[3]);
    unsigned u01 = pk2bf(up[0], up[1]), u23 = pk2bf(up[2], up[3]);
    unsigned a01 = pk2bf(gelu_tanh(lo2f(g01)), gelu_tanh(hi2f(g01)));
    unsigned a23 = pk2bf(gelu_tanh(lo2f(g23)), gelu_tanh(hi2f(g23)));
    uint2 o;
    o.x = pk2bf(lo2f(a01) * lo2f(u01), hi2f(a01) * hi2f(u01));
    o.y = pk2bf(lo2f(a23) * lo2f(u23), hi2f(a23) * hi2f(u23));
    return o;
  };
  if (wc == 0) {
#pragma unroll
    for (int i = 0; i < 4; ++i)
#pragma unroll
      for (int j = 0; j < 4; ++j)
        *reinterpret_cast<uint2*>(actl + (tl + j * 16) * 120 + i * 16 + fq * 4) = geglu4(acc[i][j], acc[i + 4][j]);
  } else {
#pragma unroll
    for (int i = 0; i < 3; ++i)
#pragma unroll
      for (int j = 0; j < 4; ++j)
        *reinterpret_cast<uint2*>(actl + (tl + j * 16) * 120 + 64 + i * 16 + fq * 4) = geglu4(acc[i][j], acc[i + 3][j]);
  }
  __syncthreads();
  STAMP(0, 3);
#pragma unroll
  for (int it = 0; it < 7; ++it) {
    int idx = it * 512 + tid;
    int row = idx / 14, c = idx - row * 14;
    bf16x8 v = *reinterpret_cast<const bf16x8*>(actl + row * 120 + c * 8);
    *reinterpret_cast<bf16x8*>(a2 + (size_t)(tm * 256 + row) * K2_ + tn * 112 + c * 8) = v;
  }
  STAMP(0, 4);
  if (ck < tm) {
    const unsigned long long* g = agg + ((size_t)(tn * 8 + ck) * 32 + ch) * 2;
    unsigned long long v0, v1;
    for (;;) {
      v0 = __hip_atomic_load(g, __ATOMIC_RELAXED, __HIP_MEMORY_SCOPE_AGENT);
      v1 = __hip_atomic_load(g + 1, __ATOMIC_RELAXED, __HIP_MEMORY_SCOPE_AGENT);
      if ((v0 >> 32) == 1ull && (v1 >> 32) == 1ull) break;
      __builtin_amdgcn_s_sleep(1);
    }
    float2 av = make_float2(__uint_as_float((unsigned)v0), __uint_as_float((unsigned)v1));
    for (int e = tm - 1 - ck; e > 0; --e) av = cmul(av, l256);
    term[ck * 32 + ch] = av;
  }
  __syncthreads();
  STAMP(0, 5);
  float2 bc = make_float2(0.f, 0.f);
  for (int j = 0; j < tm; ++j) { float2 tv = term[j * 32 + ch]; bc.x += tv.x; bc.y += tv.y; }
  float2 pw = make_float2(1.f, 0.f);
  for (int e = 0; e < ck; ++e) pw = cmul(pw, l16);
  float2 c0 = cag[ck * 32 + ch];
  float2 t3 = cmul(pw, bc);
  c0.x += t3.x; c0.y += t3.y;
  unsigned short* hl = reinterpret_cast<unsigned short*>(smem + E_BUL);
  {
    float2 pq = lam;
#pragma unroll
    for (int i = 0; i < 16; ++i) {
      float vr = hr[i] + pq.x * c0.x - pq.y * c0.y;
      float vi = hi[i] + pq.x * c0.y + pq.y * c0.x;
      hl[(ck * 16 + i) * 72 + ch] = f2bf(vr);
      hl[(ck * 16 + i) * 72 + 32 + ch] = f2bf(vi);
      pq = cmul(pq, lam);
    }
  }
  __syncthreads();
#pragma unroll
  for (int it = 0; it < 4; ++it) {
    int idx = it * 512 + tid;
    int row = idx >> 3, c = idx & 7;
    bf16x8 v = *reinterpret_cast<const bf16x8*>(hl + row * 72 + c * 8);
    *reinterpret_cast<bf16x8*>(a2 + (size_t)(tm * 256 + row) * K2_ + F_ + (c >> 2) * P_ + tn * 32 + (c & 3) * 8) = v;
  }
  STAMP(0, 6);
}

constexpr int G2_STAGE = 32768;
constexpr int G2_NSLOT = 4;
constexpr int G2_LDS = G2_NSLOT * G2_STAGE + 16;
__global__ __launch_bounds__(512, 2) void k_gemm2(const unsigned short* __restrict__ a2, const unsigned short* __restrict__ wdown,
                                                  const unsigned short* __restrict__ c2, const unsigned short* __restrict__ x,
                                                  const unsigned short* __restrict__ xn, const unsigned short* __restrict__ dskip,
                                                  float* __restrict__ out,
                                                  f32x4* __restrict__ slab, unsigned* __restrict__ cnt) {
  extern __shared__ __attribute__((aligned(16))) char smem[];
  int bid = blockIdx.x;
  int xcd = bid & 7, loc = bid >> 3;
  int tm = (xcd >> 1) * 4 + ((loc >> 3) & 3), tn = (xcd & 1) * 4 + ((loc >> 1) & 3), kz = loc & 1;
  int tile = tm * 8 + tn;
  STAMP(1, 0);
  int tid = threadIdx.x, wid = __builtin_amdgcn_readfirstlane(tid >> 6), lane = tid & 63;
  int kg = wid >> 2, wr = (wid >> 1) & 1, wc = wid & 1, fr = lane & 15, fq = lane >> 4;
  const unsigned short* ag = a2 + (size_t)tm * 128 * K2_ + kz * 2560;
  f32x4 acc[4][4], accf[4][4];
#pragma unroll
  for (int i = 0; i < 4; ++i)
#pragma unroll
    for (int j = 0; j < 4; ++j) { acc[i][j] = (f32x4){0, 0, 0, 0}; accf[i][j] = (f32x4){0, 0, 0, 0}; }
  int srow = lane >> 3, schunk = (lane & 7) ^ (srow & 7);
  int wlofs[2];
#pragma unroll
  for (int i = 0; i < 2; ++i) {
    int reg = wid * 2 + i, Q = reg >> 3, kb = reg & 7;
    int r = lane >> 3, c = (lane & 7) ^ (((r >> 1) & 1) << 1) ^ ((kb & 1) << 2);
    wlofs[i] = (kb * 8 + r) * D_ + tn * 128 + Q * 64 + c * 8;
  }
  auto stage = [&](int slot, int kt) {
    char* base = smem + slot * G2_STAGE;
    int kabs = kz * 2560 + kt * 64;
    const unsigned short* wb = (kabs < F_) ? wdown + (size_t)kabs * D_ : c2 + (size_t)(kabs - F_) * D_;
#pragma unroll
    for (int i = 0; i < 2; ++i) {
      int ins = wid * 2 + i;
      __builtin_amdgcn_global_load_lds((const unsigned*)(ag + (size_t)(ins * 8 + srow) * K2_ + kt * 64 + schunk * 8),
                                       LDS_PTR(base + ins * 1024), 16, 0, 0);
      __builtin_amdgcn_global_load_lds((const unsigned*)(wb + wlofs[i]), LDS_PTR(base + 16384 + ins * 1024), 16, 0, 0);
    }
  };
  const int ns = kz ? 48 : 40;
  const int nsf = kz ? 16 : 40;
  stage(0, 0); stage(1, 1); stage(2, 2); stage(3, 3);
  int rch = ((kg * 4 + fq) ^ (fr & 7)) * 16;
  const int xoff = (wr * 64 + fr) * 128 + rch;
  int wofs[4];
  {
    int kb = kg * 4 + fq, q = (lane >> 2) & 3, pp = lane & 3;
    int sw = ((q >> 1) << 1) ^ ((kb & 1) << 2);
#pragma unroll
    for (int i = 0; i < 4; ++i) wofs[i] = 16384 + (wc * 8 + kb) * 1024 + q * 128 + (((i * 2 + (pp >> 1)) ^ sw) * 16) + (pp & 1) * 8;
  }
  bf16x8 xa_[4], xb_[4], wa_[4], wb_[4];
  const unsigned sbase = lds_u32(smem);
  WAIT_VM(12);
  __builtin_amdgcn_s_barrier();
#pragma unroll
  for (int j = 0; j < 4; ++j) {
    xa_[j] = *reinterpret_cast<const bf16x8*>(smem + xoff + j * 2048);
    wa_[j] = tr_frag<512>(sbase + wofs[j]);
  }
  auto body = [&](int s, bf16x8 (&xc)[4], bf16x8 (&wcur)[4], bf16x8 (&xnx)[4], bf16x8 (&wnx)[4]) {
    if (s < ns - 3) WAIT_VM(8);
    else if (s == ns - 3) WAIT_VM(4);
    else WAIT_VM(0);
    __builtin_amdgcn_s_barrier();
    if (s + 4 < ns) stage(s & 3, s + 4);
    const char* nb = smem + ((s + 1) & 3) * G2_STAGE;
    unsigned nbu = sbase + ((s + 1) & 3) * G2_STAGE;
    TR_WAIT();
#pragma unroll
    for (int i = 0; i < 4; ++i) {
#pragma unroll
      for (int j = 0; j < 4; ++j) acc[i][j] = __builtin_amdgcn_mfma_f32_16x16x32_bf16(wcur[i], xc[j], acc[i][j], 0, 0, 0);
      wnx[i] = tr_frag<512>(nbu + wofs[i]);
      xnx[i] = *reinterpret_cast<const bf16x8*>(nb + xoff + i * 2048);
    }
#pragma unroll
    for (int i = 0; i < 4; ++i) {
      __builtin_amdgcn_sched_group_barrier(0x008, 4, 0);
      __builtin_amdgcn_sched_group_barrier(0x100, 3, 0);
    }
    if (s + 1 == nsf) {
#pragma unroll
      for (int i = 0; i < 4; ++i)
#pragma unroll
        for (int j = 0; j < 4; ++j) { accf[i][j] = acc[i][j]; acc[i][j] = (f32x4){0, 0, 0, 0}; }
    }
  };
  for (int s = 0; s < ns; s += 2) {
    body(s, xa_, wa_, xb_, wb_);
    body(s + 1, xb_, wb_, xa_, wa_);
  }
  __syncthreads();
  STAMP(1, 1);
  f32x4* ex = reinterpret_cast<f32x4*>(smem);
  int wq = wid & 3;
#pragma unroll
  for (int ii = 0; ii < 2; ++ii)
#pragma unroll
    for (int j = 0; j < 4; ++j) {
      f32x4 sf = kg ? accf[ii][j] : accf[2 + ii][j];
      f32x4 sy = kg ? acc[ii][j] : acc[2 + ii][j];
      ex[(((0 * 2 + kg) * 4 + wq) * 8 + ii * 4 + j) * 64 + lane] = sf;
      if (kz) ex[(((1 * 2 + kg) * 4 + wq) * 8 + ii * 4 + j) * 64 + lane] = sy;
    }
  unsigned* role_s = reinterpret_cast<unsigned*>(smem + G2_NSLOT * G2_STAGE);
  if (tid == 0) *role_s = __hip_atomic_fetch_add(&cnt[tile * 32], 1u, __ATOMIC_RELAXED, __HIP_MEMORY_SCOPE_AGENT);
  __syncthreads();
  f32x4 mf[2][4], my[2][4];
#pragma unroll
  for (int ii = 0; ii < 2; ++ii)
#pragma unroll
    for (int j = 0; j < 4; ++j) {
      f32x4 ownf = kg ? accf[2 + ii][j] : accf[ii][j];
      f32x4 owny = kg ? acc[2 + ii][j] : acc[ii][j];
      mf[ii][j] = ownf + ex[(((0 * 2 + (kg ^ 1)) * 4 + wq) * 8 + ii * 4 + j) * 64 + lane];
      my[ii][j] = owny;
      if (kz) my[ii][j] += ex[(((1 * 2 + (kg ^ 1)) * 4 + wq) * 8 + ii * 4 + j) * 64 + lane];
    }
  unsigned role = *role_s;
  STAMP(1, 2);
  f32x4* sl = slab + (size_t)tile * 8192;
  if (role == 0) {
    __amdgpu_buffer_rsrc_t rs = __builtin_amdgcn_make_buffer_rsrc((void*)sl, 0, 8192 * 16, 0x00020000);
#pragma unroll
    for (int ii = 0; ii < 2; ++ii)
#pragma unroll
      for (int j = 0; j < 4; ++j) {
        u32x4 v = __builtin_bit_cast(u32x4, mf[ii][j]);
        __builtin_amdgcn_raw_buffer_store_b128(v, rs, ((ii * 4 + j) * 512 + tid) * 16, 0, 16);
        if (kz) {
          u32x4 w = __builtin_bit_cast(u32x4, my[ii][j]);
          __builtin_amdgcn_raw_buffer_store_b128(w, rs, (4096 + (ii * 4 + j) * 512 + tid) * 16, 0, 16);
        }
      }
    asm volatile("s_waitcnt vmcnt(0)" ::: "memory");
    __syncthreads();
    if (tid == 0) __hip_atomic_store(&cnt[tile * 32 + 16], 1u, __ATOMIC_RELAXED, __HIP_MEMORY_SCOPE_AGENT);
    STAMP(1, 3);
    return;
  }
  u16x4 xv[2][4], xnv[2][4], dv[2];
#pragma unroll
  for (int ii = 0; ii < 2; ++ii) {
    int d = tn * 128 + wc * 64 + (kg * 2 + ii) * 16 + fq * 4;
    dv[ii] = *reinterpret_cast<const u16x4*>(dskip + d);
#pragma unroll
    for (int j = 0; j < 4; ++j) {
      int t = tm * 128 + wr * 64 + j * 16 + fr;
      xv[ii][j] = *reinterpret_cast<const u16x4*>(x + (size_t)t * D_ + d);
      xnv[ii][j] = *reinterpret_cast<const u16x4*>(xn + (size_t)t * D_ + d);
    }
  }
  if (tid == 0) {
    while (__hip_atomic_load(&cnt[tile * 32 + 16], __ATOMIC_RELAXED, __HIP_MEMORY_SCOPE_AGENT) == 0u) __builtin_amdgcn_s_sleep(1);
  }
  __syncthreads();
  __builtin_amdgcn_fence(__ATOMIC_ACQUIRE, "wavefront");
  STAMP(1, 3);
  {
    __amdgpu_buffer_rsrc_t rs = __builtin_amdgcn_make_buffer_rsrc((void*)sl, 0, 8192 * 16, 0x00020000);
#pragma unroll
    for (int ii = 0; ii < 2; ++ii)
#pragma unroll
      for (int j = 0; j < 4; ++j) {
        u32x4 v = __builtin_amdgcn_raw_buffer_load_b128(rs, ((ii * 4 + j) * 512 + tid) * 16, 0, 16);
        mf[ii][j] += __builtin_bit_cast(f32x4, v);
        if (!kz) {
          u32x4 w = __builtin_amdgcn_raw_buffer_load_b128(rs, (4096 + (ii * 4 + j) * 512 + tid) * 16, 0, 16);
          my[ii][j] = __builtin_bit_cast(f32x4, w);
        }
      }
  }
#pragma unroll
  for (int ii = 0; ii < 2; ++ii)
#pragma unroll
    for (int j = 0; j < 4; ++j) {
      int t = tm * 128 + wr * 64 + j * 16 + fr;
      int d = tn * 128 + wc * 64 + (kg * 2 + ii) * 16 + fq * 4;
      f32x4 o;
#pragma unroll
      for (int r = 0; r < 4; ++r) {
        float f = rbf(mf[ii][j][r]);
        float g = rbf(f * __builtin_amdgcn_rcpf(1.0f + __expf(-f)));
        float s = 2.0f * my[ii][j][r] + rbf(bf2f(xnv[ii][j][r]) * bf2f(dv[ii][r]));
        o[r] = bf2f(xv[ii][j][r]) + s * g;
      }
      *reinterpret_cast<f32x4*>(out + (size_t)t * D_ + d) = o;
    }
  STAMP(1, 4);
}

extern "C" void kernel_launch(void* const* d_in, const int* in_sizes, int n_in, void* d_out, int out_size, void* d_ws,
                              size_t ws_size, hipStream_t stream) {
  PrepArgs pa;
  pa.x = (const unsigned short*)d_in[0];
  pa.lre = (const unsigned short*)d_in[1];
  pa.lim = (const unsigned short*)d_in[2];
  pa.bre = (const unsigned short*)d_in[3];
  pa.bim = (const unsigned short*)d_in[4];
  pa.cre = (const unsigned short*)d_in[5];
  pa.cim = (const unsigned short*)d_in[6];
  pa.logdt = (const unsigned short*)d_in[7];
  const unsigned short* dskip = (const unsigned short*)d_in[8];
  const unsigned short* wgate = (const unsigned short*)d_in[10];
  const unsigned short* wup = (const unsigned short*)d_in[11];
  const unsigned short* wdown = (const unsigned short*)d_in[12];
  char* ws = (char*)d_ws;
  pa.ws = ws;
  unsigned short* xn = (unsigned short*)(ws + OFF_XN);
  unsigned short* a2 = (unsigned short*)(ws + OFF_A2);
  float* out = (float*)d_out;

  k_prep<<<PB_TOTAL, 256, 0, stream>>>(pa);
  (void)hipFuncSetAttribute((const void*)k_gemm1, hipFuncAttributeMaxDynamicSharedMemorySize, G1_LDS);
  k_gemm1<<<256, 512, G1_LDS, stream>>>(xn, wgate, wup, (const unsigned short*)(ws + OFF_BBT), a2, (const float2*)(ws + OFF_LAM),
                                        (unsigned*)(ws + OFF_TICK), (unsigned long long*)(ws + OFF_AGG));
  (void)hipFuncSetAttribute((const void*)k_gemm2, hipFuncAttributeMaxDynamicSharedMemorySize, G2_LDS);
  k_gemm2<<<256, 512, G2_LDS, stream>>>(a2, wdown, (const unsigned short*)(ws + OFF_C2), pa.x, xn, dskip, out,
                                        (f32x4*)(ws + OFF_SLAB), (unsigned*)(ws + OFF_CNT));
}
```

```cpp
#include <hip/hip_runtime.h>
#include <hip/hip_bf16.h>
#include <stdint.h>

typedef __attribute__((ext_vector_type(8))) short bf16x8;
typedef __attribute__((ext_vector_type(4))) float f32x4;
typedef __attribute__((ext_vector_type(4))) unsigned short u16x4;
typedef __attribute__((ext_vector_type(4))) unsigned u32x4;

constexpr int S_ = 2048;
constexpr int D_ = 1024;
constexpr int P_ = 1024;
constexpr int F_ = 3584;
constexpr int N1_ = 9216;
constexpr int K2_ = 5632;

constexpr size_t OFF_TICK = 0;
constexpr size_t OFF_AGG = 1024;
constexpr size_t OFF_CNT = OFF_AGG + 32 * 8 * 32 * 2 * 8;
constexpr size_t SYNC_BYTES = OFF_CNT + 128 * 32 * 4;
constexpr size_t OFF_LAM = SYNC_BYTES;
constexpr size_t OFF_XN = OFF_LAM + 8192;
constexpr size_t OFF_BBT = OFF_XN + (size_t)S_ * D_ * 2;
constexpr size_t OFF_C2 = OFF_BBT + (size_t)D_ * 2048 * 2;
constexpr size_t OFF_A2 = OFF_C2 + (size_t)2048 * D_ * 2;
constexpr size_t OFF_SLAB = OFF_A2 + (size_t)S_ * K2_ * 2;

__device__ __forceinline__ float bf2f(unsigned short v) { return __uint_as_float(((unsigned)v) << 16); }
__device__ __forceinline__ unsigned short f2bf(float f) {
  __bf16 b = (__bf16)f;
  return __builtin_bit_cast(unsigned short, b);
}
typedef __attribute__((ext_vector_type(2))) __bf16 bf16x2n;
typedef __attribute__((ext_vector_type(2))) float f32x2;
__device__ __forceinline__ unsigned pk2bf(float a, float b) {
  f32x2 v = {a, b};
  bf16x2n r = __builtin_convertvector(v, bf16x2n);
  return __builtin_bit_cast(unsigned, r);
}
__device__ __forceinline__ float lo2f(unsigned u) { return __uint_as_float(u << 16); }
__device__ __forceinline__ float hi2f(unsigned u) { return __uint_as_float(u & 0xFFFF0000u); }
__device__ __forceinline__ float rbf(float f) { return bf2f(f2bf(f)); }
__device__ __forceinline__ float2 cmul(float2 a, float2 b) { return make_float2(a.x * b.x - a.y * b.y, a.x * b.y + a.y * b.x); }

#define STAMP(k, i)
#define CSTAMP(k, i)
#define WAIT_VM(n) asm volatile("s_waitcnt vmcnt(" #n ")" ::: "memory")
#define LDS_PTR(p) ((__attribute__((address_space(3))) unsigned*)(p))

constexpr int PB_LN = 512, PB_BB = 256, PB_CP = 512;
constexpr int PB_TOTAL = PB_LN + PB_BB + PB_CP;

struct PrepArgs {
  const unsigned short *x, *lre, *lim, *bre, *bim, *cre, *cim, *logdt;
  char* ws;
};

__global__ __launch_bounds__(256) void k_prep(PrepArgs a) {
  __shared__ __attribute__((aligned(16))) unsigned short tile[2][64][72];
  __shared__ float2 coef[64];
  int b = blockIdx.x, tid = threadIdx.x, wave = tid >> 6, lane = tid & 63;
  if (b < PB_LN) {
    unsigned short* xn = (unsigned short*)(a.ws + OFF_XN);
    for (int i = b * 256 + tid; i < (int)(SYNC_BYTES / 16); i += PB_LN * 256)
      reinterpret_cast<u32x4*>(a.ws)[i] = (u32x4){0u, 0u, 0u, 0u};
    int row = b * 4 + wave;
    const bf16x8* src = reinterpret_cast<const bf16x8*>(a.x + (size_t)row * D_);
    bf16x8 v0 = src[lane], v1 = src[64 + lane];
    float f[16];
#pragma unroll
    for (int j = 0; j < 8; ++j) { f[j] = bf2f((unsigned short)v0[j]); f[8 + j] = bf2f((unsigned short)v1[j]); }
    float s = 0.f;
#pragma unroll
    for (int j = 0; j < 16; ++j) s += f[j];
#pragma unroll
    for (int o = 32; o >= 1; o >>= 1) s += __shfl_xor(s, o);
    float mean = s * (1.0f / D_);
    float q = 0.f;
#pragma unroll
    for (int j = 0; j < 16; ++j) { float d = f[j] - mean; q += d * d; }
#pragma unroll
    for (int o = 32; o >= 1; o >>= 1) q += __shfl_xor(q, o);
    float var = q * (1.0f / D_);
    float mb = rbf(mean), vb = rbf(var);
    float den = rbf(sqrtf(rbf(vb + 1e-5f)));
    bf16x8 o0, o1;
#pragma unroll
    for (int j = 0; j < 8; ++j) {
      o0[j] = (short)f2bf(rbf(f[j] - mb) / den);
      o1[j] = (short)f2bf(rbf(f[8 + j] - mb) / den);
    }
    bf16x8* dst = reinterpret_cast<bf16x8*>(xn + (size_t)row * D_);
    dst[lane] = o0; dst[64 + lane] = o1;
    return;
  }
  b -= PB_LN;
  if (b < PB_BB) {
    int tp = b >> 4, td = b & 15;
    int p0 = tp * 64, d0 = td * 64;
    if (tid < 64) {
      int p = p0 + tid;
      float lr = bf2f(a.lre[p]), li = bf2f(a.lim[p]);
      float dt = rbf(expf(bf2f(a.logdt[p])));
      float zr = dt * lr, zi = dt * li;
      float e = expf(zr);
      float br_ = e * cosf(zi), bi_ = e * sinf(zi);
      float nr = br_ - 1.0f, ni = bi_;
      float den = lr * lr + li * li;
      coef[tid] = make_float2((nr * lr + ni * li) / den, (ni * lr - nr * li) / den);
      if (td == 0) reinterpret_cast<float2*>(a.ws + OFF_LAM)[p] = make_float2(br_, bi_);
    }
#pragma unroll
    for (int i = 0; i < 2; ++i) {
      int idx = i * 256 + tid; int r = idx >> 3, ch = idx & 7;
      *reinterpret_cast<bf16x8*>(&tile[0][r][ch * 8]) = *reinterpret_cast<const bf16x8*>(a.bre + (size_t)(p0 + r) * D_ + d0 + ch * 8);
      *reinterpret_cast<bf16x8*>(&tile[1][r][ch * 8]) = *reinterpret_cast<const bf16x8*>(a.bim + (size_t)(p0 + r) * D_ + d0 + ch * 8);
    }
    __syncthreads();
    unsigned short* bbt = (unsigned short*)(a.ws + OFF_BBT);
#pragma unroll
    for (int i = 0; i < 2; ++i) {
      int idx = i * 256 + tid; int c = idx >> 3, rc = idx & 7;
      bf16x8 vr, vi;
#pragma unroll
      for (int j = 0; j < 8; ++j) {
        float x1 = bf2f(tile[0][rc * 8 + j][c]), x2 = bf2f(tile[1][rc * 8 + j][c]);
        float2 cf = coef[rc * 8 + j];
        vr[j] = (short)f2bf(cf.x * x1 - cf.y * x2);
        vi[j] = (short)f2bf(cf.x * x2 + cf.y * x1);
      }
      *reinterpret_cast<bf16x8*>(bbt + (size_t)(d0 + c) * 2048 + p0 + rc * 8) = vr;
      *reinterpret_cast<bf16x8*>(bbt + (size_t)(d0 + c) * 2048 + 1024 + p0 + rc * 8) = vi;
    }
    return;
  }
  b -= PB_BB;
  {
    int im = b >> 8; b &= 255;
    int td = b >> 4, tp = b & 15;
    int d0 = td * 64, p0 = tp * 64;
    const unsigned short* src = im ? a.cim : a.cre;
#pragma unroll
    for (int i = 0; i < 2; ++i) {
      int idx = i * 256 + tid; int r = idx >> 3, ch = idx & 7;
      *reinterpret_cast<bf16x8*>(&tile[0][r][ch * 8]) = *reinterpret_cast<const bf16x8*>(src + (size_t)(d0 + r) * P_ + p0 + ch * 8);
    }
    __syncthreads();
    unsigned short* c2 = (unsigned short*)(a.ws + OFF_C2);
    unsigned short sg = im ? 0x8000u : 0u;
#pragma unroll
    for (int i = 0; i < 2; ++i) {
      int idx = i * 256 + tid; int c = idx >> 3, rc = idx & 7;
      bf16x8 v;
#pragma unroll
      for (int j = 0; j < 8; ++j) v[j] = (short)(tile[0][rc * 8 + j][c] ^ sg);
      *reinterpret_cast<bf16x8*>(c2 + (size_t)(im * 1024 + p0 + c) * D_ + d0 + rc * 8) = v;
    }
  }
}

typedef __attribute__((ext_vector_type(4))) short s16x4;
template <int OFF2>
__device__ __forceinline__ bf16x8 tr_frag(unsigned lds_addr) {
  s16x4 lo, hi;
  asm volatile("ds_read_b64_tr_b16 %0, %2\n\tds_read_b64_tr_b16 %1, %2 offset:%c3"
               : "=&v"(lo), "=&v"(hi) : "v"(lds_addr), "i"(OFF2));
  return __builtin_shufflevector(lo, hi, 0, 1, 2, 3, 4, 5, 6, 7);
}
#define TR_WAIT() do { asm volatile("s_waitcnt lgkmcnt(0)" ::: "memory"); __builtin_amdgcn_sched_barrier(0); } while (0)
__device__ __forceinline__ unsigned lds_u32(const void* p) {
  return (unsigned)(uintptr_t)(__attribute__((address_space(3))) const char*)p;
}

__device__ __forceinline__ float gelu_tanh(float x) {
  float w = x * __builtin_fmaf(x * x, -0.10294323970650479f, -2.3022081985952363f);
  return x * __builtin_amdgcn_rcpf(1.0f + __builtin_amdgcn_exp2f(w));
}

constexpr int G1_AT = 256 * 64;
constexpr int G1_WT = 288 * 64;
constexpr int G1_STAGE = G1_AT + G1_WT;
constexpr int G1_NSLOT = 4;
constexpr int G1_LDS = G1_NSLOT * G1_STAGE;
constexpr int E_BUL = 0;
constexpr int E_ACT = 69632;
constexpr int E_CAG = 131072;
constexpr int E_TERM = 135168;
constexpr int E_MISC = 137216;

__global__ __launch_bounds__(512, 2) void k_gemm1(const unsigned short* __restrict__ xn, const unsigned short* __restrict__ wgate,
                                                  const unsigned short* __restrict__ wup, const unsigned short* __restrict__ bbt,
                                                  unsigned short* __restrict__ a2, const float2* __restrict__ lamv,
                                                  unsigned* __restrict__ tick, unsigned long long* __restrict__ agg) {
  extern __shared__ __attribute__((aligned(16))) char smem[];
  int tid = threadIdx.x, wid = __builtin_amdgcn_readfirstlane(tid >> 6), lane = tid & 63;
  int xl = blockIdx.x & 7;
  STAMP(0, 0);
  {
    unsigned* mw = reinterpret_cast<unsigned*>(smem + E_MISC);
    if (tid == 0) mw[0] = __hip_atomic_fetch_add(&tick[xl * 32], 1u, __ATOMIC_RELAXED, __HIP_MEMORY_SCOPE_AGENT);
    __syncthreads();
  }
  int loc = __builtin_amdgcn_readfirstlane(*reinterpret_cast<volatile unsigned*>(smem + E_MISC));
  __syncthreads();
  int tn = xl * 4 + (loc >> 3), tm = loc & 7;
  int wr = wid >> 1, wc = wid & 1;
  int fr = lane & 15, fq = lane >> 4;
  const unsigned short* xg = xn + (size_t)tm * 256 * D_;

  f32x4 acc[9][4];
#pragma unroll
  for (int i = 0; i < 9; ++i)
#pragma unroll
    for (int j = 0; j < 4; ++j) acc[i][j] = (f32x4){0.f, 0.f, 0.f, 0.f};

  int srow = lane >> 2;
  int schunk = (lane & 3) ^ (((srow >> 3) & 1) << 1);
  const char* wsrc[3]; int wstr[3];
#pragma unroll
  for (int i = 0; i < 3; ++i) {
    int reg = (i < 2) ? wid * 2 + i : 16 + (wid & 1);
    const unsigned short* mat; int col, k, ld;
    if (reg < 16) {
      int Q = reg >> 2, kb = reg & 3;
      int r = lane >> 3, c = (lane & 7) ^ (((r >> 1) & 1) << 1) ^ ((kb & 1) << 2);
      int Gl = c >> 1, h = c & 1;
      k = kb * 8 + r;
      int G = Q * 4 + Gl;
      if (G < 4) { mat = wgate; col = tn * 112 + G * 16; ld = F_; }
      else if (G < 8) { mat = wup; col = tn * 112 + (G - 4) * 16; ld = F_; }
      else if (G == 8) { mat = bbt; col = tn * 32; ld = 2048; }
      else if (G < 12) { mat = wgate; col = tn * 112 + 64 + (G - 9) * 16; ld = F_; }
      else if (G < 15) { mat = wup; col = tn * 112 + 64 + (G - 12) * 16; ld = F_; }
      else { mat = bbt; col = tn * 32 + 16; ld = 2048; }
      col += h * 8;
    } else {
      int r = lane >> 3, c = (lane & 7) ^ (((r >> 1) & 1) << 1);
      k = ((reg - 16) * 2 + (c >> 2)) * 8 + r;
      mat = bbt; col = 1024 + tn * 32 + (c & 3) * 8; ld = 2048;
    }
    wsrc[i] = (const char*)(mat + (size_t)k * ld + col);
    wstr[i] = ld * 64;
  }
  auto stage = [&](int slot, int ks) {
    char* base = smem + slot * G1_STAGE;
#pragma unroll
    for (int i = 0; i < 2; ++i) {
      int ins = wid * 2 + i;
      __builtin_amdgcn_global_load_lds((const unsigned*)(xg + (size_t)(ins * 16 + srow) * D_ + ks * 32 + schunk * 8),
                                       LDS_PTR(base + ins * 1024), 16, 0, 0);
    }
#pragma unroll
    for (int i = 0; i < 2; ++i)
      __builtin_amdgcn_global_load_lds((const unsigned*)(wsrc[i] + (size_t)ks * wstr[i]), LDS_PTR(base + G1_AT + (wid * 2 + i) * 1024), 16, 0, 0);
    if (wid < 2)
      __builtin_amdgcn_global_load_lds((const unsigned*)(wsrc[2] + (size_t)ks * wstr[2]), LDS_PTR(base + G1_AT + (16 + wid) * 1024), 16, 0, 0);
  };

  const int ns = D_ / 32;
  stage(0, 0); stage(1, 1); stage(2, 2); stage(3, 3);
  int rchunk = fq ^ (((fr >> 3) & 1) << 1);
  const int xoff = (wr * 64 + fr) * 64 + rchunk * 16;
  int wofs[9];
  {
    int kb = fq, q = (lane >> 2) & 3, pp = lane & 3;
    int sw = ((q >> 1) << 1) ^ ((kb & 1) << 2);
#pragma unroll
    for (int i = 0; i < 9; ++i) {
      int G = wc * 9 + i;
      if (G < 16) wofs[i] = G1_AT + ((G >> 2) * 4 + kb) * 1024 + q * 128 + ((((G & 3) * 2 + (pp >> 1)) ^ sw) * 16) + (pp & 1) * 8;
      else wofs[i] = G1_AT + (16 + (kb >> 1)) * 1024 + q * 128 + ((((kb & 1) * 4 + (G - 16) * 2 + (pp >> 1)) ^ ((q >> 1) << 1)) * 16) + (pp & 1) * 8;
    }
  }
  unsigned wpk[5];
#pragma unroll
  for (int i = 0; i < 4; ++i) wpk[i] = (unsigned)wofs[2 * i] | ((unsigned)wofs[2 * i + 1] << 16);
  wpk[4] = (unsigned)wofs[8];
#define WOFS(i) (((i) & 1) ? (wpk[(i) >> 1] >> 16) : (wpk[(i) >> 1] & 0xFFFFu))
  bf16x8 xa_[4], xb_[4], wf[9];
  const unsigned sbase = lds_u32(smem);
  if (wid < 2) WAIT_VM(15); else WAIT_VM(12);
  __builtin_amdgcn_s_barrier();
#pragma unroll
  for (int j = 0; j < 4; ++j) xa_[j] = *reinterpret_cast<const bf16x8*>(smem + xoff + j * 1024);
#pragma unroll
  for (int i = 0; i < 9; ++i) wf[i] = tr_frag<512>(sbase + WOFS(i));

  auto body = [&](int s, bf16x8 (&xc)[4], bf16x8 (&xnx)[4]) {
    if (s < ns - 3) { if (wid < 2) WAIT_VM(10); else WAIT_VM(8); }
    else if (s == ns - 3) { if (wid < 2) WAIT_VM(5); else WAIT_VM(4); }
    else WAIT_VM(0);
    __builtin_amdgcn_s_barrier();
    if (s + 4 < ns) stage(s & 3, s + 4);
    const char* nb = smem + ((s + 1) & 3) * G1_STAGE;
    unsigned nbu = sbase + ((s + 1) & 3) * G1_STAGE;
    TR_WAIT();
#pragma unroll
    for (int i = 0; i < 9; ++i) {
#pragma unroll
      for (int j = 0; j < 4; ++j) acc[i][j] = __builtin_amdgcn_mfma_f32_16x16x32_bf16(wf[i], xc[j], acc[i][j], 0, 0, 0);
      wf[i] = tr_frag<512>(nbu + WOFS(i));
      if (i < 4) xnx[i] = *reinterpret_cast<const bf16x8*>(nb + xoff + i * 1024);
    }
#pragma unroll
    for (int i = 0; i < 4; ++i) {
      __builtin_amdgcn_sched_group_barrier(0x008, 4, 0);
      __builtin_amdgcn_sched_group_barrier(0x100, 3, 0);
    }
#pragma unroll
    for (int i = 0; i < 5; ++i) {
      __builtin_amdgcn_sched_group_barrier(0x008, 4, 0);
      __builtin_amdgcn_sched_group_barrier(0x100, 2, 0);
    }
  };
  STAMP(0, 10); CSTAMP(0, 11);
  for (int s = 0; s < ns; s += 2) {
    body(s, xa_, xb_);
    body(s + 1, xb_, xa_);
  }
  STAMP(0, 12); CSTAMP(0, 13);

  __syncthreads();
  STAMP(0, 1);
  float* bul = reinterpret_cast<float*>(smem + E_BUL);
  unsigned short* actl = reinterpret_cast<unsigned short*>(smem + E_ACT);
  float2* cag = reinterpret_cast<float2*>(smem + E_CAG);
  float2* term = reinterpret_cast<float2*>(smem + E_TERM);
  int tl = wr * 64 + fr;
  if (wc == 0) {
#pragma unroll
    for (int j = 0; j < 4; ++j) *reinterpret_cast<f32x4*>(bul + (tl + j * 16) * 68 + fq * 4) = acc[8][j];
  } else {
#pragma unroll
    for (int j = 0; j < 4; ++j) {
      *reinterpret_cast<f32x4*>(bul + (tl + j * 16) * 68 + 16 + fq * 4) = acc[6][j];
      *reinterpret_cast<f32x4*>(bul + (tl + j * 16) * 68 + 32 + fq * 4) = acc[7][j];
      *reinterpret_cast<f32x4*>(bul + (tl + j * 16) * 68 + 48 + fq * 4) = acc[8][j];
    }
  }
  __syncthreads();
  int ch = tid & 31, ck = tid >> 5;
  int p = tn * 32 + ch;
  float2 lam = lamv[p];
  float hr[16], hi[16];
  {
    float sr = 0.f, si = 0.f;
#pragma unroll
    for (int i = 0; i < 16; ++i) {
      float br = bul[(ck * 16 + i) * 68 + ch], bi = bul[(ck * 16 + i) * 68 + 32 + ch];
      float nr = lam.x * sr - lam.y * si + br;
      float ni = lam.x * si + lam.y * sr + bi;
      sr = nr; si = ni; hr[i] = sr; hi[i] = si;
    }
    cag[ck * 32 + ch] = make_float2(sr, si);
  }
  float2 l16 = lam;
#pragma unroll
  for (int i = 0; i < 4; ++i) l16 = cmul(l16, l16);
  float2 l256 = l16;
#pragma unroll
  for (int i = 0; i < 4; ++i) l256 = cmul(l256, l256);
  __syncthreads();
  if (tid < 32) {
    float2 c = make_float2(0.f, 0.f);
#pragma unroll
    for (int k = 0; k < 16; ++k) {
      float2 aa = cag[k * 32 + ch];
      cag[k * 32 + ch] = c;
      float2 t2 = cmul(l16, c);
      c = make_float2(t2.x + aa.x, t2.y + aa.y);
    }
    if (tm < 7) {
      unsigned long long* g = agg + ((size_t)(tn * 8 + tm) * 32 + ch) * 2;
      __hip_atomic_store(g, (1ull << 32) | (unsigned long long)__float_as_uint(c.x), __ATOMIC_RELAXED, __HIP_MEMORY_SCOPE_AGENT);
      __hip_atomic_store(g + 1, (1ull << 32) | (unsigned long long)__float_as_uint(c.y), __ATOMIC_RELAXED, __HIP_MEMORY_SCOPE_AGENT);
    }
  }
  STAMP(0, 2);
  auto geglu4 = [&](const f32x4& gt, const f32x4& up) -> uint2 {
    unsigned g01 = pk2bf(gt[0], gt[1]), g23 = pk2bf(gt[2], gt[3]);
    unsigned u01 = pk2bf(up[0], up[1]), u23 = pk2bf(up[2], up[3]);
    unsigned a01 = pk2bf(gelu_tanh(lo2f(g01)), gelu_tanh(hi2f(g01)));
    unsigned a23 = pk2bf(gelu_tanh(lo2f(g23)), gelu_tanh(hi2f(g23)));
    uint2 o;
    o.x = pk2bf(lo2f(a01) * lo2f(u01), hi2f(a01) * hi2f(u01));
    o.y = pk2bf(lo2f(a23) * lo2f(u23), hi2f(a23) * hi2f(u23));
    return o;
  };
  if (wc == 0) {
#pragma unroll
    for (int i = 0; i < 4; ++i)
#pragma unroll
      for (int j = 0; j < 4; ++j)
        *reinterpret_cast<uint2*>(actl + (tl + j * 16) * 120 + i * 16 + fq * 4) = geglu4(acc[i][j], acc[i + 4][j]);
  } else {
#pragma unroll
    for (int i = 0; i < 3; ++i)
#pragma unroll
      for (int j = 0; j < 4; ++j)
        *reinterpret_cast<uint2*>(actl + (tl + j * 16) * 120 + 64 + i * 16 + fq * 4) = geglu4(acc[i][j], acc[i + 3][j]);
  }
  __syncthreads();
  STAMP(0, 3);
#pragma unroll
  for (int it = 0; it < 7; ++it) {
    int idx = it * 512 + tid;
    int row = idx / 14, c = idx - row * 14;
    bf16x8 v = *reinterpret_cast<const bf16x8*>(actl + row * 120 + c * 8);
    *reinterpret_cast<bf16x8*>(a2 + (size_t)(tm * 256 + row) * K2_ + tn * 112 + c * 8) = v;
  }
  STAMP(0, 4);
  if (ck < tm) {
    const unsigned long long* g = agg + ((size_t)(tn * 8 + ck) * 32 + ch) * 2;
    unsigned long long v0, v1;
    for (;;) {
      v0 = __hip_atomic_load(g, __ATOMIC_RELAXED, __HIP_MEMORY_SCOPE_AGENT);
      v1 = __hip_atomic_load(g + 1, __ATOMIC_RELAXED, __HIP_MEMORY_SCOPE_AGENT);
      if ((v0 >> 32) == 1ull && (v1 >> 32) == 1ull) break;
      __builtin_amdgcn_s_sleep(1);
    }
    float2 av = make_float2(__uint_as_float((unsigned)v0), __uint_as_float((unsigned)v1));
    for (int e = tm - 1 - ck; e > 0; --e) av = cmul(av, l256);
    term[ck * 32 + ch] = av;
  }
  __syncthreads();
  STAMP(0, 5);
  float2 bc = make_float2(0.f, 0.f);
  for (int j = 0; j < tm; ++j) { float2 tv = term[j * 32 + ch]; bc.x += tv.x; bc.y += tv.y; }
  float2 pw = make_float2(1.f, 0.f);
  for (int e = 0; e < ck; ++e) pw = cmul(pw, l16);
  float2 c0 = cag[ck * 32 + ch];
  float2 t3 = cmul(pw, bc);
  c0.x += t3.x; c0.y += t3.y;
  unsigned short* hl = reinterpret_cast<unsigned short*>(smem + E_BUL);
  {
    float2 pq = lam;
#pragma unroll
    for (int i = 0; i < 16; ++i) {
      float vr = hr[i] + pq.x * c0.x - pq.y * c0.y;
      float vi = hi[i] + pq.x * c0.y + pq.y * c0.x;
      hl[(ck * 16 + i) * 72 + ch] = f2bf(vr);
      hl[(ck * 16 + i) * 72 + 32 + ch] = f2bf(vi);
      pq = cmul(pq, lam);
    }
  }
  __syncthreads();
#pragma unroll
  for (int it = 0; it < 4; ++it) {
    int idx = it * 512 + tid;
    int row = idx >> 3, c = idx & 7;
    bf16x8 v = *reinterpret_cast<const bf16x8*>(hl + row * 72 + c * 8);
    *reinterpret_cast<bf16x8*>(a2 + (size_t)(tm * 256 + row) * K2_ + F_ + (c >> 2) * P_ + tn * 32 + (c & 3) * 8) = v;
  }
  STAMP(0, 6);
}

constexpr int G2_STAGE = 32768;
constexpr int G2_NSLOT = 4;
constexpr int G2_LDS = G2_NSLOT * G2_STAGE + 16;
__global__ __launch_bounds__(512, 2) void k_gemm2(const unsigned short* __restrict__ a2, const unsigned short* __restrict__ wdown,
                                                  const unsigned short* __restrict__ c2, const unsigned short* __restrict__ x,
                                                  const unsigned short* __restrict__ xn, const unsigned short* __restrict__ dskip,
                                                  float* __restrict__ out,
                                                  f32x4* __restrict__ slab, unsigned* __restrict__ cnt) {
  extern __shared__ __attribute__((aligned(16))) char smem[];
  int bid = blockIdx.x;
  int xcd = bid & 7, loc = bid >> 3;
  int tm = (xcd >> 1) * 4 + ((loc >> 3) & 3), tn = (xcd & 1) * 4 + ((loc >> 1) & 3), kz = loc & 1;
  int tile = tm * 8 + tn;
  STAMP(1, 0);
  int tid = threadIdx.x, wid = __builtin_amdgcn_readfirstlane(tid >> 6), lane = tid & 63;
  int kg = wid >> 2, wr = (wid >> 1) & 1, wc = wid & 1, fr = lane & 15, fq = lane >> 4;
  const unsigned short* ag = a2 + (size_t)tm * 128 * K2_ + kz * 2560;
  f32x4 acc[4][4], accf[4][4];
#pragma unroll
  for (int i = 0; i < 4; ++i)
#pragma unroll
    for (int j = 0; j < 4; ++j) { acc[i][j] = (f32x4){0, 0, 0, 0}; accf[i][j] = (f32x4){0, 0, 0, 0}; }
  int srow = lane >> 3, schunk = (lane & 7) ^ (srow & 7);
  int wlofs[2];
#pragma unroll
  for (int i = 0; i < 2; ++i) {
    int reg = wid * 2 + i, Q = reg >> 3, kb = reg & 7;
    int r = lane >> 3, c = (lane & 7) ^ (((r >> 1) & 1) << 1) ^ ((kb & 1) << 2);
    wlofs[i] = (kb * 8 + r) * D_ + tn * 128 + Q * 64 + c * 8;
  }
  auto stage = [&](int slot, int kt) {
    char* base = smem + slot * G2_STAGE;
    int kabs = kz * 2560 + kt * 64;
    const unsigned short* wb = (kabs < F_) ? wdown + (size_t)kabs * D_ : c2 + (size_t)(kabs - F_) * D_;
#pragma unroll
    for (int i = 0; i < 2; ++i) {
      int ins = wid * 2 + i;
      __builtin_amdgcn_global_load_lds((const unsigned*)(ag + (size_t)(ins * 8 + srow) * K2_ + kt * 64 + schunk * 8),
                                       LDS_PTR(base + ins * 1024), 16, 0, 0);
      __builtin_amdgcn_global_load_lds((const unsigned*)(wb + wlofs[i]), LDS_PTR(base + 16384 + ins * 1024), 16, 0, 0);
    }
  };
  const int ns = kz ? 48 : 40;
  const int nsf = kz ? 16 : 40;
  stage(0, 0); stage(1, 1); stage(2, 2); stage(3, 3);
  int rch = ((kg * 4 + fq) ^ (fr & 7)) * 16;
  const int xoff = (wr * 64 + fr) * 128 + rch;
  int wofs[4];
  {
    int kb = kg * 4 + fq, q = (lane >> 2) & 3, pp = lane & 3;
    int sw = ((q >> 1) << 1) ^ ((kb & 1) << 2);
#pragma unroll
    for (int i = 0; i < 4; ++i) wofs[i] = 16384 + (wc * 8 + kb) * 1024 + q * 128 + (((i * 2 + (pp >> 1)) ^ sw) * 16) + (pp & 1) * 8;
  }
  bf16x8 xa_[4], xb_[4], wa_[4], wb_[4];
  const unsigned sbase = lds_u32(smem);
  WAIT_VM(12);
  __builtin_amdgcn_s_barrier();
#pragma unroll
  for (int j = 0; j < 4; ++j) {
    xa_[j] = *reinterpret_cast<const bf16x8*>(smem + xoff + j * 2048);
    wa_[j] = tr_frag<512>(sbase + wofs[j]);
  }
  auto body = [&](int s, bf16x8 (&xc)[4], bf16x8 (&wcur)[4], bf16x8 (&xnx)[4], bf16x8 (&wnx)[4]) {
    if (s < ns - 3) WAIT_VM(8);
    else if (s == ns - 3) WAIT_VM(4);
    else WAIT_VM(0);
    __builtin_amdgcn_s_barrier();
    if (s + 4 < ns) stage(s & 3, s + 4);
    const char* nb = smem + ((s + 1) & 3) * G2_STAGE;
    unsigned nbu = sbase + ((s + 1) & 3) * G2_STAGE;
    TR_WAIT();
#pragma unroll
    for (int i = 0; i < 4; ++i) {
#pragma unroll
      for (int j = 0; j < 4; ++j) acc[i][j] = __builtin_amdgcn_mfma_f32_16x16x32_bf16(wcur[i], xc[j], acc[i][j], 0, 0, 0);
      wnx[i] = tr_frag<512>(nbu + wofs[i]);
      xnx[i] = *reinterpret_cast<const bf16x8*>(nb + xoff + i * 2048);
    }
#pragma unroll
    for (int i = 0; i < 4; ++i) {
      __builtin_amdgcn_sched_group_barrier(0x008, 4, 0);
      __builtin_amdgcn_sched_group_barrier(0x100, 3, 0);
    }
    if (s + 1 == nsf) {
#pragma unroll
      for (int i = 0; i < 4; ++i)
#pragma unroll
        for (int j = 0; j < 4; ++j) { accf[i][j] = acc[i][j]; acc[i][j] = (f32x4){0, 0, 0, 0}; }
    }
  };
  STAMP(1, 10); CSTAMP(1, 11);
  for (int s = 0; s < ns; s += 2) {
    body(s, xa_, wa_, xb_, wb_);
    body(s + 1, xb_, wb_, xa_, wa_);
  }
  STAMP(1, 12); CSTAMP(1, 13);
  __syncthreads();
  STAMP(1, 1);
  f32x4* ex = reinterpret_cast<f32x4*>(smem);
  int wq = wid & 3;
#pragma unroll
  for (int ii = 0; ii < 2; ++ii)
#pragma unroll
    for (int j = 0; j < 4; ++j) {
      f32x4 sf = kg ? accf[ii][j] : accf[2 + ii][j];
      f32x4 sy = kg ? acc[ii][j] : acc[2 + ii][j];
      ex[(((0 * 2 + kg) * 4 + wq) * 8 + ii * 4 + j) * 64 + lane] = sf;
      if (kz) ex[(((1 * 2 + kg) * 4 + wq) * 8 + ii * 4 + j) * 64 + lane] = sy;
    }
  unsigned* role_s = reinterpret_cast<unsigned*>(smem + G2_NSLOT * G2_STAGE);
  if (tid == 0) *role_s = __hip_atomic_fetch_add(&cnt[tile * 32], 1u, __ATOMIC_RELAXED, __HIP_MEMORY_SCOPE_AGENT);
  __syncthreads();
  f32x4 mf[2][4], my[2][4];
#pragma unroll
  for (int ii = 0; ii < 2; ++ii)
#pragma unroll
    for (int j = 0; j < 4; ++j) {
      f32x4 ownf = kg ? accf[2 + ii][j] : accf[ii][j];
      f32x4 owny = kg ? acc[2 + ii][j] : acc[ii][j];
      mf[ii][j] = ownf + ex[(((0 * 2 + (kg ^ 1)) * 4 + wq) * 8 + ii * 4 + j) * 64 + lane];
      my[ii][j] = owny;
      if (kz) my[ii][j] += ex[(((1 * 2 + (kg ^ 1)) * 4 + wq) * 8 + ii * 4 + j) * 64 + lane];
    }
  unsigned role = *role_s;
  STAMP(1, 2);
  f32x4* sl = slab + (size_t)tile * 8192;
  if (role == 0) {
    __amdgpu_buffer_rsrc_t rs = __builtin_amdgcn_make_buffer_rsrc((void*)sl, 0, 8192 * 16, 0x00020000);
#pragma unroll
    for (int ii = 0; ii < 2; ++ii)
#pragma unroll
      for (int j = 0; j < 4; ++j) {
        u32x4 v = __builtin_bit_cast(u32x4, mf[ii][j]);
        __builtin_amdgcn_raw_buffer_store_b128(v, rs, ((ii * 4 + j) * 512 + tid) * 16, 0, 16);
        if (kz) {
          u32x4 w = __builtin_bit_cast(u32x4, my[ii][j]);
          __builtin_amdgcn_raw_buffer_store_b128(w, rs, (4096 + (ii * 4 + j) * 512 + tid) * 16, 0, 16);
        }
      }
    asm volatile("s_waitcnt vmcnt(0)" ::: "memory");
    __syncthreads();
    if (tid == 0) __hip_atomic_store(&cnt[tile * 32 + 16], 1u, __ATOMIC_RELAXED, __HIP_MEMORY_SCOPE_AGENT);
    STAMP(1, 3);
    return;
  }
  u16x4 xv[2][4], xnv[2][4], dv[2];
#pragma unroll
  for (int ii = 0; ii < 2; ++ii) {
    int d = tn * 128 + wc * 64 + (kg * 2 + ii) * 16 + fq * 4;
    dv[ii] = *reinterpret_cast<const u16x4*>(dskip + d);
#pragma unroll
    for (int j = 0; j < 4; ++j) {
      int t = tm * 128 + wr * 64 + j * 16 + fr;
      xv[ii][j] = *reinterpret_cast<const u16x4*>(x + (size_t)t * D_ + d);
      xnv[ii][j] = *reinterpret_cast<const u16x4*>(xn + (size_t)t * D_ + d);
    }
  }
  if (tid == 0) {
    while (__hip_atomic_load(&cnt[tile * 32 + 16], __ATOMIC_RELAXED, __HIP_MEMORY_SCOPE_AGENT) == 0u) __builtin_amdgcn_s_sleep(1);
  }
  __syncthreads();
  __builtin_amdgcn_fence(__ATOMIC_ACQUIRE, "wavefront");
  STAMP(1, 3);
  {
    __amdgpu_buffer_rsrc_t rs = __builtin_amdgcn_make_buffer_rsrc((void*)sl, 0, 8192 * 16, 0x00020000);
#pragma unroll
    for (int ii = 0; ii < 2; ++ii)
#pragma unroll
      for (int j = 0; j < 4; ++j) {
        u32x4 v = __builtin_amdgcn_raw_buffer_load_b128(rs, ((ii * 4 + j) * 512 + tid) * 16, 0, 16);
        mf[ii][j] += __builtin_bit_cast(f32x4, v);
        if (!kz) {
          u32x4 w = __builtin_amdgcn_raw_buffer_load_b128(rs, (4096 + (ii * 4 + j) * 512 + tid) * 16, 0, 16);
          my[ii][j] = __builtin_bit_cast(f32x4, w);
        }
      }
  }
#pragma unroll
  for (int ii = 0; ii < 2; ++ii)
#pragma unroll
    for (int j = 0; j < 4; ++j) {
      int t = tm * 128 + wr * 64 + j * 16 + fr;
      int d = tn * 128 + wc * 64 + (kg * 2 + ii) * 16 + fq * 4;
      f32x4 o;
#pragma unroll
      for (int r = 0; r < 4; ++r) {
        float f = rbf(mf[ii][j][r]);
        float g = rbf(f * __builtin_amdgcn_rcpf(1.0f + __expf(-f)));
        float s = 2.0f * my[ii][j][r] + rbf(bf2f(xnv[ii][j][r]) * bf2f(dv[ii][r]));
        o[r] = bf2f(xv[ii][j][r]) + s * g;
      }
      *reinterpret_cast<f32x4*>(out + (size_t)t * D_ + d) = o;
    }
  STAMP(1, 4);
}

extern "C" void kernel_launch(void* const* d_in, const int* in_sizes, int n_in, void* d_out, int out_size, void* d_ws,
                              size_t ws_size, hipStream_t stream) {
  PrepArgs pa;
  pa.x = (const unsigned short*)d_in[0];
  pa.lre = (const unsigned short*)d_in[1];
  pa.lim = (const unsigned short*)d_in[2];
  pa.bre = (const unsigned short*)d_in[3];
  pa.bim = (const unsigned short*)d_in[4];
  pa.cre = (const unsigned short*)d_in[5];
  pa.cim = (const unsigned short*)d_in[6];
  pa.logdt = (const unsigned short*)d_in[7];
  const unsigned short* dskip = (const unsigned short*)d_in[8];
  const unsigned short* wgate = (const unsigned short*)d_in[10];
  const unsigned short* wup = (const unsigned short*)d_in[11];
  const unsigned short* wdown = (const unsigned short*)d_in[12];
  char* ws = (char*)d_ws;
  pa.ws = ws;
  unsigned short* xn = (unsigned short*)(ws + OFF_XN);
  unsigned short* a2 = (unsigned short*)(ws + OFF_A2);
  float* out = (float*)d_out;

  k_prep<<<PB_TOTAL, 256, 0, stream>>>(pa);
  (void)hipFuncSetAttribute((const void*)k_gemm1, hipFuncAttributeMaxDynamicSharedMemorySize, G1_LDS);
  k_gemm1<<<256, 512, G1_LDS, stream>>>(xn, wgate, wup, (const unsigned short*)(ws + OFF_BBT), a2, (const float2*)(ws + OFF_LAM),
                                        (unsigned*)(ws + OFF_TICK), (unsigned long long*)(ws + OFF_AGG));
  (void)hipFuncSetAttribute((const void*)k_gemm2, hipFuncAttributeMaxDynamicSharedMemorySize, G2_LDS);
  k_gemm2<<<256, 512, G2_LDS, stream>>>(a2, wdown, (const unsigned short*)(ws + OFF_C2), pa.x, xn, dskip, out,
                                        (f32x4*)(ws + OFF_SLAB), (unsigned*)(ws + OFF_CNT));
}
```

```cpp
#include <hip/hip_runtime.h>
#include <hip/hip_bf16.h>
#include <stdint.h>

typedef __attribute__((ext_vector_type(8))) short bf16x8;
typedef __attribute__((ext_vector_type(4))) float f32x4;
typedef __attribute__((ext_vector_type(4))) unsigned short u16x4;
typedef __attribute__((ext_vector_type(4))) unsigned u32x4;

constexpr int S_ = 2048;
constexpr int D_ = 1024;
constexpr int P_ = 1024;
constexpr int F_ = 3584;
constexpr int N1_ = 9216;
constexpr int K2_ = 5632;

constexpr size_t OFF_AGG = 0;
constexpr size_t OFF_CNT = OFF_AGG + 32 * 8 * 32 * 2 * 8;
constexpr size_t SYNC_BYTES = OFF_CNT + 128 * 32 * 4;
constexpr size_t OFF_XN = SYNC_BYTES;
constexpr size_t OFF_A2 = OFF_XN + (size_t)S_ * D_ * 2;
constexpr size_t OFF_SLAB = OFF_A2 + (size_t)S_ * K2_ * 2;

__device__ __forceinline__ float bf2f(unsigned short v) { return __uint_as_float(((unsigned)v) << 16); }
__device__ __forceinline__ unsigned short f2bf(float f) {
  __bf16 b = (__bf16)f;
  return __builtin_bit_cast(unsigned short, b);
}
typedef __attribute__((ext_vector_type(2))) __bf16 bf16x2n;
typedef __attribute__((ext_vector_type(2))) float f32x2;
__device__ __forceinline__ unsigned pk2bf(float a, float b) {
  f32x2 v = {a, b};
  bf16x2n r = __builtin_convertvector(v, bf16x2n);
  return __builtin_bit_cast(unsigned, r);
}
__device__ __forceinline__ float lo2f(unsigned u) { return __uint_as_float(u << 16); }
__device__ __forceinline__ float hi2f(unsigned u) { return __uint_as_float(u & 0xFFFF0000u); }
__device__ __forceinline__ float rbf(float f) { return bf2f(f2bf(f)); }
__device__ __forceinline__ float2 cmul(float2 a, float2 b) { return make_float2(a.x * b.x - a.y * b.y, a.x * b.y + a.y * b.x); }

#define STAMP(k, i)
#define CSTAMP(k, i)
#define WAIT_VM(n) asm volatile("s_waitcnt vmcnt(" #n ")" ::: "memory")
#define LDS_PTR(p) ((__attribute__((address_space(3))) unsigned*)(p))

constexpr int PB_LN = 512;
__global__ __launch_bounds__(256) void k_prep(const unsigned short* __restrict__ x, char* __restrict__ ws) {
  int b = blockIdx.x, tid = threadIdx.x, wave = tid >> 6, lane = tid & 63;
  unsigned short* xn = (unsigned short*)(ws + OFF_XN);
  for (int i = b * 256 + tid; i < (int)(SYNC_BYTES / 16); i += PB_LN * 256)
    reinterpret_cast<u32x4*>(ws)[i] = (u32x4){0u, 0u, 0u, 0u};
  int row = b * 4 + wave;
  const bf16x8* src = reinterpret_cast<const bf16x8*>(x + (size_t)row * D_);
  bf16x8 v0 = src[lane], v1 = src[64 + lane];
  float f[16];
#pragma unroll
  for (int j = 0; j < 8; ++j) { f[j] = bf2f((unsigned short)v0[j]); f[8 + j] = bf2f((unsigned short)v1[j]); }
  float s = 0.f;
#pragma unroll
  for (int j = 0; j < 16; ++j) s += f[j];
#pragma unroll
  for (int o = 32; o >= 1; o >>= 1) s += __shfl_xor(s, o);
  float mean = s * (1.0f / D_);
  float q = 0.f;
#pragma unroll
  for (int j = 0; j < 16; ++j) { float d = f[j] - mean; q += d * d; }
#pragma unroll
  for (int o = 32; o >= 1; o >>= 1) q += __shfl_xor(q, o);
  float var = q * (1.0f / D_);
  float mb = rbf(mean), vb = rbf(var);
  float den = rbf(sqrtf(rbf(vb + 1e-5f)));
  bf16x8 o0, o1;
#pragma unroll
  for (int j = 0; j < 8; ++j) {
    o0[j] = (short)f2bf(rbf(f[j] - mb) / den);
    o1[j] = (short)f2bf(rbf(f[8 + j] - mb) / den);
  }
  bf16x8* dst = reinterpret_cast<bf16x8*>(xn + (size_t)row * D_);
  dst[lane] = o0; dst[64 + lane] = o1;
}

typedef __attribute__((ext_vector_type(4))) short s16x4;
template <int OFF2>
__device__ __forceinline__ bf16x8 tr_frag(unsigned lds_addr) {
  s16x4 lo, hi;
  asm volatile("ds_read_b64_tr_b16 %0, %2\n\tds_read_b64_tr_b16 %1, %2 offset:%c3"
               : "=&v"(lo), "=&v"(hi) : "v"(lds_addr), "i"(OFF2));
  return __builtin_shufflevector(lo, hi, 0, 1, 2, 3, 4, 5, 6, 7);
}
#define TR_WAIT() do { asm volatile("s_waitcnt lgkmcnt(0)" ::: "memory"); __builtin_amdgcn_sched_barrier(0); } while (0)
__device__ __forceinline__ unsigned lds_u32(const void* p) {
  return (unsigned)(uintptr_t)(__attribute__((address_space(3))) const char*)p;
}

__device__ __forceinline__ float gelu_tanh(float x) {
  float w = x * __builtin_fmaf(x * x, -0.10294323970650479f, -2.3022081985952363f);
  return x * __builtin_amdgcn_rcpf(1.0f + __builtin_amdgcn_exp2f(w));
}

constexpr int G1_AT = 256 * 64;
constexpr int G1_WT = 288 * 64;
constexpr int G1_STAGE = G1_AT + G1_WT;
constexpr int G1_NSLOT = 4;
constexpr int G1_LDS = G1_NSLOT * G1_STAGE;
constexpr int E_BUL = 0;
constexpr int E_ACT = 69632;
constexpr int E_CAG = 131072;
constexpr int E_TERM = 135168;
constexpr int E_MISC = 137216;

struct G1Args {
  const unsigned short *xn, *wgate, *wup, *bre, *bim, *lre, *lim, *logdt;
  unsigned short* a2;
  unsigned long long* agg;
};
template <int V> struct IC { static constexpr int value = V; };

__global__ __launch_bounds__(512, 2) void k_gemm1(G1Args ga) {
  extern __shared__ __attribute__((aligned(16))) char smem[];
  int tid = threadIdx.x, wid = __builtin_amdgcn_readfirstlane(tid >> 6), lane = tid & 63;
  int xl = blockIdx.x & 7, loc = blockIdx.x >> 3;
  STAMP(0, 0);
  int tn = xl * 4 + (loc >> 3), tm = loc & 7;
  int wr = wid >> 1, wc = wid & 1;
  int fr = lane & 15, fq = lane >> 4;
  const unsigned short* xg = ga.xn + (size_t)tm * 256 * D_;
  unsigned short* a2 = ga.a2;
  unsigned long long* agg = ga.agg;

  f32x4 acc[9][4];
#pragma unroll
  for (int i = 0; i < 9; ++i)
#pragma unroll
    for (int j = 0; j < 4; ++j) acc[i][j] = (f32x4){0.f, 0.f, 0.f, 0.f};

  int srow = lane >> 2;
  int schunk = (lane & 3) ^ (((srow >> 3) & 1) << 1);
  const char* wsrc[3]; int wstr[3];
#pragma unroll
  for (int i = 0; i < 3; ++i) {
    int reg = (i < 2) ? wid * 2 + i : 16 + (wid & 1);
    const unsigned short* ptr; int str;
    if (reg < 12) {
      int Q = reg >> 2, kb = reg & 3;
      int r = lane >> 3, c = (lane & 7) ^ (((r >> 1) & 1) << 1) ^ ((kb & 1) << 2);
      int k = kb * 8 + r;
      const unsigned short* mat; int col;
      if (Q == 0) { mat = ga.wgate; col = c * 8; }
      else if (Q == 1) { mat = ga.wup; col = c * 8; }
      else if (c < 6) { mat = ga.wgate; col = 64 + c * 8; }
      else { mat = ga.wup; col = 64 + (c - 6) * 8; }
      ptr = mat + (size_t)k * F_ + tn * 112 + col; str = F_ * 64;
    } else if (reg < 14) {
      int r = lane >> 3, c = (lane & 7) ^ (((r >> 1) & 1) << 1);
      int k = ((reg - 12) * 2 + (c >> 2)) * 8 + r;
      ptr = ga.wup + (size_t)k * F_ + tn * 112 + 80 + (c & 3) * 8; str = F_ * 64;
    } else {
      int row = (reg - 14) * 16 + srow;
      const unsigned short* mat = (row < 32) ? ga.bre : ga.bim;
      ptr = mat + (size_t)(tn * 32 + (row & 31)) * D_ + schunk * 8; str = 64;
    }
    wsrc[i] = (const char*)ptr; wstr[i] = str;
  }
  auto stage = [&](int slot, int ks) {
    char* base = smem + slot * G1_STAGE;
#pragma unroll
    for (int i = 0; i < 2; ++i) {
      int ins = wid * 2 + i;
      __builtin_amdgcn_global_load_lds((const unsigned*)(xg + (size_t)(ins * 16 + srow) * D_ + ks * 32 + schunk * 8),
                                       LDS_PTR(base + ins * 1024), 16, 0, 0);
    }
#pragma unroll
    for (int i = 0; i < 2; ++i)
      __builtin_amdgcn_global_load_lds((const unsigned*)(wsrc[i] + (size_t)ks * wstr[i]), LDS_PTR(base + G1_AT + (wid * 2 + i) * 1024), 16, 0, 0);
    if (wid < 2)
      __builtin_amdgcn_global_load_lds((const unsigned*)(wsrc[2] + (size_t)ks * wstr[2]), LDS_PTR(base + G1_AT + (16 + wid) * 1024), 16, 0, 0);
  };

  const int ns = D_ / 32;
  stage(0, 0); stage(1, 1); stage(2, 2); stage(3, 3);
  int rchunk = fq ^ (((fr >> 3) & 1) << 1);
  const int xoff = (wr * 64 + fr) * 64 + rchunk * 16;
  const int boff = G1_AT + 14 * 1024 + fr * 64 + rchunk * 16;
  const unsigned sbase = lds_u32(smem);
  auto mainloop = [&](auto WCc) {
    constexpr int WC = decltype(WCc)::value;
    constexpr int NTR = WC ? 6 : 8;
    int kb = fq, q = (lane >> 2) & 3, pp = lane & 3;
    int sw = ((q >> 1) << 1) ^ ((kb & 1) << 2);
    int wo[NTR];
#pragma unroll
    for (int i = 0; i < NTR; ++i) {
      if (WC == 0) wo[i] = G1_AT + ((i >> 2) * 4 + kb) * 1024 + q * 128 + ((((i & 3) * 2 + (pp >> 1)) ^ sw) * 16) + (pp & 1) * 8;
      else if (i < 4) wo[i] = G1_AT + (8 + kb) * 1024 + q * 128 + (((i * 2 + (pp >> 1)) ^ sw) * 16) + (pp & 1) * 8;
      else wo[i] = G1_AT + (12 + (kb >> 1)) * 1024 + q * 128 + ((((kb & 1) * 4 + (i - 4) * 2 + (pp >> 1)) ^ ((q >> 1) << 1)) * 16) + (pp & 1) * 8;
    }
    unsigned wpk[NTR / 2];
#pragma unroll
    for (int i = 0; i < NTR / 2; ++i) wpk[i] = (unsigned)wo[2 * i] | ((unsigned)wo[2 * i + 1] << 16);
    auto ldw = [&](int i, const char* nb, unsigned nbu) -> bf16x8 {
      if (i < NTR) return tr_frag<512>(nbu + ((i & 1) ? (wpk[i >> 1] >> 16) : (wpk[i >> 1] & 0xFFFFu)));
      return *reinterpret_cast<const bf16x8*>(nb + boff + (WC ? (i - NTR + 1) : 0) * 1024);
    };
    bf16x8 xa_[4], xb_[4], wf[9];
    if (wid < 2) WAIT_VM(15); else WAIT_VM(12);
    __builtin_amdgcn_s_barrier();
#pragma unroll
    for (int j = 0; j < 4; ++j) xa_[j] = *reinterpret_cast<const bf16x8*>(smem + xoff + j * 1024);
#pragma unroll
    for (int i = 0; i < 9; ++i) wf[i] = ldw(i, smem, sbase);

    auto body = [&](int s, bf16x8 (&xc)[4], bf16x8 (&xnx)[4]) {
      if (s < ns - 3) { if (wid < 2) WAIT_VM(10); else WAIT_VM(8); }
      else if (s == ns - 3) { if (wid < 2) WAIT_VM(5); else WAIT_VM(4); }
      else WAIT_VM(0);
      __builtin_amdgcn_s_barrier();
      if (s + 4 < ns) stage(s & 3, s + 4);
      const char* nb = smem + ((s + 1) & 3) * G1_STAGE;
      unsigned nbu = sbase + ((s + 1) & 3) * G1_STAGE;
      TR_WAIT();
#pragma unroll
      for (int i = 0; i < 9; ++i) {
#pragma unroll
        for (int j = 0; j < 4; ++j) acc[i][j] = __builtin_amdgcn_mfma_f32_16x16x32_bf16(wf[i], xc[j], acc[i][j], 0, 0, 0);
        wf[i] = ldw(i, nb, nbu);
        if (i < 4) xnx[i] = *reinterpret_cast<const bf16x8*>(nb + xoff + i * 1024);
      }
#pragma unroll
      for (int i = 0; i < 9; ++i) {
        __builtin_amdgcn_sched_group_barrier(0x008, 4, 0);
        __builtin_amdgcn_sched_group_barrier(0x100, 1, 0);
      }
    };
    STAMP(0, 10); CSTAMP(0, 11);
    for (int s = 0; s < ns; s += 2) {
      body(s, xa_, xb_);
      body(s + 1, xb_, xa_);
    }
    STAMP(0, 12); CSTAMP(0, 13);
  };
  if (wc == 0) mainloop(IC<0>{}); else mainloop(IC<1>{});

  __syncthreads();
  STAMP(0, 1);
  float* bul = reinterpret_cast<float*>(smem + E_BUL);
  unsigned short* actl = reinterpret_cast<unsigned short*>(smem + E_ACT);
  float2* cag = reinterpret_cast<float2*>(smem + E_CAG);
  float2* term = reinterpret_cast<float2*>(smem + E_TERM);
  int tl = wr * 64 + fr;
  if (wc == 0) {
#pragma unroll
    for (int j = 0; j < 4; ++j) *reinterpret_cast<f32x4*>(bul + (tl + j * 16) * 68 + fq * 4) = acc[8][j];
  } else {
#pragma unroll
    for (int j = 0; j < 4; ++j) {
      *reinterpret_cast<f32x4*>(bul + (tl + j * 16) * 68 + 16 + fq * 4) = acc[6][j];
      *reinterpret_cast<f32x4*>(bul + (tl + j * 16) * 68 + 32 + fq * 4) = acc[7][j];
      *reinterpret_cast<f32x4*>(bul + (tl + j * 16) * 68 + 48 + fq * 4) = acc[8][j];
    }
  }
  __syncthreads();
  int ch = tid & 31, ck = tid >> 5;
  int p = tn * 32 + ch;
  float2 lam, cf;
  {
    float lr = bf2f(ga.lre[p]), li = bf2f(ga.lim[p]);
    float dt = rbf(expf(bf2f(ga.logdt[p])));
    float zr = dt * lr, zi = dt * li;
    float e = expf(zr);
    lam = make_float2(e * cosf(zi), e * sinf(zi));
    float nr = lam.x - 1.0f, ni = lam.y;
    float den = lr * lr + li * li;
    cf = make_float2((nr * lr + ni * li) / den, (ni * lr - nr * li) / den);
  }
  float hr[16], hi[16];
  {
    float sr = 0.f, si = 0.f;
#pragma unroll
    for (int i = 0; i < 16; ++i) {
      float rr = bul[(ck * 16 + i) * 68 + ch], ri = bul[(ck * 16 + i) * 68 + 32 + ch];
      float br = cf.x * rr - cf.y * ri, bi = cf.x * ri + cf.y * rr;
      float nr = lam.x * sr - lam.y * si + br;
      float ni = lam.x * si + lam.y * sr + bi;
      sr = nr; si = ni; hr[i] = sr; hi[i] = si;
    }
    cag[ck * 32 + ch] = make_float2(sr, si);
  }
  float2 l16 = lam;
#pragma unroll
  for (int i = 0; i < 4; ++i) l16 = cmul(l16, l16);
  float2 l256 = l16;
#pragma unroll
  for (int i = 0; i < 4; ++i) l256 = cmul(l256, l256);
  __syncthreads();
  if (tid < 32) {
    float2 c = make_float2(0.f, 0.f);
#pragma unroll
    for (int k = 0; k < 16; ++k) {
      float2 aa = cag[k * 32 + ch];
      cag[k * 32 + ch] = c;
      float2 t2 = cmul(l16, c);
      c = make_float2(t2.x + aa.x, t2.y + aa.y);
    }
    if (tm < 7) {
      unsigned long long* g = agg + ((size_t)(tn * 8 + tm) * 32 + ch) * 2;
      __hip_atomic_store(g, (1ull << 32) | (unsigned long long)__float_as_uint(c.x), __ATOMIC_RELAXED, __HIP_MEMORY_SCOPE_AGENT);
      __hip_atomic_store(g + 1, (1ull << 32) | (unsigned long long)__float_as_uint(c.y), __ATOMIC_RELAXED, __HIP_MEMORY_SCOPE_AGENT);
    }
  }
  STAMP(0, 2);
  auto geglu4 = [&](const f32x4& gt, const f32x4& up) -> uint2 {
    unsigned g01 = pk2bf(gt[0], gt[1]), g23 = pk2bf(gt[2], gt[3]);
    unsigned u01 = pk2bf(up[0], up[1]), u23 = pk2bf(up[2], up[3]);
    unsigned a01 = pk2bf(gelu_tanh(lo2f(g01)), gelu_tanh(hi2f(g01)));
    unsigned a23 = pk2bf(gelu_tanh(lo2f(g23)), gelu_tanh(hi2f(g23)));
    uint2 o;
    o.x = pk2bf(lo2f(a01) * lo2f(u01), hi2f(a01) * hi2f(u01));
    o.y = pk2bf(lo2f(a23) * lo2f(u23), hi2f(a23) * hi2f(u23));
    return o;
  };
  if (wc == 0) {
#pragma unroll
    for (int i = 0; i < 4; ++i)
#pragma unroll
      for (int j = 0; j < 4; ++j)
        *reinterpret_cast<uint2*>(actl + (tl + j * 16) * 120 + i * 16 + fq * 4) = geglu4(acc[i][j], acc[i + 4][j]);
  } else {
#pragma unroll
    for (int i = 0; i < 3; ++i)
#pragma unroll
      for (int j = 0; j < 4; ++j)
        *reinterpret_cast<uint2*>(actl + (tl + j * 16) * 120 + 64 + i * 16 + fq * 4) = geglu4(acc[i][j], acc[i + 3][j]);
  }
  __syncthreads();
  STAMP(0, 3);
#pragma unroll
  for (int it = 0; it < 7; ++it) {
    int idx = it * 512 + tid;
    int row = idx / 14, c = idx - row * 14;
    bf16x8 v = *reinterpret_cast<const bf16x8*>(actl + row * 120 + c * 8);
    *reinterpret_cast<bf16x8*>(a2 + (size_t)(tm * 256 + row) * K2_ + tn * 112 + c * 8) = v;
  }
  STAMP(0, 4);
  if (ck < tm) {
    const unsigned long long* g = agg + ((size_t)(tn * 8 + ck) * 32 + ch) * 2;
    unsigned long long v0, v1;
    for (;;) {
      v0 = __hip_atomic_load(g, __ATOMIC_RELAXED, __HIP_MEMORY_SCOPE_AGENT);
      v1 = __hip_atomic_load(g + 1, __ATOMIC_RELAXED, __HIP_MEMORY_SCOPE_AGENT);
      if ((v0 >> 32) == 1ull && (v1 >> 32) == 1ull) break;
      __builtin_amdgcn_s_sleep(1);
    }
    float2 av = make_float2(__uint_as_float((unsigned)v0), __uint_as_float((unsigned)v1));
    for (int e = tm - 1 - ck; e > 0; --e) av = cmul(av, l256);
    term[ck * 32 + ch] = av;
  }
  __syncthreads();
  STAMP(0, 5);
  float2 bc = make_float2(0.f, 0.f);
  for (int j = 0; j < tm; ++j) { float2 tv = term[j * 32 + ch]; bc.x += tv.x; bc.y += tv.y; }
  float2 pw = make_float2(1.f, 0.f);
  for (int e = 0; e < ck; ++e) pw = cmul(pw, l16);
  float2 c0 = cag[ck * 32 + ch];
  float2 t3 = cmul(pw, bc);
  c0.x += t3.x; c0.y += t3.y;
  unsigned short* hl = reinterpret_cast<unsigned short*>(smem + E_BUL);
  {
    float2 pq = lam;
#pragma unroll
    for (int i = 0; i < 16; ++i) {
      float vr = hr[i] + pq.x * c0.x - pq.y * c0.y;
      float vi = hi[i] + pq.x * c0.y + pq.y * c0.x;
      hl[(ck * 16 + i) * 72 + ch] = f2bf(vr);
      hl[(ck * 16 + i) * 72 + 32 + ch] = f2bf(-vi);
      pq = cmul(pq, lam);
    }
  }
  __syncthreads();
#pragma unroll
  for (int it = 0; it < 4; ++it) {
    int idx = it * 512 + tid;
    int row = idx >> 3, c = idx & 7;
    bf16x8 v = *reinterpret_cast<const bf16x8*>(hl + row * 72 + c * 8);
    *reinterpret_cast<bf16x8*>(a2 + (size_t)(tm * 256 + row) * K2_ + F_ + (c >> 2) * P_ + tn * 32 + (c & 3) * 8) = v;
  }
  STAMP(0, 6);
}

constexpr int G2_STAGE = 32768;
constexpr int G2_NSLOT = 4;
constexpr int G2_LDS = G2_NSLOT * G2_STAGE + 16;
__global__ __launch_bounds__(512, 2) void k_gemm2(const unsigned short* __restrict__ a2, const unsigned short* __restrict__ wdown,
                                                  const unsigned short* __restrict__ cre, const unsigned short* __restrict__ cim,
                                                  const unsigned short* __restrict__ x,
                                                  const unsigned short* __restrict__ xn, const unsigned short* __restrict__ dskip,
                                                  float* __restrict__ out,
                                                  f32x4* __restrict__ slab, unsigned* __restrict__ cnt) {
  extern __shared__ __attribute__((aligned(16))) char smem[];
  int bid = blockIdx.x;
  int xcd = bid & 7, loc = bid >> 3;
  int tm = (xcd >> 1) * 4 + ((loc >> 3) & 3), tn = (xcd & 1) * 4 + ((loc >> 1) & 3), kz = loc & 1;
  int tile = tm * 8 + tn;
  STAMP(1, 0);
  int tid = threadIdx.x, wid = __builtin_amdgcn_readfirstlane(tid >> 6), lane = tid & 63;
  int kg = wid >> 2, wr = (wid >> 1) & 1, wc = wid & 1, fr = lane & 15, fq = lane >> 4;
  const unsigned short* ag = a2 + (size_t)tm * 128 * K2_ + kz * 2560;
  f32x4 acc[4][4], accf[4][4];
#pragma unroll
  for (int i = 0; i < 4; ++i)
#pragma unroll
    for (int j = 0; j < 4; ++j) { acc[i][j] = (f32x4){0, 0, 0, 0}; accf[i][j] = (f32x4){0, 0, 0, 0}; }
  int srow = lane >> 3, schunk = (lane & 7) ^ (srow & 7);
  int wlofs[2];
#pragma unroll
  for (int i = 0; i < 2; ++i) {
    int reg = wid * 2 + i, Q = reg >> 3, kb = reg & 7;
    int r = lane >> 3, c = (lane & 7) ^ (((r >> 1) & 1) << 1) ^ ((kb & 1) << 2);
    wlofs[i] = (kb * 8 + r) * D_ + tn * 128 + Q * 64 + c * 8;
  }
  auto stage = [&](int slot, int kt) {
    char* base = smem + slot * G2_STAGE;
    int kabs = kz * 2560 + kt * 64;
    bool ffn = kabs < F_;
    const unsigned short* wb = ffn ? wdown + (size_t)kabs * D_ : ((kabs < F_ + P_) ? cre + (kabs - F_) : cim + (kabs - F_ - P_));
#pragma unroll
    for (int i = 0; i < 2; ++i) {
      int ins = wid * 2 + i;
      __builtin_amdgcn_global_load_lds((const unsigned*)(ag + (size_t)(ins * 8 + srow) * K2_ + kt * 64 + schunk * 8),
                                       LDS_PTR(base + ins * 1024), 16, 0, 0);
      __builtin_amdgcn_global_load_lds((const unsigned*)(wb + (ffn ? wlofs[i] : (tn * 128 + ins * 8 + srow) * P_ + schunk * 8)),
                                       LDS_PTR(base + 16384 + ins * 1024), 16, 0, 0);
    }
  };
  const int ns = kz ? 48 : 40;
  const int nsf = kz ? 16 : 40;
  stage(0, 0); stage(1, 1); stage(2, 2); stage(3, 3);
  int rch = ((kg * 4 + fq) ^ (fr & 7)) * 16;
  const int xoff = (wr * 64 + fr) * 128 + rch;
  const int woffc = 16384 + (wc * 64 + fr) * 128 + rch;
  int wofs[4];
  {
    int kb = kg * 4 + fq, q = (lane >> 2) & 3, pp = lane & 3;
    int sw = ((q >> 1) << 1) ^ ((kb & 1) << 2);
#pragma unroll
    for (int i = 0; i < 4; ++i) wofs[i] = 16384 + (wc * 8 + kb) * 1024 + q * 128 + (((i * 2 + (pp >> 1)) ^ sw) * 16) + (pp & 1) * 8;
  }
  bf16x8 xa_[4], xb_[4], wa_[4], wb_[4];
  const unsigned sbase = lds_u32(smem);
  WAIT_VM(12);
  __builtin_amdgcn_s_barrier();
#pragma unroll
  for (int j = 0; j < 4; ++j) {
    xa_[j] = *reinterpret_cast<const bf16x8*>(smem + xoff + j * 2048);
    wa_[j] = tr_frag<512>(sbase + wofs[j]);
  }
  auto body = [&](int s, bf16x8 (&xc)[4], bf16x8 (&wcur)[4], bf16x8 (&xnx)[4], bf16x8 (&wnx)[4]) {
    if (s < ns - 3) WAIT_VM(8);
    else if (s == ns - 3) WAIT_VM(4);
    else WAIT_VM(0);
    __builtin_amdgcn_s_barrier();
    if (s + 4 < ns) stage(s & 3, s + 4);
    const char* nb = smem + ((s + 1) & 3) * G2_STAGE;
    unsigned nbu = sbase + ((s + 1) & 3) * G2_STAGE;
    TR_WAIT();
#pragma unroll
    for (int i = 0; i < 4; ++i) {
#pragma unroll
      for (int j = 0; j < 4; ++j) acc[i][j] = __builtin_amdgcn_mfma_f32_16x16x32_bf16(wcur[i], xc[j], acc[i][j], 0, 0, 0);
      if (s + 1 < nsf) wnx[i] = tr_frag<512>(nbu + wofs[i]);
      else wnx[i] = *reinterpret_cast<const bf16x8*>(nb + woffc + i * 2048);
      xnx[i] = *reinterpret_cast<const bf16x8*>(nb + xoff + i * 2048);
    }
#pragma unroll
    for (int i = 0; i < 4; ++i) {
      __builtin_amdgcn_sched_group_barrier(0x008, 4, 0);
      __builtin_amdgcn_sched_group_barrier(0x100, 3, 0);
    }
    if (s + 1 == nsf) {
#pragma unroll
      for (int i = 0; i < 4; ++i)
#pragma unroll
        for (int j = 0; j < 4; ++j) { accf[i][j] = acc[i][j]; acc[i][j] = (f32x4){0, 0, 0, 0}; }
    }
  };
  STAMP(1, 10); CSTAMP(1, 11);
  for (int s = 0; s < ns; s += 2) {
    body(s, xa_, wa_, xb_, wb_);
    body(s + 1, xb_, wb_, xa_, wa_);
  }
  STAMP(1, 12); CSTAMP(1, 13);
  __syncthreads();
  STAMP(1, 1);
  f32x4* ex = reinterpret_cast<f32x4*>(smem);
  int wq = wid & 3;
#pragma unroll
  for (int ii = 0; ii < 2; ++ii)
#pragma unroll
    for (int j = 0; j < 4; ++j) {
      f32x4 sf = kg ? accf[ii][j] : accf[2 + ii][j];
      f32x4 sy = kg ? acc[ii][j] : acc[2 + ii][j];
      ex[(((0 * 2 + kg) * 4 + wq) * 8 + ii * 4 + j) * 64 + lane] = sf;
      if (kz) ex[(((1 * 2 + kg) * 4 + wq) * 8 + ii * 4 + j) * 64 + lane] = sy;
    }
  unsigned* role_s = reinterpret_cast<unsigned*>(smem + G2_NSLOT * G2_STAGE);
  if (tid == 0) *role_s = __hip_atomic_fetch_add(&cnt[tile * 32], 1u, __ATOMIC_RELAXED, __HIP_MEMORY_SCOPE_AGENT);
  __syncthreads();
  f32x4 mf[2][4], my[2][4];
#pragma unroll
  for (int ii = 0; ii < 2; ++ii)
#pragma unroll
    for (int j = 0; j < 4; ++j) {
      f32x4 ownf = kg ? accf[2 + ii][j] : accf[ii][j];
      f32x4 owny = kg ? acc[2 + ii][j] : acc[ii][j];
      mf[ii][j] = ownf + ex[(((0 * 2 + (kg ^ 1)) * 4 + wq) * 8 + ii * 4 + j) * 64 + lane];
      my[ii][j] = owny;
      if (kz) my[ii][j] += ex[(((1 * 2 + (kg ^ 1)) * 4 + wq) * 8 + ii * 4 + j) * 64 + lane];
    }
  unsigned role = *role_s;
  STAMP(1, 2);
  f32x4* sl = slab + (size_t)tile * 8192;
  if (role == 0) {
    __amdgpu_buffer_rsrc_t rs = __builtin_amdgcn_make_buffer_rsrc((void*)sl, 0, 8192 * 16, 0x00020000);
#pragma unroll
    for (int ii = 0; ii < 2; ++ii)
#pragma unroll
      for (int j = 0; j < 4; ++j) {
        u32x4 v = __builtin_bit_cast(u32x4, mf[ii][j]);
        __builtin_amdgcn_raw_buffer_store_b128(v, rs, ((ii * 4 + j) * 512 + tid) * 16, 0, 16);
        if (kz) {
          u32x4 w = __builtin_bit_cast(u32x4, my[ii][j]);
          __builtin_amdgcn_raw_buffer_store_b128(w, rs, (4096 + (ii * 4 + j) * 512 + tid) * 16, 0, 16);
        }
      }
    asm volatile("s_waitcnt vmcnt(0)" ::: "memory");
    __syncthreads();
    if (tid == 0) __hip_atomic_store(&cnt[tile * 32 + 16], 1u, __ATOMIC_RELAXED, __HIP_MEMORY_SCOPE_AGENT);
    STAMP(1, 3);
    return;
  }
  u16x4 xv[2][4], xnv[2][4], dv[2];
#pragma unroll
  for (int ii = 0; ii < 2; ++ii) {
    int d = tn * 128 + wc * 64 + (kg * 2 + ii) * 16 + fq * 4;
    dv[ii] = *reinterpret_cast<const u16x4*>(dskip + d);
#pragma unroll
    for (int j = 0; j < 4; ++j) {
      int t = tm * 128 + wr * 64 + j * 16 + fr;
      xv[ii][j] = *reinterpret_cast<const u16x4*>(x + (size_t)t * D_ + d);
      xnv[ii][j] = *reinterpret_cast<const u16x4*>(xn + (size_t)t * D_ + d);
    }
  }
  if (tid == 0) {
    while (__hip_atomic_load(&cnt[tile * 32 + 16], __ATOMIC_RELAXED, __HIP_MEMORY_SCOPE_AGENT) == 0u) __builtin_amdgcn_s_sleep(1);
  }
  __syncthreads();
  __builtin_amdgcn_fence(__ATOMIC_ACQUIRE, "wavefront");
  STAMP(1, 3);
  {
    __amdgpu_buffer_rsrc_t rs = __builtin_amdgcn_make_buffer_rsrc((void*)sl, 0, 8192 * 16, 0x00020000);
#pragma unroll
    for (int ii = 0; ii < 2; ++ii)
#pragma unroll
      for (int j = 0; j < 4; ++j) {
        u32x4 v = __builtin_amdgcn_raw_buffer_load_b128(rs, ((ii * 4 + j) * 512 + tid) * 16, 0, 16);
        mf[ii][j] += __builtin_bit_cast(f32x4, v);
        if (!kz) {
          u32x4 w = __builtin_amdgcn_raw_buffer_load_b128(rs, (4096 + (ii * 4 + j) * 512 + tid) * 16, 0, 16);
          my[ii][j] = __builtin_bit_cast(f32x4, w);
        }
      }
  }
#pragma unroll
  for (int ii = 0; ii < 2; ++ii)
#pragma unroll
    for (int j = 0; j < 4; ++j) {
      int t = tm * 128 + wr * 64 + j * 16 + fr;
      int d = tn * 128 + wc * 64 + (kg * 2 + ii) * 16 + fq * 4;
      f32x4 o;
#pragma unroll
      for (int r = 0; r < 4; ++r) {
        float f = rbf(mf[ii][j][r]);
        float g = rbf(f * __builtin_amdgcn_rcpf(1.0f + __expf(-f)));
        float s = 2.0f * my[ii][j][r] + rbf(bf2f(xnv[ii][j][r]) * bf2f(dv[ii][r]));
        o[r] = bf2f(xv[ii][j][r]) + s * g;
      }
      *reinterpret_cast<f32x4*>(out + (size_t)t * D_ + d) = o;
    }
  STAMP(1, 4);
}

extern "C" void kernel_launch(void* const* d_in, const int* in_sizes, int n_in, void* d_out, int out_size, void* d_ws,
                              size_t ws_size, hipStream_t stream) {
  const unsigned short* x = (const unsigned short*)d_in[0];
  const unsigned short* cre = (const unsigned short*)d_in[5];
  const unsigned short* cim = (const unsigned short*)d_in[6];
  const unsigned short* dskip = (const unsigned short*)d_in[8];
  const unsigned short* wdown = (const unsigned short*)d_in[12];
  char* ws = (char*)d_ws;
  unsigned short* xn = (unsigned short*)(ws + OFF_XN);
  unsigned short* a2 = (unsigned short*)(ws + OFF_A2);
  float* out = (float*)d_out;
  G1Args ga;
  ga.xn = xn;
  ga.wgate = (const unsigned short*)d_in[10];
  ga.wup = (const unsigned short*)d_in[11];
  ga.bre = (const unsigned short*)d_in[3];
  ga.bim = (const unsigned short*)d_in[4];
  ga.lre = (const unsigned short*)d_in[1];
  ga.lim = (const unsigned short*)d_in[2];
  ga.logdt = (const unsigned short*)d_in[7];
  ga.a2 = a2;
  ga.agg = (unsigned long long*)(ws + OFF_AGG);

  k_prep<<<PB_LN, 256, 0, stream>>>(x, ws);
  (void)hipFuncSetAttribute((const void*)k_gemm1, hipFuncAttributeMaxDynamicSharedMemorySize, G1_LDS);
  k_gemm1<<<256, 512, G1_LDS, stream>>>(ga);
  (void)hipFuncSetAttribute((const void*)k_gemm2, hipFuncAttributeMaxDynamicSharedMemorySize, G2_LDS);
  k_gemm2<<<256, 512, G2_LDS, stream>>>(a2, wdown, cre, cim, x, xn, dskip, out, (f32x4*)(ws + OFF_SLAB), (unsigned*)(ws + OFF_CNT));
}
```

```cpp
#include <hip/hip_runtime.h>
#include <hip/hip_bf16.h>
#include <stdint.h>

typedef __attribute__((ext_vector_type(8))) short bf16x8;
typedef __attribute__((ext_vector_type(4))) float f32x4;
typedef __attribute__((ext_vector_type(4))) unsigned short u16x4;
typedef __attribute__((ext_vector_type(4))) unsigned u32x4;

constexpr int S_ = 2048;
constexpr int D_ = 1024;
constexpr int P_ = 1024;
constexpr int F_ = 3584;
constexpr int N1_ = 9216;
constexpr int K2_ = 5632;

constexpr size_t OFF_AGG = 0;
constexpr size_t OFF_CNT = OFF_AGG + 32 * 8 * 32 * 2 * 8;
constexpr size_t SYNC_BYTES = OFF_CNT + 128 * 32 * 4;
constexpr size_t OFF_XN = SYNC_BYTES;
constexpr size_t OFF_A2 = OFF_XN + (size_t)S_ * D_ * 2;
constexpr size_t OFF_SLAB = OFF_A2 + (size_t)S_ * K2_ * 2;

__device__ __forceinline__ float bf2f(unsigned short v) { return __uint_as_float(((unsigned)v) << 16); }
__device__ __forceinline__ unsigned short f2bf(float f) {
  __bf16 b = (__bf16)f;
  return __builtin_bit_cast(unsigned short, b);
}
typedef __attribute__((ext_vector_type(2))) __bf16 bf16x2n;
typedef __attribute__((ext_vector_type(2))) float f32x2;
__device__ __forceinline__ unsigned pk2bf(float a, float b) {
  f32x2 v = {a, b};
  bf16x2n r = __builtin_convertvector(v, bf16x2n);
  return __builtin_bit_cast(unsigned, r);
}
__device__ __forceinline__ float lo2f(unsigned u) { return __uint_as_float(u << 16); }
__device__ __forceinline__ float hi2f(unsigned u) { return __uint_as_float(u & 0xFFFF0000u); }
__device__ __forceinline__ float rbf(float f) { return bf2f(f2bf(f)); }
__device__ __forceinline__ float2 cmul(float2 a, float2 b) { return make_float2(a.x * b.x - a.y * b.y, a.x * b.y + a.y * b.x); }

#define STAMP(k, i)
#define CSTAMP(k, i)
#define WAIT_VM(n) asm volatile("s_waitcnt vmcnt(" #n ")" ::: "memory")
#define LDS_PTR(p) ((__attribute__((address_space(3))) unsigned*)(p))

constexpr int PB_LN = 512;
__global__ __launch_bounds__(256) void k_prep(const unsigned short* __restrict__ x, char* __restrict__ ws) {
  int b = blockIdx.x, tid = threadIdx.x, wave = tid >> 6, lane = tid & 63;
  unsigned short* xn = (unsigned short*)(ws + OFF_XN);
  for (int i = b * 256 + tid; i < (int)(SYNC_BYTES / 16); i += PB_LN * 256)
    reinterpret_cast<u32x4*>(ws)[i] = (u32x4){0u, 0u, 0u, 0u};
  int row = b * 4 + wave;
  const bf16x8* src = reinterpret_cast<const bf16x8*>(x + (size_t)row * D_);
  bf16x8 v0 = src[lane], v1 = src[64 + lane];
  float f[16];
#pragma unroll
  for (int j = 0; j < 8; ++j) { f[j] = bf2f((unsigned short)v0[j]); f[8 + j] = bf2f((unsigned short)v1[j]); }
  float s = 0.f;
#pragma unroll
  for (int j = 0; j < 16; ++j) s += f[j];
#pragma unroll
  for (int o = 32; o >= 1; o >>= 1) s += __shfl_xor(s, o);
  float mean = s * (1.0f / D_);
  float q = 0.f;
#pragma unroll
  for (int j = 0; j < 16; ++j) { float d = f[j] - mean; q += d * d; }
#pragma unroll
  for (int o = 32; o >= 1; o >>= 1) q += __shfl_xor(q, o);
  float var = q * (1.0f / D_);
  float mb = rbf(mean), vb = rbf(var);
  float den = rbf(sqrtf(rbf(vb + 1e-5f)));
  bf16x8 o0, o1;
#pragma unroll
  for (int j = 0; j < 8; ++j) {
    o0[j] = (short)f2bf(rbf(f[j] - mb) / den);
    o1[j] = (short)f2bf(rbf(f[8 + j] - mb) / den);
  }
  bf16x8* dst = reinterpret_cast<bf16x8*>(xn + (size_t)row * D_);
  dst[lane] = o0; dst[64 + lane] = o1;
}

typedef __attribute__((ext_vector_type(4))) short s16x4;
template <int OFF2>
__device__ __forceinline__ bf16x8 tr_frag(unsigned lds_addr) {
  s16x4 lo, hi;
  asm volatile("ds_read_b64_tr_b16 %0, %2\n\tds_read_b64_tr_b16 %1, %2 offset:%c3"
               : "=&v"(lo), "=&v"(hi) : "v"(lds_addr), "i"(OFF2));
  return __builtin_shufflevector(lo, hi, 0, 1, 2, 3, 4, 5, 6, 7);
}
#define TR_WAIT() do { asm volatile("s_waitcnt lgkmcnt(0)" ::: "memory"); __builtin_amdgcn_sched_barrier(0); } while (0)
__device__ __forceinline__ unsigned lds_u32(const void* p) {
  return (unsigned)(uintptr_t)(__attribute__((address_space(3))) const char*)p;
}

__device__ __forceinline__ float gelu_tanh(float x) {
  float w = x * __builtin_fmaf(x * x, -0.10294323970650479f, -2.3022081985952363f);
  return x * __builtin_amdgcn_rcpf(1.0f + __builtin_amdgcn_exp2f(w));
}

constexpr int G1_AT = 256 * 64;
constexpr int G1_WT = 288 * 64;
constexpr int G1_STAGE = G1_AT + G1_WT;
constexpr int G1_NSLOT = 4;
constexpr int G1_LDS = G1_NSLOT * G1_STAGE;
constexpr int E_BUL = 0;
constexpr int E_ACT = 69632;
constexpr int E_CAG = 131072;
constexpr int E_TERM = 135168;
constexpr int E_MISC = 137216;

struct G1Args {
  const unsigned short *xn, *wgate, *wup, *bre, *bim, *lre, *lim, *logdt;
  unsigned short* a2;
  unsigned long long* agg;
};
template <int V> struct IC { static constexpr int value = V; };

__global__ __launch_bounds__(512, 2) void k_gemm1(G1Args ga) {
  extern __shared__ __attribute__((aligned(16))) char smem[];
  int tid = threadIdx.x, wid = __builtin_amdgcn_readfirstlane(tid >> 6), lane = tid & 63;
  int xl = blockIdx.x & 7, loc = blockIdx.x >> 3;
  STAMP(0, 0);
  int tn = xl * 4 + (loc >> 3), tm = loc & 7;
  int wr = wid >> 1, wc = wid & 1;
  int fr = lane & 15, fq = lane >> 4;
  const unsigned short* xg = ga.xn + (size_t)tm * 256 * D_;
  unsigned short* a2 = ga.a2;
  unsigned long long* agg = ga.agg;

  f32x4 acc[9][4];
#pragma unroll
  for (int i = 0; i < 9; ++i)
#pragma unroll
    for (int j = 0; j < 4; ++j) acc[i][j] = (f32x4){0.f, 0.f, 0.f, 0.f};

  int srow = lane >> 2;
  int schunk = (lane & 3) ^ (((srow >> 3) & 1) << 1);
  const char* wsrc[3]; int wstr[3];
#pragma unroll
  for (int i = 0; i < 3; ++i) {
    int reg = (i < 2) ? wid * 2 + i : 16 + (wid & 1);
    const unsigned short* ptr; int str;
    if (reg < 12) {
      int Q = reg >> 2, kb = reg & 3;
      int r = lane >> 3, c = (lane & 7) ^ (((r >> 1) & 1) << 1) ^ ((kb & 1) << 2);
      int k = kb * 8 + r;
      const unsigned short* mat; int col;
      if (Q == 0) { mat = ga.wgate; col = c * 8; }
      else if (Q == 1) { mat = ga.wup; col = c * 8; }
      else if (c < 6) { mat = ga.wgate; col = 64 + c * 8; }
      else { mat = ga.wup; col = 64 + (c - 6) * 8; }
      ptr = mat + (size_t)k * F_ + tn * 112 + col; str = F_ * 64;
    } else if (reg < 14) {
      int r = lane >> 3, c = (lane & 7) ^ (((r >> 1) & 1) << 1);
      int k = ((reg - 12) * 2 + (c >> 2)) * 8 + r;
      ptr = ga.wup + (size_t)k * F_ + tn * 112 + 80 + (c & 3) * 8; str = F_ * 64;
    } else {
      int row = (reg - 14) * 16 + srow;
      const unsigned short* mat = (row < 32) ? ga.bre : ga.bim;
      ptr = mat + (size_t)(tn * 32 + (row & 31)) * D_ + schunk * 8; str = 64;
    }
    wsrc[i] = (const char*)ptr; wstr[i] = str;
  }
  auto stage = [&](int slot, int ks) {
    char* base = smem + slot * G1_STAGE;
#pragma unroll
    for (int i = 0; i < 2; ++i) {
      int ins = wid * 2 + i;
      __builtin_amdgcn_global_load_lds((const unsigned*)(xg + (size_t)(ins * 16 + srow) * D_ + ks * 32 + schunk * 8),
                                       LDS_PTR(base + ins * 1024), 16, 0, 0);
    }
#pragma unroll
    for (int i = 0; i < 2; ++i)
      __builtin_amdgcn_global_load_lds((const unsigned*)(wsrc[i] + (size_t)ks * wstr[i]), LDS_PTR(base + G1_AT + (wid * 2 + i) * 1024), 16, 0, 0);
    if (wid < 2)
      __builtin_amdgcn_global_load_lds((const unsigned*)(wsrc[2] + (size_t)ks * wstr[2]), LDS_PTR(base + G1_AT + (16 + wid) * 1024), 16, 0, 0);
  };

  const int ns = D_ / 32;
  stage(0, 0); stage(1, 1); stage(2, 2); stage(3, 3);
  int rchunk = fq ^ (((fr >> 3) & 1) << 1);
  const int xoff = (wr * 64 + fr) * 64 + rchunk * 16;
  const int boff = G1_AT + 14 * 1024 + fr * 64 + rchunk * 16;
  const unsigned sbase = lds_u32(smem);
  auto mainloop = [&](auto WCc) {
    constexpr int WC = decltype(WCc)::value;
    constexpr int NTR = WC ? 6 : 8;
    int kb = fq, q = (lane >> 2) & 3, pp = lane & 3;
    int sw = ((q >> 1) << 1) ^ ((kb & 1) << 2);
    int wo[NTR];
#pragma unroll
    for (int i = 0; i < NTR; ++i) {
      if (WC == 0) wo[i] = G1_AT + ((i >> 2) * 4 + kb) * 1024 + q * 128 + ((((i & 3) * 2 + (pp >> 1)) ^ sw) * 16) + (pp & 1) * 8;
      else if (i < 4) wo[i] = G1_AT + (8 + kb) * 1024 + q * 128 + (((i * 2 + (pp >> 1)) ^ sw) * 16) + (pp & 1) * 8;
      else wo[i] = G1_AT + (12 + (kb >> 1)) * 1024 + q * 128 + ((((kb & 1) * 4 + (i - 4) * 2 + (pp >> 1)) ^ ((q >> 1) << 1)) * 16) + (pp & 1) * 8;
    }
    unsigned wpk[NTR / 2];
#pragma unroll
    for (int i = 0; i < NTR / 2; ++i) wpk[i] = (unsigned)wo[2 * i] | ((unsigned)wo[2 * i + 1] << 16);
    auto ldw = [&](int i, const char* nb, unsigned nbu) -> bf16x8 {
      if (i < NTR) return tr_frag<512>(nbu + ((i & 1) ? (wpk[i >> 1] >> 16) : (wpk[i >> 1] & 0xFFFFu)));
      return *reinterpret_cast<const bf16x8*>(nb + boff + (WC ? (i - NTR + 1) : 0) * 1024);
    };
    bf16x8 xa_[4], xb_[4], wf[9];
    if (wid < 2) WAIT_VM(15); else WAIT_VM(12);
    __builtin_amdgcn_s_barrier();
#pragma unroll
    for (int j = 0; j < 4; ++j) xa_[j] = *reinterpret_cast<const bf16x8*>(smem + xoff + j * 1024);
#pragma unroll
    for (int i = 0; i < 9; ++i) wf[i] = ldw(i, smem, sbase);

    auto body = [&](int s, bf16x8 (&xc)[4], bf16x8 (&xnx)[4]) {
      if (s < ns - 3) { if (wid < 2) WAIT_VM(10); else WAIT_VM(8); }
      else if (s == ns - 3) { if (wid < 2) WAIT_VM(5); else WAIT_VM(4); }
      else WAIT_VM(0);
      __builtin_amdgcn_s_barrier();
      if (s + 4 < ns) stage(s & 3, s + 4);
      const char* nb = smem + ((s + 1) & 3) * G1_STAGE;
      unsigned nbu = sbase + ((s + 1) & 3) * G1_STAGE;
      TR_WAIT();
#pragma unroll
      for (int i = 0; i < 9; ++i) {
#pragma unroll
        for (int j = 0; j < 4; ++j) acc[i][j] = __builtin_amdgcn_mfma_f32_16x16x32_bf16(wf[i], xc[j], acc[i][j], 0, 0, 0);
        wf[i] = ldw(i, nb, nbu);
        if (i < 4) xnx[i] = *reinterpret_cast<const bf16x8*>(nb + xoff + i * 1024);
      }
#pragma unroll
      for (int i = 0; i < 9; ++i) {
        __builtin_amdgcn_sched_group_barrier(0x008, 4, 0);
        __builtin_amdgcn_sched_group_barrier(0x100, 1, 0);
      }
    };
    STAMP(0, 10); CSTAMP(0, 11);
    for (int s = 0; s < ns; s += 2) {
      body(s, xa_, xb_);
      body(s + 1, xb_, xa_);
    }
    STAMP(0, 12); CSTAMP(0, 13);
  };
  if (wc == 0) mainloop(IC<0>{}); else mainloop(IC<1>{});

  __syncthreads();
  STAMP(0, 1);
  float* bul = reinterpret_cast<float*>(smem + E_BUL);
  unsigned short* actl = reinterpret_cast<unsigned short*>(smem + E_ACT);
  float2* cag = reinterpret_cast<float2*>(smem + E_CAG);
  float2* term = reinterpret_cast<float2*>(smem + E_TERM);
  int tl = wr * 64 + fr;
  if (wc == 0) {
#pragma unroll
    for (int j = 0; j < 4; ++j) *reinterpret_cast<f32x4*>(bul + (tl + j * 16) * 68 + fq * 4) = acc[8][j];
  } else {
#pragma unroll
    for (int j = 0; j < 4; ++j) {
      *reinterpret_cast<f32x4*>(bul + (tl + j * 16) * 68 + 16 + fq * 4) = acc[6][j];
      *reinterpret_cast<f32x4*>(bul + (tl + j * 16) * 68 + 32 + fq * 4) = acc[7][j];
      *reinterpret_cast<f32x4*>(bul + (tl + j * 16) * 68 + 48 + fq * 4) = acc[8][j];
    }
  }
  __syncthreads();
  int ch = tid & 31, ck = tid >> 5;
  int p = tn * 32 + ch;
  float2 lam, cf;
  {
    float lr = bf2f(ga.lre[p]), li = bf2f(ga.lim[p]);
    float dt = rbf(expf(bf2f(ga.logdt[p])));
    float zr = dt * lr, zi = dt * li;
    float e = expf(zr);
    lam = make_float2(e * cosf(zi), e * sinf(zi));
    float nr = lam.x - 1.0f, ni = lam.y;
    float den = lr * lr + li * li;
    cf = make_float2((nr * lr + ni * li) / den, (ni * lr - nr * li) / den);
  }
  float hr[16], hi[16];
  {
    float sr = 0.f, si = 0.f;
#pragma unroll
    for (int i = 0; i < 16; ++i) {
      float rr = bul[(ck * 16 + i) * 68 + ch], ri = bul[(ck * 16 + i) * 68 + 32 + ch];
      float br = cf.x * rr - cf.y * ri, bi = cf.x * ri + cf.y * rr;
      float nr = lam.x * sr - lam.y * si + br;
      float ni = lam.x * si + lam.y * sr + bi;
      sr = nr; si = ni; hr[i] = sr; hi[i] = si;
    }
    cag[ck * 32 + ch] = make_float2(sr, si);
  }
  float2 l16 = lam;
#pragma unroll
  for (int i = 0; i < 4; ++i) l16 = cmul(l16, l16);
  float2 l256 = l16;
#pragma unroll
  for (int i = 0; i < 4; ++i) l256 = cmul(l256, l256);
  __syncthreads();
  if (tid < 32) {
    float2 c = make_float2(0.f, 0.f);
#pragma unroll
    for (int k = 0; k < 16; ++k) {
      float2 aa = cag[k * 32 + ch];
      cag[k * 32 + ch] = c;
      float2 t2 = cmul(l16, c);
      c = make_float2(t2.x + aa.x, t2.y + aa.y);
    }
    if (tm < 7) {
      unsigned long long* g = agg + ((size_t)(tn * 8 + tm) * 32 + ch) * 2;
      __hip_atomic_store(g, (1ull << 32) | (unsigned long long)__float_as_uint(c.x), __ATOMIC_RELAXED, __HIP_MEMORY_SCOPE_AGENT);
      __hip_atomic_store(g + 1, (1ull << 32) | (unsigned long long)__float_as_uint(c.y), __ATOMIC_RELAXED, __HIP_MEMORY_SCOPE_AGENT);
    }
  }
  STAMP(0, 2);
  auto geglu4 = [&](const f32x4& gt, const f32x4& up) -> uint2 {
    unsigned g01 = pk2bf(gt[0], gt[1]), g23 = pk2bf(gt[2], gt[3]);
    unsigned u01 = pk2bf(up[0], up[1]), u23 = pk2bf(up[2], up[3]);
    unsigned a01 = pk2bf(gelu_tanh(lo2f(g01)), gelu_tanh(hi2f(g01)));
    unsigned a23 = pk2bf(gelu_tanh(lo2f(g23)), gelu_tanh(hi2f(g23)));
    uint2 o;
    o.x = pk2bf(lo2f(a01) * lo2f(u01), hi2f(a01) * hi2f(u01));
    o.y = pk2bf(lo2f(a23) * lo2f(u23), hi2f(a23) * hi2f(u23));
    return o;
  };
  if (wc == 0) {
#pragma unroll
    for (int i = 0; i < 4; ++i)
#pragma unroll
      for (int j = 0; j < 4; ++j)
        *reinterpret_cast<uint2*>(actl + (tl + j * 16) * 120 + i * 16 + fq * 4) = geglu4(acc[i][j], acc[i + 4][j]);
  } else {
#pragma unroll
    for (int i = 0; i < 3; ++i)
#pragma unroll
      for (int j = 0; j < 4; ++j)
        *reinterpret_cast<uint2*>(actl + (tl + j * 16) * 120 + 64 + i * 16 + fq * 4) = geglu4(acc[i][j], acc[i + 3][j]);
  }
  __syncthreads();
  STAMP(0, 3);
#pragma unroll
  for (int it = 0; it < 7; ++it) {
    int idx = it * 512 + tid;
    int row = idx / 14, c = idx - row * 14;
    bf16x8 v = *reinterpret_cast<const bf16x8*>(actl + row * 120 + c * 8);
    *reinterpret_cast<bf16x8*>(a2 + (size_t)(tm * 256 + row) * K2_ + tn * 112 + c * 8) = v;
  }
  STAMP(0, 4);
  if (ck < tm) {
    const unsigned long long* g = agg + ((size_t)(tn * 8 + ck) * 32 + ch) * 2;
    unsigned long long v0, v1;
    for (;;) {
      v0 = __hip_atomic_load(g, __ATOMIC_RELAXED, __HIP_MEMORY_SCOPE_AGENT);
      v1 = __hip_atomic_load(g + 1, __ATOMIC_RELAXED, __HIP_MEMORY_SCOPE_AGENT);
      if ((v0 >> 32) == 1ull && (v1 >> 32) == 1ull) break;
      __builtin_amdgcn_s_sleep(1);
    }
    float2 av = make_float2(__uint_as_float((unsigned)v0), __uint_as_float((unsigned)v1));
    for (int e = tm - 1 - ck; e > 0; --e) av = cmul(av, l256);
    term[ck * 32 + ch] = av;
  }
  __syncthreads();
  STAMP(0, 5);
  float2 bc = make_float2(0.f, 0.f);
  for (int j = 0; j < tm; ++j) { float2 tv = term[j * 32 + ch]; bc.x += tv.x; bc.y += tv.y; }
  float2 pw = make_float2(1.f, 0.f);
  for (int e = 0; e < ck; ++e) pw = cmul(pw, l16);
  float2 c0 = cag[ck * 32 + ch];
  float2 t3 = cmul(pw, bc);
  c0.x += t3.x; c0.y += t3.y;
  unsigned short* hl = reinterpret_cast<unsigned short*>(smem + E_BUL);
  {
    float2 pq = lam;
#pragma unroll
    for (int i = 0; i < 16; ++i) {
      float vr = hr[i] + pq.x * c0.x - pq.y * c0.y;
      float vi = hi[i] + pq.x * c0.y + pq.y * c0.x;
      hl[(ck * 16 + i) * 72 + ch] = f2bf(vr);
      hl[(ck * 16 + i) * 72 + 32 + ch] = f2bf(-vi);
      pq = cmul(pq, lam);
    }
  }
  __syncthreads();
#pragma unroll
  for (int it = 0; it < 4; ++it) {
    int idx = it * 512 + tid;
    int row = idx >> 3, c = idx & 7;
    bf16x8 v = *reinterpret_cast<const bf16x8*>(hl + row * 72 + c * 8);
    *reinterpret_cast<bf16x8*>(a2 + (size_t)(tm * 256 + row) * K2_ + F_ + (c >> 2) * P_ + tn * 32 + (c & 3) * 8) = v;
  }
  STAMP(0, 6);
}

constexpr int G2_STAGE = 32768;
constexpr int G2_NSLOT = 4;
constexpr int G2_LDS = G2_NSLOT * G2_STAGE + 16;
__global__ __launch_bounds__(512, 2) void k_gemm2(const unsigned short* __restrict__ a2, const unsigned short* __restrict__ wdown,
                                                  const unsigned short* __restrict__ cre, const unsigned short* __restrict__ cim,
                                                  const unsigned short* __restrict__ x,
                                                  const unsigned short* __restrict__ xn, const unsigned short* __restrict__ dskip,
                                                  float* __restrict__ out,
                                                  f32x4* __restrict__ slab, unsigned* __restrict__ cnt) {
  extern __shared__ __attribute__((aligned(16))) char smem[];
  int bid = blockIdx.x;
  int xcd = bid & 7, loc = bid >> 3;
  int tm = (xcd >> 1) * 4 + ((loc >> 3) & 3), tn = (xcd & 1) * 4 + ((loc >> 1) & 3), kz = loc & 1;
  int tile = tm * 8 + tn;
  STAMP(1, 0);
  int tid = threadIdx.x, wid = __builtin_amdgcn_readfirstlane(tid >> 6), lane = tid & 63;
  int kg = wid >> 2, wr = (wid >> 1) & 1, wc = wid & 1, fr = lane & 15, fq = lane >> 4;
  const unsigned short* ag = a2 + (size_t)tm * 128 * K2_ + kz * 2560;
  f32x4 acc[4][4], accf[4][4];
#pragma unroll
  for (int i = 0; i < 4; ++i)
#pragma unroll
    for (int j = 0; j < 4; ++j) { acc[i][j] = (f32x4){0, 0, 0, 0}; accf[i][j] = (f32x4){0, 0, 0, 0}; }
  int srow = lane >> 3, schunk = (lane & 7) ^ (srow & 7);
  int wlofs[2];
#pragma unroll
  for (int i = 0; i < 2; ++i) {
    int reg = wid * 2 + i, Q = reg >> 3, kb = reg & 7;
    int r = lane >> 3, c = (lane & 7) ^ (((r >> 1) & 1) << 1) ^ ((kb & 1) << 2);
    wlofs[i] = (kb * 8 + r) * D_ + tn * 128 + Q * 64 + c * 8;
  }
  auto stage = [&](int slot, int kt) {
    char* base = smem + slot * G2_STAGE;
    int kabs = kz * 2560 + kt * 64;
    bool ffn = kabs < F_;
    const unsigned short* wb = ffn ? wdown + (size_t)kabs * D_ : ((kabs < F_ + P_) ? cre + (kabs - F_) : cim + (kabs - F_ - P_));
#pragma unroll
    for (int i = 0; i < 2; ++i) {
      int ins = wid * 2 + i;
      __builtin_amdgcn_global_load_lds((const unsigned*)(ag + (size_t)(ins * 8 + srow) * K2_ + kt * 64 + schunk * 8),
                                       LDS_PTR(base + ins * 1024), 16, 0, 0);
      __builtin_amdgcn_global_load_lds((const unsigned*)(wb + (ffn ? wlofs[i] : (tn * 128 + ins * 8 + srow) * P_ + schunk * 8)),
                                       LDS_PTR(base + 16384 + ins * 1024), 16, 0, 0);
    }
  };
  const int ns = kz ? 48 : 40;
  const int nsf = kz ? 16 : 40;
  stage(0, 0); stage(1, 1); stage(2, 2); stage(3, 3);
  int rch = ((kg * 4 + fq) ^ (fr & 7)) * 16;
  const int xoff = (wr * 64 + fr) * 128 + rch;
  const int woffc = 16384 + (wc * 64 + fr) * 128 + rch;
  int wofs[4];
  {
    int kb = kg * 4 + fq, q = (lane >> 2) & 3, pp = lane & 3;
    int sw = ((q >> 1) << 1) ^ ((kb & 1) << 2);
#pragma unroll
    for (int i = 0; i < 4; ++i) wofs[i] = 16384 + (wc * 8 + kb) * 1024 + q * 128 + (((i * 2 + (pp >> 1)) ^ sw) * 16) + (pp & 1) * 8;
  }
  bf16x8 xa_[4], xb_[4], wa_[4], wb_[4];
  const unsigned sbase = lds_u32(smem);
  WAIT_VM(12);
  __builtin_amdgcn_s_barrier();
#pragma unroll
  for (int j = 0; j < 4; ++j) {
    xa_[j] = *reinterpret_cast<const bf16x8*>(smem + xoff + j * 2048);
    wa_[j] = tr_frag<512>(sbase + wofs[j]);
  }
  auto body = [&](auto NTRc, int s, bf16x8 (&xc)[4], bf16x8 (&wcur)[4], bf16x8 (&xnx)[4], bf16x8 (&wnx)[4]) {
    constexpr int NTR = decltype(NTRc)::value;
    if (s < ns - 3) WAIT_VM(8);
    else if (s == ns - 3) WAIT_VM(4);
    else WAIT_VM(0);
    __builtin_amdgcn_s_barrier();
    if (s + 4 < ns) stage(s & 3, s + 4);
    const char* nb = smem + ((s + 1) & 3) * G2_STAGE;
    unsigned nbu = sbase + ((s + 1) & 3) * G2_STAGE;
    TR_WAIT();
#pragma unroll
    for (int i = 0; i < 4; ++i) {
#pragma unroll
      for (int j = 0; j < 4; ++j) acc[i][j] = __builtin_amdgcn_mfma_f32_16x16x32_bf16(wcur[i], xc[j], acc[i][j], 0, 0, 0);
      if (NTR) wnx[i] = tr_frag<512>(nbu + wofs[i]);
      else wnx[i] = *reinterpret_cast<const bf16x8*>(nb + woffc + i * 2048);
      xnx[i] = *reinterpret_cast<const bf16x8*>(nb + xoff + i * 2048);
    }
#pragma unroll
    for (int i = 0; i < 4; ++i) {
      __builtin_amdgcn_sched_group_barrier(0x008, 4, 0);
      __builtin_amdgcn_sched_group_barrier(0x100, 3, 0);
    }
    if (s + 1 == nsf) {
#pragma unroll
      for (int i = 0; i < 4; ++i)
#pragma unroll
        for (int j = 0; j < 4; ++j) { accf[i][j] = acc[i][j]; acc[i][j] = (f32x4){0, 0, 0, 0}; }
    }
  };
  STAMP(1, 10); CSTAMP(1, 11);
  for (int s = 0; s < nsf - 2; s += 2) {
    body(IC<1>{}, s, xa_, wa_, xb_, wb_);
    body(IC<1>{}, s + 1, xb_, wb_, xa_, wa_);
  }
  body(IC<1>{}, nsf - 2, xa_, wa_, xb_, wb_);
  body(IC<0>{}, nsf - 1, xb_, wb_, xa_, wa_);
  for (int s = nsf; s < ns; s += 2) {
    body(IC<0>{}, s, xa_, wa_, xb_, wb_);
    body(IC<0>{}, s + 1, xb_, wb_, xa_, wa_);
  }
  STAMP(1, 12); CSTAMP(1, 13);
  __syncthreads();
  STAMP(1, 1);
  f32x4* ex = reinterpret_cast<f32x4*>(smem);
  int wq = wid & 3;
#pragma unroll
  for (int ii = 0; ii < 2; ++ii)
#pragma unroll
    for (int j = 0; j < 4; ++j) {
      f32x4 sf = kg ? accf[ii][j] : accf[2 + ii][j];
      f32x4 sy = kg ? acc[ii][j] : acc[2 + ii][j];
      ex[(((0 * 2 + kg) * 4 + wq) * 8 + ii * 4 + j) * 64 + lane] = sf;
      if (kz) ex[(((1 * 2 + kg) * 4 + wq) * 8 + ii * 4 + j) * 64 + lane] = sy;
    }
  unsigned* role_s = reinterpret_cast<unsigned*>(smem + G2_NSLOT * G2_STAGE);
  if (tid == 0) *role_s = __hip_atomic_fetch_add(&cnt[tile * 32], 1u, __ATOMIC_RELAXED, __HIP_MEMORY_SCOPE_AGENT);
  __syncthreads();
  f32x4 mf[2][4], my[2][4];
#pragma unroll
  for (int ii = 0; ii < 2; ++ii)
#pragma unroll
    for (int j = 0; j < 4; ++j) {
      f32x4 ownf = kg ? accf[2 + ii][j] : accf[ii][j];
      f32x4 owny = kg ? acc[2 + ii][j] : acc[ii][j];
      mf[ii][j] = ownf + ex[(((0 * 2 + (kg ^ 1)) * 4 + wq) * 8 + ii * 4 + j) * 64 + lane];
      my[ii][j] = owny;
      if (kz) my[ii][j] += ex[(((1 * 2 + (kg ^ 1)) * 4 + wq) * 8 + ii * 4 + j) * 64 + lane];
    }
  unsigned role = *role_s;
  STAMP(1, 2);
  f32x4* sl = slab + (size_t)tile * 8192;
  if (role == 0) {
    __amdgpu_buffer_rsrc_t rs = __builtin_amdgcn_make_buffer_rsrc((void*)sl, 0, 8192 * 16, 0x00020000);
#pragma unroll
    for (int ii = 0; ii < 2; ++ii)
#pragma unroll
      for (int j = 0; j < 4; ++j) {
        u32x4 v = __builtin_bit_cast(u32x4, mf[ii][j]);
        __builtin_amdgcn_raw_buffer_store_b128(v, rs, ((ii * 4 + j) * 512 + tid) * 16, 0, 16);
        if (kz) {
          u32x4 w = __builtin_bit_cast(u32x4, my[ii][j]);
          __builtin_amdgcn_raw_buffer_store_b128(w, rs, (4096 + (ii * 4 + j) * 512 + tid) * 16, 0, 16);
        }
      }
    asm volatile("s_waitcnt vmcnt(0)" ::: "memory");
    __syncthreads();
    if (tid == 0) __hip_atomic_store(&cnt[tile * 32 + 16], 1u, __ATOMIC_RELAXED, __HIP_MEMORY_SCOPE_AGENT);
    STAMP(1, 3);
    return;
  }
  u16x4 xv[2][4], xnv[2][4], dv[2];
#pragma unroll
  for (int ii = 0; ii < 2; ++ii) {
    int d = tn * 128 + wc * 64 + (kg * 2 + ii) * 16 + fq * 4;
    dv[ii] = *reinterpret_cast<const u16x4*>(dskip + d);
#pragma unroll
    for (int j = 0; j < 4; ++j) {
      int t = tm * 128 + wr * 64 + j * 16 + fr;
      xv[ii][j] = *reinterpret_cast<const u16x4*>(x + (size_t)t * D_ + d);
      xnv[ii][j] = *reinterpret_cast<const u16x4*>(xn + (size_t)t * D_ + d);
    }
  }
  if (tid == 0) {
    while (__hip_atomic_load(&cnt[tile * 32 + 16], __ATOMIC_RELAXED, __HIP_MEMORY_SCOPE_AGENT) == 0u) __builtin_amdgcn_s_sleep(1);
  }
  __syncthreads();
  __builtin_amdgcn_fence(__ATOMIC_ACQUIRE, "wavefront");
  STAMP(1, 3);
  {
    __amdgpu_buffer_rsrc_t rs = __builtin_amdgcn_make_buffer_rsrc((void*)sl, 0, 8192 * 16, 0x00020000);
#pragma unroll
    for (int ii = 0; ii < 2; ++ii)
#pragma unroll
      for (int j = 0; j < 4; ++j) {
        u32x4 v = __builtin_amdgcn_raw_buffer_load_b128(rs, ((ii * 4 + j) * 512 + tid) * 16, 0, 16);
        mf[ii][j] += __builtin_bit_cast(f32x4, v);
        if (!kz) {
          u32x4 w = __builtin_amdgcn_raw_buffer_load_b128(rs, (4096 + (ii * 4 + j) * 512 + tid) * 16, 0, 16);
          my[ii][j] = __builtin_bit_cast(f32x4, w);
        }
      }
  }
#pragma unroll
  for (int ii = 0; ii < 2; ++ii)
#pragma unroll
    for (int j = 0; j < 4; ++j) {
      int t = tm * 128 + wr * 64 + j * 16 + fr;
      int d = tn * 128 + wc * 64 + (kg * 2 + ii) * 16 + fq * 4;
      f32x4 o;
#pragma unroll
      for (int r = 0; r < 4; ++r) {
        float f = rbf(mf[ii][j][r]);
        float g = rbf(f * __builtin_amdgcn_rcpf(1.0f + __expf(-f)));
        float s = 2.0f * my[ii][j][r] + rbf(bf2f(xnv[ii][j][r]) * bf2f(dv[ii][r]));
        o[r] = bf2f(xv[ii][j][r]) + s * g;
      }
      *reinterpret_cast<f32x4*>(out + (size_t)t * D_ + d) = o;
    }
  STAMP(1, 4);
}

extern "C" void kernel_launch(void* const* d_in, const int* in_sizes, int n_in, void* d_out, int out_size, void* d_ws,
                              size_t ws_size, hipStream_t stream) {
  const unsigned short* x = (const unsigned short*)d_in[0];
  const unsigned short* cre = (const unsigned short*)d_in[5];
  const unsigned short* cim = (const unsigned short*)d_in[6];
  const unsigned short* dskip = (const unsigned short*)d_in[8];
  const unsigned short* wdown = (const unsigned short*)d_in[12];
  char* ws = (char*)d_ws;
  unsigned short* xn = (unsigned short*)(ws + OFF_XN);
  unsigned short* a2 = (unsigned short*)(ws + OFF_A2);
  float* out = (float*)d_out;
  G1Args ga;
  ga.xn = xn;
  ga.wgate = (const unsigned short*)d_in[10];
  ga.wup = (const unsigned short*)d_in[11];
  ga.bre = (const unsigned short*)d_in[3];
  ga.bim = (const unsigned short*)d_in[4];
  ga.lre = (const unsigned short*)d_in[1];
  ga.lim = (const unsigned short*)d_in[2];
  ga.logdt = (const unsigned short*)d_in[7];
  ga.a2 = a2;
  ga.agg = (unsigned long long*)(ws + OFF_AGG);

  k_prep<<<PB_LN, 256, 0, stream>>>(x, ws);
  (void)hipFuncSetAttribute((const void*)k_gemm1, hipFuncAttributeMaxDynamicSharedMemorySize, G1_LDS);
  k_gemm1<<<256, 512, G1_LDS, stream>>>(ga);
  (void)hipFuncSetAttribute((const void*)k_gemm2, hipFuncAttributeMaxDynamicSharedMemorySize, G2_LDS);
  k_gemm2<<<256, 512, G2_LDS, stream>>>(a2, wdown, cre, cim, x, xn, dskip, out, (f32x4*)(ws + OFF_SLAB), (unsigned*)(ws + OFF_CNT));
}
```

```cpp
#include <hip/hip_runtime.h>
#include <hip/hip_bf16.h>
#include <stdint.h>

typedef __attribute__((ext_vector_type(8))) short bf16x8;
typedef __attribute__((ext_vector_type(4))) float f32x4;
typedef __attribute__((ext_vector_type(4))) unsigned short u16x4;
typedef __attribute__((ext_vector_type(4))) unsigned u32x4;

constexpr int S_ = 2048;
constexpr int D_ = 1024;
constexpr int P_ = 1024;
constexpr int F_ = 3584;
constexpr int N1_ = 9216;
constexpr int K2_ = 5632;

constexpr size_t OFF_AGG = 0;
constexpr size_t OFF_CNT = OFF_AGG + 32 * 8 * 32 * 2 * 8;
constexpr size_t SYNC_BYTES = OFF_CNT + 128 * 32 * 4;
constexpr size_t OFF_XN = SYNC_BYTES;
constexpr size_t OFF_A2 = OFF_XN + (size_t)S_ * D_ * 2;
constexpr size_t OFF_SLAB = OFF_A2 + (size_t)S_ * K2_ * 2;

__device__ __forceinline__ float bf2f(unsigned short v) { return __uint_as_float(((unsigned)v) << 16); }
__device__ __forceinline__ unsigned short f2bf(float f) {
  __bf16 b = (__bf16)f;
  return __builtin_bit_cast(unsigned short, b);
}
typedef __attribute__((ext_vector_type(2))) __bf16 bf16x2n;
typedef __attribute__((ext_vector_type(2))) float f32x2;
__device__ __forceinline__ unsigned pk2bf(float a, float b) {
  f32x2 v = {a, b};
  bf16x2n r = __builtin_convertvector(v, bf16x2n);
  return __builtin_bit_cast(unsigned, r);
}
__device__ __forceinline__ float lo2f(unsigned u) { return __uint_as_float(u << 16); }
__device__ __forceinline__ float hi2f(unsigned u) { return __uint_as_float(u & 0xFFFF0000u); }
__device__ __forceinline__ float rbf(float f) { return bf2f(f2bf(f)); }
__device__ __forceinline__ float2 cmul(float2 a, float2 b) { return make_float2(a.x * b.x - a.y * b.y, a.x * b.y + a.y * b.x); }

#define STAMP(k, i)
#define CSTAMP(k, i)
#define WAIT_VM(n) asm volatile("s_waitcnt vmcnt(" #n ")" ::: "memory")
#define LDS_PTR(p) ((__attribute__((address_space(3))) unsigned*)(p))

constexpr int PB_LN = 512;
#define PF_ALL 0
constexpr int PF_N = 6;
struct PrepArgs { const unsigned short *x, *wdown, *cre, *cim, *wgate, *wup, *bre, *bim; char* ws; };
__global__ __launch_bounds__(256) void k_prep(PrepArgs pa) {
  const unsigned short* __restrict__ x = pa.x; char* __restrict__ ws = pa.ws;
  int b = blockIdx.x, tid = threadIdx.x, wave = tid >> 6, lane = tid & 63;
  unsigned short* xn = (unsigned short*)(ws + OFF_XN);
  for (int i = b * 256 + tid; i < (int)(SYNC_BYTES / 16); i += PB_LN * 256)
    reinterpret_cast<u32x4*>(ws)[i] = (u32x4){0u, 0u, 0u, 0u};
  int row = b * 4 + wave;
  const bf16x8* src = reinterpret_cast<const bf16x8*>(x + (size_t)row * D_);
  bf16x8 v0 = src[lane], v1 = src[64 + lane];
  float f[16];
#pragma unroll
  for (int j = 0; j < 8; ++j) { f[j] = bf2f((unsigned short)v0[j]); f[8 + j] = bf2f((unsigned short)v1[j]); }
  float s = 0.f;
#pragma unroll
  for (int j = 0; j < 16; ++j) s += f[j];
#pragma unroll
  for (int o = 32; o >= 1; o >>= 1) s += __shfl_xor(s, o);
  float mean = s * (1.0f / D_);
  float q = 0.f;
#pragma unroll
  for (int j = 0; j < 16; ++j) { float d = f[j] - mean; q += d * d; }
#pragma unroll
  for (int o = 32; o >= 1; o >>= 1) q += __shfl_xor(q, o);
  float var = q * (1.0f / D_);
  float mb = rbf(mean), vb = rbf(var);
  float den = rbf(sqrtf(rbf(vb + 1e-5f)));
  bf16x8 o0, o1;
#pragma unroll
  for (int j = 0; j < 8; ++j) {
    o0[j] = (short)f2bf(rbf(f[j] - mb) / den);
    o1[j] = (short)f2bf(rbf(f[8 + j] - mb) / den);
  }
  bf16x8* dst = reinterpret_cast<bf16x8*>(xn + (size_t)row * D_);
  dst[lane] = o0; dst[64 + lane] = o1;
}

typedef __attribute__((ext_vector_type(4))) short s16x4;
template <int OFF2>
__device__ __forceinline__ bf16x8 tr_frag(unsigned lds_addr) {
  s16x4 lo, hi;
  asm volatile("ds_read_b64_tr_b16 %0, %2\n\tds_read_b64_tr_b16 %1, %2 offset:%c3"
               : "=&v"(lo), "=&v"(hi) : "v"(lds_addr), "i"(OFF2));
  return __builtin_shufflevector(lo, hi, 0, 1, 2, 3, 4, 5, 6, 7);
}
#define TR_WAIT() do { asm volatile("s_waitcnt lgkmcnt(0)" ::: "memory"); __builtin_amdgcn_sched_barrier(0); } while (0)
__device__ __forceinline__ unsigned lds_u32(const void* p) {
  return (unsigned)(uintptr_t)(__attribute__((address_space(3))) const char*)p;
}

__device__ __forceinline__ float gelu_tanh(float x) {
  float w = x * __builtin_fmaf(x * x, -0.10294323970650479f, -2.3022081985952363f);
  return x * __builtin_amdgcn_rcpf(1.0f + __builtin_amdgcn_exp2f(w));
}

constexpr int G1_AT = 256 * 64;
constexpr int G1_WT = 288 * 64;
constexpr int G1_STAGE = G1_AT + G1_WT;
constexpr int G1_NSLOT = 4;
constexpr int G1_DUMMY = G1_NSLOT * G1_STAGE;
constexpr int G1_LDS = G1_DUMMY + 1024;
constexpr int E_BUL = 0;
constexpr int E_ACT = 69632;
constexpr int E_CAG = 131072;
constexpr int E_TERM = 135168;
constexpr int E_MISC = 137216;

struct G1Args {
  const unsigned short *xn, *wgate, *wup, *bre, *bim, *lre, *lim, *logdt, *wdown, *cre, *cim;
  unsigned short* a2;
  unsigned long long* agg;
};
template <int V> struct IC { static constexpr int value = V; };

__global__ __launch_bounds__(512, 2) void k_gemm1(G1Args ga) {
  extern __shared__ __attribute__((aligned(16))) char smem[];
  int tid = threadIdx.x, wid = __builtin_amdgcn_readfirstlane(tid >> 6), lane = tid & 63;
  int xl = blockIdx.x & 7, loc = blockIdx.x >> 3;
  STAMP(0, 0);
  int tn = xl * 4 + (loc >> 3), tm = loc & 7;
  int wr = wid >> 1, wc = wid & 1;
  int fr = lane & 15, fq = lane >> 4;
  const unsigned short* xg = ga.xn + (size_t)tm * 256 * D_;
  unsigned short* a2 = ga.a2;
  unsigned long long* agg = ga.agg;

  f32x4 acc[9][4];
#pragma unroll
  for (int i = 0; i < 9; ++i)
#pragma unroll
    for (int j = 0; j < 4; ++j) acc[i][j] = (f32x4){0.f, 0.f, 0.f, 0.f};

  int srow = lane >> 2;
  int schunk = (lane & 3) ^ (((srow >> 3) & 1) << 1);
  const char* wsrc[3]; int wstr[3];
#pragma unroll
  for (int i = 0; i < 3; ++i) {
    int reg = (i < 2) ? wid * 2 + i : 16 + (wid & 1);
    const unsigned short* ptr; int str;
    if (reg < 12) {
      int Q = reg >> 2, kb = reg & 3;
      int r = lane >> 3, c = (lane & 7) ^ (((r >> 1) & 1) << 1) ^ ((kb & 1) << 2);
      int k = kb * 8 + r;
      const unsigned short* mat; int col;
      if (Q == 0) { mat = ga.wgate; col = c * 8; }
      else if (Q == 1) { mat = ga.wup; col = c * 8; }
      else if (c < 6) { mat = ga.wgate; col = 64 + c * 8; }
      else { mat = ga.wup; col = 64 + (c - 6) * 8; }
      ptr = mat + (size_t)k * F_ + tn * 112 + col; str = F_ * 64;
    } else if (reg < 14) {
      int r = lane >> 3, c = (lane & 7) ^ (((r >> 1) & 1) << 1);
      int k = ((reg - 12) * 2 + (c >> 2)) * 8 + r;
      ptr = ga.wup + (size_t)k * F_ + tn * 112 + 80 + (c & 3) * 8; str = F_ * 64;
    } else {
      int row = (reg - 14) * 16 + srow;
      const unsigned short* mat = (row < 32) ? ga.bre : ga.bim;
      ptr = mat + (size_t)(tn * 32 + (row & 31)) * D_ + schunk * 8; str = 64;
    }
    wsrc[i] = (const char*)ptr; wstr[i] = str;
  }
  auto stage = [&](int slot, int ks) {
    char* base = smem + slot * G1_STAGE;
#pragma unroll
    for (int i = 0; i < 2; ++i) {
      int ins = wid * 2 + i;
      __builtin_amdgcn_global_load_lds((const unsigned*)(xg + (size_t)(ins * 16 + srow) * D_ + ks * 32 + schunk * 8),
                                       LDS_PTR(base + ins * 1024), 16, 0, 0);
    }
#pragma unroll
    for (int i = 0; i < 2; ++i)
      __builtin_amdgcn_global_load_lds((const unsigned*)(wsrc[i] + (size_t)ks * wstr[i]), LDS_PTR(base + G1_AT + (wid * 2 + i) * 1024), 16, 0, 0);
    if (wid < 2)
      __builtin_amdgcn_global_load_lds((const unsigned*)(wsrc[2] + (size_t)ks * wstr[2]), LDS_PTR(base + G1_AT + (16 + wid) * 1024), 16, 0, 0);
  };

  const int ns = D_ / 32;
  stage(0, 0); stage(1, 1); stage(2, 2); stage(3, 3);
  int rchunk = fq ^ (((fr >> 3) & 1) << 1);
  const int xoff = (wr * 64 + fr) * 64 + rchunk * 16;
  const int boff = G1_AT + 14 * 1024 + fr * 64 + rchunk * 16;
  const unsigned sbase = lds_u32(smem);
  auto mainloop = [&](auto WCc) {
    constexpr int WC = decltype(WCc)::value;
    constexpr int NTR = WC ? 6 : 8;
    int kb = fq, q = (lane >> 2) & 3, pp = lane & 3;
    int sw = ((q >> 1) << 1) ^ ((kb & 1) << 2);
    int wo[NTR];
#pragma unroll
    for (int i = 0; i < NTR; ++i) {
      if (WC == 0) wo[i] = G1_AT + ((i >> 2) * 4 + kb) * 1024 + q * 128 + ((((i & 3) * 2 + (pp >> 1)) ^ sw) * 16) + (pp & 1) * 8;
      else if (i < 4) wo[i] = G1_AT + (8 + kb) * 1024 + q * 128 + (((i * 2 + (pp >> 1)) ^ sw) * 16) + (pp & 1) * 8;
      else wo[i] = G1_AT + (12 + (kb >> 1)) * 1024 + q * 128 + ((((kb & 1) * 4 + (i - 4) * 2 + (pp >> 1)) ^ ((q >> 1) << 1)) * 16) + (pp & 1) * 8;
    }
    unsigned wpk[NTR / 2];
#pragma unroll
    for (int i = 0; i < NTR / 2; ++i) wpk[i] = (unsigned)wo[2 * i] | ((unsigned)wo[2 * i + 1] << 16);
    auto ldw = [&](int i, const char* nb, unsigned nbu) -> bf16x8 {
      if (i < NTR) return tr_frag<512>(nbu + ((i & 1) ? (wpk[i >> 1] >> 16) : (wpk[i >> 1] & 0xFFFFu)));
      return *reinterpret_cast<const bf16x8*>(nb + boff + (WC ? (i - NTR + 1) : 0) * 1024);
    };
    bf16x8 xa_[4], xb_[4], wf[9];
    if (wid < 2) WAIT_VM(15); else WAIT_VM(12);
    __builtin_amdgcn_s_barrier();
#pragma unroll
    for (int j = 0; j < 4; ++j) xa_[j] = *reinterpret_cast<const bf16x8*>(smem + xoff + j * 1024);
#pragma unroll
    for (int i = 0; i < 9; ++i) wf[i] = ldw(i, smem, sbase);

    auto body = [&](int s, bf16x8 (&xc)[4], bf16x8 (&xnx)[4]) {
      if (s < ns - 3) { if (wid < 2) WAIT_VM(10); else WAIT_VM(8); }
      else if (s == ns - 3) { if (wid < 2) WAIT_VM(5); else WAIT_VM(4); }
      else WAIT_VM(0);
      __builtin_amdgcn_s_barrier();
      if (s + 4 < ns) stage(s & 3, s + 4);
      const char* nb = smem + ((s + 1) & 3) * G1_STAGE;
      unsigned nbu = sbase + ((s + 1) & 3) * G1_STAGE;
      TR_WAIT();
#pragma unroll
      for (int i = 0; i < 9; ++i) {
#pragma unroll
        for (int j = 0; j < 4; ++j) acc[i][j] = __builtin_amdgcn_mfma_f32_16x16x32_bf16(wf[i], xc[j], acc[i][j], 0, 0, 0);
        wf[i] = ldw(i, nb, nbu);
        if (i < 4) xnx[i] = *reinterpret_cast<const bf16x8*>(nb + xoff + i * 1024);
      }
#pragma unroll
      for (int i = 0; i < 9; ++i) {
        __builtin_amdgcn_sched_group_barrier(0x008, 4, 0);
        __builtin_amdgcn_sched_group_barrier(0x100, 1, 0);
      }
    };
    STAMP(0, 10); CSTAMP(0, 11);
    for (int s = 0; s < ns; s += 2) {
      body(s, xa_, xb_);
      body(s + 1, xb_, xa_);
    }
    STAMP(0, 12); CSTAMP(0, 13);
  };
  if (wc == 0) mainloop(IC<0>{}); else mainloop(IC<1>{});

  __syncthreads();
  STAMP(0, 1);
  float* bul = reinterpret_cast<float*>(smem + E_BUL);
  unsigned short* actl = reinterpret_cast<unsigned short*>(smem + E_ACT);
  float2* cag = reinterpret_cast<float2*>(smem + E_CAG);
  float2* term = reinterpret_cast<float2*>(smem + E_TERM);
  int tl = wr * 64 + fr;
  if (wc == 0) {
#pragma unroll
    for (int j = 0; j < 4; ++j) *reinterpret_cast<f32x4*>(bul + (tl + j * 16) * 68 + fq * 4) = acc[8][j];
  } else {
#pragma unroll
    for (int j = 0; j < 4; ++j) {
      *reinterpret_cast<f32x4*>(bul + (tl + j * 16) * 68 + 16 + fq * 4) = acc[6][j];
      *reinterpret_cast<f32x4*>(bul + (tl + j * 16) * 68 + 32 + fq * 4) = acc[7][j];
      *reinterpret_cast<f32x4*>(bul + (tl + j * 16) * 68 + 48 + fq * 4) = acc[8][j];
    }
  }
  __syncthreads();
  int ch = tid & 31, ck = tid >> 5;
  int p = tn * 32 + ch;
  float2 lam, cf;
  {
    float lr = bf2f(ga.lre[p]), li = bf2f(ga.lim[p]);
    float dt = rbf(expf(bf2f(ga.logdt[p])));
    float zr = dt * lr, zi = dt * li;
    float e = expf(zr);
    lam = make_float2(e * cosf(zi), e * sinf(zi));
    float nr = lam.x - 1.0f, ni = lam.y;
    float den = lr * lr + li * li;
    cf = make_float2((nr * lr + ni * li) / den, (ni * lr - nr * li) / den);
  }
  float hr[16], hi[16];
  {
    float sr = 0.f, si = 0.f;
#pragma unroll
    for (int i = 0; i < 16; ++i) {
      float rr = bul[(ck * 16 + i) * 68 + ch], ri = bul[(ck * 16 + i) * 68 + 32 + ch];
      float br = cf.x * rr - cf.y * ri, bi = cf.x * ri + cf.y * rr;
      float nr = lam.x * sr - lam.y * si + br;
      float ni = lam.x * si + lam.y * sr + bi;
      sr = nr; si = ni; hr[i] = sr; hi[i] = si;
    }
    cag[ck * 32 + ch] = make_float2(sr, si);
  }
  float2 l16 = lam;
#pragma unroll
  for (int i = 0; i < 4; ++i) l16 = cmul(l16, l16);
  float2 l256 = l16;
#pragma unroll
  for (int i = 0; i < 4; ++i) l256 = cmul(l256, l256);
  __syncthreads();
  if (tid < 32) {
    float2 c = make_float2(0.f, 0.f);
#pragma unroll
    for (int k = 0; k < 16; ++k) {
      float2 aa = cag[k * 32 + ch];
      cag[k * 32 + ch] = c;
      float2 t2 = cmul(l16, c);
      c = make_float2(t2.x + aa.x, t2.y + aa.y);
    }
    if (tm < 7) {
      unsigned long long* g = agg + ((size_t)(tn * 8 + tm) * 32 + ch) * 2;
      __hip_atomic_store(g, (1ull << 32) | (unsigned long long)__float_as_uint(c.x), __ATOMIC_RELAXED, __HIP_MEMORY_SCOPE_AGENT);
      __hip_atomic_store(g + 1, (1ull << 32) | (unsigned long long)__float_as_uint(c.y), __ATOMIC_RELAXED, __HIP_MEMORY_SCOPE_AGENT);
    }
  }
  STAMP(0, 2);
  auto geglu4 = [&](const f32x4& gt, const f32x4& up) -> uint2 {
    unsigned g01 = pk2bf(gt[0], gt[1]), g23 = pk2bf(gt[2], gt[3]);
    unsigned u01 = pk2bf(up[0], up[1]), u23 = pk2bf(up[2], up[3]);
    unsigned a01 = pk2bf(gelu_tanh(lo2f(g01)), gelu_tanh(hi2f(g01)));
    unsigned a23 = pk2bf(gelu_tanh(lo2f(g23)), gelu_tanh(hi2f(g23)));
    uint2 o;
    o.x = pk2bf(lo2f(a01) * lo2f(u01), hi2f(a01) * hi2f(u01));
    o.y = pk2bf(lo2f(a23) * lo2f(u23), hi2f(a23) * hi2f(u23));
    return o;
  };
  if (wc == 0) {
#pragma unroll
    for (int i = 0; i < 4; ++i)
#pragma unroll
      for (int j = 0; j < 4; ++j)
        *reinterpret_cast<uint2*>(actl + (tl + j * 16) * 120 + i * 16 + fq * 4) = geglu4(acc[i][j], acc[i + 4][j]);
  } else {
#pragma unroll
    for (int i = 0; i < 3; ++i)
#pragma unroll
      for (int j = 0; j < 4; ++j)
        *reinterpret_cast<uint2*>(actl + (tl + j * 16) * 120 + 64 + i * 16 + fq * 4) = geglu4(acc[i][j], acc[i + 3][j]);
  }
  __syncthreads();
  STAMP(0, 3);
#pragma unroll
  for (int it = 0; it < 7; ++it) {
    int idx = it * 512 + tid;
    int row = idx / 14, c = idx - row * 14;
    bf16x8 v = *reinterpret_cast<const bf16x8*>(actl + row * 120 + c * 8);
    *reinterpret_cast<bf16x8*>(a2 + (size_t)(tm * 256 + row) * K2_ + tn * 112 + c * 8) = v;
  }
  STAMP(0, 4);
#pragma unroll
  for (int i = 0; i < 6; ++i) {
    int g = min((blockIdx.x * 8 + wid) * 6 + i, 11263);
    const unsigned short* src = (g < 7168) ? ga.wdown + (size_t)g * 512 : (g < 9216 ? ga.cre + (size_t)(g - 7168) * 512 : ga.cim + (size_t)(g - 9216) * 512);
    __builtin_amdgcn_global_load_lds((const unsigned*)(src + lane * 8), LDS_PTR(smem + G1_DUMMY), 16, 0, 0);
  }
  if (ck < tm) {
    const unsigned long long* g = agg + ((size_t)(tn * 8 + ck) * 32 + ch) * 2;
    unsigned long long v0, v1;
    for (;;) {
      v0 = __hip_atomic_load(g, __ATOMIC_RELAXED, __HIP_MEMORY_SCOPE_AGENT);
      v1 = __hip_atomic_load(g + 1, __ATOMIC_RELAXED, __HIP_MEMORY_SCOPE_AGENT);
      if ((v0 >> 32) == 1ull && (v1 >> 32) == 1ull) break;
      __builtin_amdgcn_s_sleep(1);
    }
    float2 av = make_float2(__uint_as_float((unsigned)v0), __uint_as_float((unsigned)v1));
    for (int e = tm - 1 - ck; e > 0; --e) av = cmul(av, l256);
    term[ck * 32 + ch] = av;
  }
  __syncthreads();
  STAMP(0, 5);
  float2 bc = make_float2(0.f, 0.f);
  for (int j = 0; j < tm; ++j) { float2 tv = term[j * 32 + ch]; bc.x += tv.x; bc.y += tv.y; }
  float2 pw = make_float2(1.f, 0.f);
  for (int e = 0; e < ck; ++e) pw = cmul(pw, l16);
  float2 c0 = cag[ck * 32 + ch];
  float2 t3 = cmul(pw, bc);
  c0.x += t3.x; c0.y += t3.y;
  unsigned short* hl = reinterpret_cast<unsigned short*>(smem + E_BUL);
  {
    float2 pq = lam;
#pragma unroll
    for (int i = 0; i < 16; ++i) {
      float vr = hr[i] + pq.x * c0.x - pq.y * c0.y;
      float vi = hi[i] + pq.x * c0.y + pq.y * c0.x;
      hl[(ck * 16 + i) * 72 + ch] = f2bf(vr);
      hl[(ck * 16 + i) * 72 + 32 + ch] = f2bf(-vi);
      pq = cmul(pq, lam);
    }
  }
  __syncthreads();
#pragma unroll
  for (int it = 0; it < 4; ++it) {
    int idx = it * 512 + tid;
    int row = idx >> 3, c = idx & 7;
    bf16x8 v = *reinterpret_cast<const bf16x8*>(hl + row * 72 + c * 8);
    *reinterpret_cast<bf16x8*>(a2 + (size_t)(tm * 256 + row) * K2_ + F_ + (c >> 2) * P_ + tn * 32 + (c & 3) * 8) = v;
  }
  STAMP(0, 6);
}

constexpr int G2_STAGE = 32768;
constexpr int G2_NSLOT = 4;
constexpr int G2_LDS = G2_NSLOT * G2_STAGE + 16;
__global__ __launch_bounds__(512, 2) void k_gemm2(const unsigned short* __restrict__ a2, const unsigned short* __restrict__ wdown,
                                                  const unsigned short* __restrict__ cre, const unsigned short* __restrict__ cim,
                                                  const unsigned short* __restrict__ x,
                                                  const unsigned short* __restrict__ xn, const unsigned short* __restrict__ dskip,
                                                  float* __restrict__ out,
                                                  f32x4* __restrict__ slab, unsigned* __restrict__ cnt) {
  extern __shared__ __attribute__((aligned(16))) char smem[];
  int bid = blockIdx.x;
  int xcd = bid & 7, loc = bid >> 3;
  int tm = (xcd >> 1) * 4 + ((loc >> 3) & 3), tn = (xcd & 1) * 4 + ((loc >> 1) & 3), kz = loc & 1;
  int tile = tm * 8 + tn;
  STAMP(1, 0);
  int tid = threadIdx.x, wid = __builtin_amdgcn_readfirstlane(tid >> 6), lane = tid & 63;
  int kg = wid >> 2, wr = (wid >> 1) & 1, wc = wid & 1, fr = lane & 15, fq = lane >> 4;
  const unsigned short* ag = a2 + (size_t)tm * 128 * K2_ + kz * 2560;
  f32x4 acc[4][4], accf[4][4];
#pragma unroll
  for (int i = 0; i < 4; ++i)
#pragma unroll
    for (int j = 0; j < 4; ++j) { acc[i][j] = (f32x4){0, 0, 0, 0}; accf[i][j] = (f32x4){0, 0, 0, 0}; }
  int srow = lane >> 3, schunk = (lane & 7) ^ (srow & 7);
  int wlofs[2];
#pragma unroll
  for (int i = 0; i < 2; ++i) {
    int reg = wid * 2 + i, Q = reg >> 3, kb = reg & 7;
    int r = lane >> 3, c = (lane & 7) ^ (((r >> 1) & 1) << 1) ^ ((kb & 1) << 2);
    wlofs[i] = (kb * 8 + r) * D_ + tn * 128 + Q * 64 + c * 8;
  }
  auto stage = [&](int slot, int kt) {
    char* base = smem + slot * G2_STAGE;
    int kabs = kz * 2560 + kt * 64;
    bool ffn = kabs < F_;
    const unsigned short* wb = ffn ? wdown + (size_t)kabs * D_ : ((kabs < F_ + P_) ? cre + (kabs - F_) : cim + (kabs - F_ - P_));
#pragma unroll
    for (int i = 0; i < 2; ++i) {
      int ins = wid * 2 + i;
      __builtin_amdgcn_global_load_lds((const unsigned*)(ag + (size_t)(ins * 8 + srow) * K2_ + kt * 64 + schunk * 8),
                                       LDS_PTR(base + ins * 1024), 16, 0, 0);
      __builtin_amdgcn_global_load_lds((const unsigned*)(wb + (ffn ? wlofs[i] : (tn * 128 + ins * 8 + srow) * P_ + schunk * 8)),
                                       LDS_PTR(base + 16384 + ins * 1024), 16, 0, 0);
    }
  };
  const int ns = kz ? 48 : 40;
  const int nsf = kz ? 16 : 40;
  stage(0, 0); stage(1, 1); stage(2, 2); stage(3, 3);
  int rch = ((kg * 4 + fq) ^ (fr & 7)) * 16;
  const int xoff = (wr * 64 + fr) * 128 + rch;
  const int woffc = 16384 + (wc * 64 + fr) * 128 + rch;
  int wofs[4];
  {
    int kb = kg * 4 + fq, q = (lane >> 2) & 3, pp = lane & 3;
    int sw = ((q >> 1) << 1) ^ ((kb & 1) << 2);
#pragma unroll
    for (int i = 0; i < 4; ++i) wofs[i] = 16384 + (wc * 8 + kb) * 1024 + q * 128 + (((i * 2 + (pp >> 1)) ^ sw) * 16) + (pp & 1) * 8;
  }
  bf16x8 xa_[4], xb_[4], wa_[4], wb_[4];
  const unsigned sbase = lds_u32(smem);
  WAIT_VM(12);
  __builtin_amdgcn_s_barrier();
#pragma unroll
  for (int j = 0; j < 4; ++j) {
    xa_[j] = *reinterpret_cast<const bf16x8*>(smem + xoff + j * 2048);
    wa_[j] = tr_frag<512>(sbase + wofs[j]);
  }
  auto body = [&](auto NTRc, int s, bf16x8 (&xc)[4], bf16x8 (&wcur)[4], bf16x8 (&xnx)[4], bf16x8 (&wnx)[4]) {
    constexpr int NTR = decltype(NTRc)::value;
    if (s < ns - 3) WAIT_VM(8);
    else if (s == ns - 3) WAIT_VM(4);
    else WAIT_VM(0);
    __builtin_amdgcn_s_barrier();
    if (s + 4 < ns) stage(s & 3, s + 4);
    const char* nb = smem + ((s + 1) & 3) * G2_STAGE;
    unsigned nbu = sbase + ((s + 1) & 3) * G2_STAGE;
    TR_WAIT();
#pragma unroll
    for (int i = 0; i < 4; ++i) {
#pragma unroll
      for (int j = 0; j < 4; ++j) acc[i][j] = __builtin_amdgcn_mfma_f32_16x16x32_bf16(wcur[i], xc[j], acc[i][j], 0, 0, 0);
      if (NTR) wnx[i] = tr_frag<512>(nbu + wofs[i]);
      else wnx[i] = *reinterpret_cast<const bf16x8*>(nb + woffc + i * 2048);
      xnx[i] = *reinterpret_cast<const bf16x8*>(nb + xoff + i * 2048);
    }
#pragma unroll
    for (int i = 0; i < 4; ++i) {
      __builtin_amdgcn_sched_group_barrier(0x008, 4, 0);
      __builtin_amdgcn_sched_group_barrier(0x100, 3, 0);
    }
    if (s + 1 == nsf) {
#pragma unroll
      for (int i = 0; i < 4; ++i)
#pragma unroll
        for (int j = 0; j < 4; ++j) { accf[i][j] = acc[i][j]; acc[i][j] = (f32x4){0, 0, 0, 0}; }
    }
  };
  STAMP(1, 10); CSTAMP(1, 11);
  for (int s = 0; s < nsf - 2; s += 2) {
    body(IC<1>{}, s, xa_, wa_, xb_, wb_);
    body(IC<1>{}, s + 1, xb_, wb_, xa_, wa_);
  }
  body(IC<1>{}, nsf - 2, xa_, wa_, xb_, wb_);
  body(IC<0>{}, nsf - 1, xb_, wb_, xa_, wa_);
  for (int s = nsf; s < ns; s += 2) {
    body(IC<0>{}, s, xa_, wa_, xb_, wb_);
    body(IC<0>{}, s + 1, xb_, wb_, xa_, wa_);
  }
  STAMP(1, 12); CSTAMP(1, 13);
  __syncthreads();
  STAMP(1, 1);
  f32x4* ex = reinterpret_cast<f32x4*>(smem);
  int wq = wid & 3;
#pragma unroll
  for (int ii = 0; ii < 2; ++ii)
#pragma unroll
    for (int j = 0; j < 4; ++j) {
      f32x4 sf = kg ? accf[ii][j] : accf[2 + ii][j];
      f32x4 sy = kg ? acc[ii][j] : acc[2 + ii][j];
      ex[(((0 * 2 + kg) * 4 + wq) * 8 + ii * 4 + j) * 64 + lane] = sf;
      if (kz) ex[(((1 * 2 + kg) * 4 + wq) * 8 + ii * 4 + j) * 64 + lane] = sy;
    }
  unsigned* role_s = reinterpret_cast<unsigned*>(smem + G2_NSLOT * G2_STAGE);
  if (tid == 0) *role_s = __hip_atomic_fetch_add(&cnt[tile * 32], 1u, __ATOMIC_RELAXED, __HIP_MEMORY_SCOPE_AGENT);
  __syncthreads();
  f32x4 mf[2][4], my[2][4];
#pragma unroll
  for (int ii = 0; ii < 2; ++ii)
#pragma unroll
    for (int j = 0; j < 4; ++j) {
      f32x4 ownf = kg ? accf[2 + ii][j] : accf[ii][j];
      f32x4 owny = kg ? acc[2 + ii][j] : acc[ii][j];
      mf[ii][j] = ownf + ex[(((0 * 2 + (kg ^ 1)) * 4 + wq) * 8 + ii * 4 + j) * 64 + lane];
      my[ii][j] = owny;
      if (kz) my[ii][j] += ex[(((1 * 2 + (kg ^ 1)) * 4 + wq) * 8 + ii * 4 + j) * 64 + lane];
    }
  unsigned role = *role_s;
  STAMP(1, 2);
  f32x4* sl = slab + (size_t)tile * 8192;
  if (role == 0) {
    __amdgpu_buffer_rsrc_t rs = __builtin_amdgcn_make_buffer_rsrc((void*)sl, 0, 8192 * 16, 0x00020000);
#pragma unroll
    for (int ii = 0; ii < 2; ++ii)
#pragma unroll
      for (int j = 0; j < 4; ++j) {
        u32x4 v = __builtin_bit_cast(u32x4, mf[ii][j]);
        __builtin_amdgcn_raw_buffer_store_b128(v, rs, ((ii * 4 + j) * 512 + tid) * 16, 0, 16);
        if (kz) {
          u32x4 w = __builtin_bit_cast(u32x4, my[ii][j]);
          __builtin_amdgcn_raw_buffer_store_b128(w, rs, (4096 + (ii * 4 + j) * 512 + tid) * 16, 0, 16);
        }
      }
    asm volatile("s_waitcnt vmcnt(0)" ::: "memory");
    __syncthreads();
    if (tid == 0) __hip_atomic_store(&cnt[tile * 32 + 16], 1u, __ATOMIC_RELAXED, __HIP_MEMORY_SCOPE_AGENT);
    STAMP(1, 3);
    return;
  }
  u16x4 xv[2][4], xnv[2][4], dv[2];
#pragma unroll
  for (int ii = 0; ii < 2; ++ii) {
    int d = tn * 128 + wc * 64 + (kg * 2 + ii) * 16 + fq * 4;
    dv[ii] = *reinterpret_cast<const u16x4*>(dskip + d);
#pragma unroll
    for (int j = 0; j < 4; ++j) {
      int t = tm * 128 + wr * 64 + j * 16 + fr;
      xv[ii][j] = *reinterpret_cast<const u16x4*>(x + (size_t)t * D_ + d);
      xnv[ii][j] = *reinterpret_cast<const u16x4*>(xn + (size_t)t * D_ + d);
    }
  }
  if (tid == 0) {
    while (__hip_atomic_load(&cnt[tile * 32 + 16], __ATOMIC_RELAXED, __HIP_MEMORY_SCOPE_AGENT) == 0u) __builtin_amdgcn_s_sleep(1);
  }
  __syncthreads();
  __builtin_amdgcn_fence(__ATOMIC_ACQUIRE, "wavefront");
  STAMP(1, 3);
  {
    __amdgpu_buffer_rsrc_t rs = __builtin_amdgcn_make_buffer_rsrc((void*)sl, 0, 8192 * 16, 0x00020000);
#pragma unroll
    for (int ii = 0; ii < 2; ++ii)
#pragma unroll
      for (int j = 0; j < 4; ++j) {
        u32x4 v = __builtin_amdgcn_raw_buffer_load_b128(rs, ((ii * 4 + j) * 512 + tid) * 16, 0, 16);
        mf[ii][j] += __builtin_bit_cast(f32x4, v);
        if (!kz) {
          u32x4 w = __builtin_amdgcn_raw_buffer_load_b128(rs, (4096 + (ii * 4 + j) * 512 + tid) * 16, 0, 16);
          my[ii][j] = __builtin_bit_cast(f32x4, w);
        }
      }
  }
#pragma unroll
  for (int ii = 0; ii < 2; ++ii)
#pragma unroll
    for (int j = 0; j < 4; ++j) {
      int t = tm * 128 + wr * 64 + j * 16 + fr;
      int d = tn * 128 + wc * 64 + (kg * 2 + ii) * 16 + fq * 4;
      f32x4 o;
#pragma unroll
      for (int r = 0; r < 4; ++r) {
        float f = rbf(mf[ii][j][r]);
        float g = rbf(f * __builtin_amdgcn_rcpf(1.0f + __expf(-f)));
        float s = 2.0f * my[ii][j][r] + rbf(bf2f(xnv[ii][j][r]) * bf2f(dv[ii][r]));
        o[r] = bf2f(xv[ii][j][r]) + s * g;
      }
      *reinterpret_cast<f32x4*>(out + (size_t)t * D_ + d) = o;
    }
  STAMP(1, 4);
}

extern "C" void kernel_launch(void* const* d_in, const int* in_sizes, int n_in, void* d_out, int out_size, void* d_ws,
                              size_t ws_size, hipStream_t stream) {
  const unsigned short* x = (const unsigned short*)d_in[0];
  const unsigned short* cre = (const unsigned short*)d_in[5];
  const unsigned short* cim = (const unsigned short*)d_in[6];
  const unsigned short* dskip = (const unsigned short*)d_in[8];
  const unsigned short* wdown = (const unsigned short*)d_in[12];
  char* ws = (char*)d_ws;
  unsigned short* xn = (unsigned short*)(ws + OFF_XN);
  unsigned short* a2 = (unsigned short*)(ws + OFF_A2);
  float* out = (float*)d_out;
  G1Args ga;
  ga.xn = xn;
  ga.wgate = (const unsigned short*)d_in[10];
  ga.wup = (const unsigned short*)d_in[11];
  ga.bre = (const unsigned short*)d_in[3];
  ga.bim = (const unsigned short*)d_in[4];
  ga.lre = (const unsigned short*)d_in[1];
  ga.lim = (const unsigned short*)d_in[2];
  ga.logdt = (const unsigned short*)d_in[7];
  ga.wdown = wdown; ga.cre = cre; ga.cim = cim;
  ga.a2 = a2;
  ga.agg = (unsigned long long*)(ws + OFF_AGG);

  PrepArgs pa; pa.x = x; pa.wdown = wdown; pa.cre = cre; pa.cim = cim; pa.wgate = ga.wgate; pa.wup = ga.wup; pa.bre = ga.bre; pa.bim = ga.bim; pa.ws = ws;
  k_prep<<<PB_LN, 256, 0, stream>>>(pa);
  (void)hipFuncSetAttribute((const void*)k_gemm1, hipFuncAttributeMaxDynamicSharedMemorySize, G1_LDS);
  k_gemm1<<<256, 512, G1_LDS, stream>>>(ga);
  (void)hipFuncSetAttribute((const void*)k_gemm2, hipFuncAttributeMaxDynamicSharedMemorySize, G2_LDS);
  k_gemm2<<<256, 512, G2_LDS, stream>>>(a2, wdown, cre, cim, x, xn, dskip, out, (f32x4*)(ws + OFF_SLAB), (unsigned*)(ws + OFF_CNT));
}
```

```cpp
#include <hip/hip_runtime.h>
#include <hip/hip_bf16.h>
#include <stdint.h>

typedef __attribute__((ext_vector_type(8))) short bf16x8;
typedef __attribute__((ext_vector_type(4))) float f32x4;
typedef __attribute__((ext_vector_type(4))) unsigned short u16x4;
typedef __attribute__((ext_vector_type(4))) unsigned u32x4;

constexpr int S_ = 2048;
constexpr int D_ = 1024;
constexpr int P_ = 1024;
constexpr int F_ = 3584;
constexpr int N1_ = 9216;
constexpr int K2_ = 5632;

constexpr size_t OFF_AGG = 0;
constexpr size_t OFF_CNT = OFF_AGG + 32 * 8 * 32 * 2 * 8;
constexpr size_t SYNC_BYTES = OFF_CNT + 128 * 32 * 4;
constexpr size_t OFF_XN = SYNC_BYTES;
constexpr size_t OFF_A2 = OFF_XN + (size_t)S_ * D_ * 2;
constexpr size_t OFF_SLAB = OFF_A2 + (size_t)S_ * K2_ * 2;

__device__ __forceinline__ float bf2f(unsigned short v) { return __uint_as_float(((unsigned)v) << 16); }
__device__ __forceinline__ unsigned short f2bf(float f) {
  __bf16 b = (__bf16)f;
  return __builtin_bit_cast(unsigned short, b);
}
typedef __attribute__((ext_vector_type(2))) __bf16 bf16x2n;
typedef __attribute__((ext_vector_type(2))) float f32x2;
__device__ __forceinline__ unsigned pk2bf(float a, float b) {
  f32x2 v = {a, b};
  bf16x2n r = __builtin_convertvector(v, bf16x2n);
  return __builtin_bit_cast(unsigned, r);
}
__device__ __forceinline__ float lo2f(unsigned u) { return __uint_as_float(u << 16); }
__device__ __forceinline__ float hi2f(unsigned u) { return __uint_as_float(u & 0xFFFF0000u); }
__device__ __forceinline__ float rbf(float f) { return bf2f(f2bf(f)); }
__device__ __forceinline__ float2 cmul(float2 a, float2 b) { return make_float2(a.x * b.x - a.y * b.y, a.x * b.y + a.y * b.x); }

#define STAMP(k, i)
#define CSTAMP(k, i)
#define WAIT_VM(n) asm volatile("s_waitcnt vmcnt(" #n ")" ::: "memory")
#define LDS_PTR(p) ((__attribute__((address_space(3))) unsigned*)(p))

constexpr int PB_LN = 512;
#define PF_ALL 0
constexpr int PF_N = 6;
struct PrepArgs { const unsigned short *x, *wdown, *cre, *cim, *wgate, *wup, *bre, *bim; char* ws; };
__global__ __launch_bounds__(256) void k_prep(PrepArgs pa) {
  const unsigned short* __restrict__ x = pa.x; char* __restrict__ ws = pa.ws;
  int b = blockIdx.x, tid = threadIdx.x, wave = tid >> 6, lane = tid & 63;
  unsigned short* xn = (unsigned short*)(ws + OFF_XN);
  for (int i = b * 256 + tid; i < (int)(SYNC_BYTES / 16); i += PB_LN * 256)
    reinterpret_cast<u32x4*>(ws)[i] = (u32x4){0u, 0u, 0u, 0u};
  int row = b * 4 + wave;
  const bf16x8* src = reinterpret_cast<const bf16x8*>(x + (size_t)row * D_);
  bf16x8 v0 = src[lane], v1 = src[64 + lane];
  float f[16];
#pragma unroll
  for (int j = 0; j < 8; ++j) { f[j] = bf2f((unsigned short)v0[j]); f[8 + j] = bf2f((unsigned short)v1[j]); }
  float s = 0.f;
#pragma unroll
  for (int j = 0; j < 16; ++j) s += f[j];
#pragma unroll
  for (int o = 32; o >= 1; o >>= 1) s += __shfl_xor(s, o);
  float mean = s * (1.0f / D_);
  float q = 0.f;
#pragma unroll
  for (int j = 0; j < 16; ++j) { float d = f[j] - mean; q += d * d; }
#pragma unroll
  for (int o = 32; o >= 1; o >>= 1) q += __shfl_xor(q, o);
  float var = q * (1.0f / D_);
  float mb = rbf(mean), vb = rbf(var);
  float den = rbf(sqrtf(rbf(vb + 1e-5f)));
  bf16x8 o0, o1;
#pragma unroll
  for (int j = 0; j < 8; ++j) {
    o0[j] = (short)f2bf(rbf(f[j] - mb) / den);
    o1[j] = (short)f2bf(rbf(f[8 + j] - mb) / den);
  }
  bf16x8* dst = reinterpret_cast<bf16x8*>(xn + (size_t)row * D_);
  dst[lane] = o0; dst[64 + lane] = o1;
}

typedef __attribute__((ext_vector_type(4))) short s16x4;
template <int OFF2>
__device__ __forceinline__ bf16x8 tr_frag(unsigned lds_addr) {
  s16x4 lo, hi;
  asm volatile("ds_read_b64_tr_b16 %0, %2\n\tds_read_b64_tr_b16 %1, %2 offset:%c3"
               : "=&v"(lo), "=&v"(hi) : "v"(lds_addr), "i"(OFF2));
  return __builtin_shufflevector(lo, hi, 0, 1, 2, 3, 4, 5, 6, 7);
}
#define TR_WAIT() do { asm volatile("s_waitcnt lgkmcnt(0)" ::: "memory"); __builtin_amdgcn_sched_barrier(0); } while (0)
__device__ __forceinline__ unsigned lds_u32(const void* p) {
  return (unsigned)(uintptr_t)(__attribute__((address_space(3))) const char*)p;
}

__device__ __forceinline__ float gelu_tanh(float x) {
  float w = x * __builtin_fmaf(x * x, -0.10294323970650479f, -2.3022081985952363f);
  return x * __builtin_amdgcn_rcpf(1.0f + __builtin_amdgcn_exp2f(w));
}

constexpr int G1_AT = 256 * 64;
constexpr int G1_WT = 288 * 64;
constexpr int G1_STAGE = G1_AT + G1_WT;
constexpr int G1_NSLOT = 4;
constexpr int G1_DUMMY = G1_NSLOT * G1_STAGE;
constexpr int G1_LDS = G1_DUMMY + 1024;
constexpr int E_BUL = 0;
constexpr int E_ACT = 69632;
constexpr int E_CAG = 131072;
constexpr int E_TERM = 135168;
constexpr int E_MISC = 137216;

struct G1Args {
  const unsigned short *xn, *wgate, *wup, *bre, *bim, *lre, *lim, *logdt, *wdown, *cre, *cim;
  unsigned short* a2;
  unsigned long long* agg;
};
template <int V> struct IC { static constexpr int value = V; };

__global__ __launch_bounds__(512, 2) void k_gemm1(G1Args ga) {
  extern __shared__ __attribute__((aligned(16))) char smem[];
  int tid = threadIdx.x, wid = __builtin_amdgcn_readfirstlane(tid >> 6), lane = tid & 63;
  int xl = blockIdx.x & 7, loc = blockIdx.x >> 3;
  STAMP(0, 0);
  int tn = xl * 4 + (loc >> 3), tm = loc & 7;
  int wr = wid >> 1, wc = wid & 1;
  int fr = lane & 15, fq = lane >> 4;
  const unsigned short* xg = ga.xn + (size_t)tm * 256 * D_;
  unsigned short* a2 = ga.a2;
  unsigned long long* agg = ga.agg;

  f32x4 acc[9][4];
#pragma unroll
  for (int i = 0; i < 9; ++i)
#pragma unroll
    for (int j = 0; j < 4; ++j) acc[i][j] = (f32x4){0.f, 0.f, 0.f, 0.f};

  int srow = lane >> 2;
  int schunk = (lane & 3) ^ (((srow >> 3) & 1) << 1);
  const char* wsrc[3]; int wstr[3];
#pragma unroll
  for (int i = 0; i < 3; ++i) {
    int reg = (i < 2) ? wid * 2 + i : 16 + (wid & 1);
    const unsigned short* ptr; int str;
    if (reg < 12) {
      int Q = reg >> 2, kb = reg & 3;
      int r = lane >> 3, c = (lane & 7) ^ (((r >> 1) & 1) << 1) ^ ((kb & 1) << 2);
      int k = kb * 8 + r;
      const unsigned short* mat; int col;
      if (Q == 0) { mat = ga.wgate; col = c * 8; }
      else if (Q == 1) { mat = ga.wup; col = c * 8; }
      else if (c < 6) { mat = ga.wgate; col = 64 + c * 8; }
      else { mat = ga.wup; col = 64 + (c - 6) * 8; }
      ptr = mat + (size_t)k * F_ + tn * 112 + col; str = F_ * 64;
    } else if (reg < 14) {
      int r = lane >> 3, c = (lane & 7) ^ (((r >> 1) & 1) << 1);
      int k = ((reg - 12) * 2 + (c >> 2)) * 8 + r;
      ptr = ga.wup + (size_t)k * F_ + tn * 112 + 80 + (c & 3) * 8; str = F_ * 64;
    } else {
      int row = (reg - 14) * 16 + srow;
      const unsigned short* mat = (row < 32) ? ga.bre : ga.bim;
      ptr = mat + (size_t)(tn * 32 + (row & 31)) * D_ + schunk * 8; str = 64;
    }
    wsrc[i] = (const char*)ptr; wstr[i] = str;
  }
  auto stage = [&](int slot, int ks) {
    char* base = smem + slot * G1_STAGE;
#pragma unroll
    for (int i = 0; i < 2; ++i) {
      int ins = wid * 2 + i;
      __builtin_amdgcn_global_load_lds((const unsigned*)(xg + (size_t)(ins * 16 + srow) * D_ + ks * 32 + schunk * 8),
                                       LDS_PTR(base + ins * 1024), 16, 0, 0);
    }
#pragma unroll
    for (int i = 0; i < 2; ++i)
      __builtin_amdgcn_global_load_lds((const unsigned*)(wsrc[i] + (size_t)ks * wstr[i]), LDS_PTR(base + G1_AT + (wid * 2 + i) * 1024), 16, 0, 0);
    if (wid < 2)
      __builtin_amdgcn_global_load_lds((const unsigned*)(wsrc[2] + (size_t)ks * wstr[2]), LDS_PTR(base + G1_AT + (16 + wid) * 1024), 16, 0, 0);
  };

  auto stage_piece = [&](int slot, int ks, int pc) {
    char* base = smem + slot * G1_STAGE;
    if (pc < 2) {
      int ins = wid * 2 + pc;
      __builtin_amdgcn_global_load_lds((const unsigned*)(xg + (size_t)(ins * 16 + srow) * D_ + ks * 32 + schunk * 8),
                                       LDS_PTR(base + ins * 1024), 16, 0, 0);
    } else if (pc < 4) {
      int i = pc - 2;
      __builtin_amdgcn_global_load_lds((const unsigned*)(wsrc[i] + (size_t)ks * wstr[i]), LDS_PTR(base + G1_AT + (wid * 2 + i) * 1024), 16, 0, 0);
    } else if (wid < 2) {
      __builtin_amdgcn_global_load_lds((const unsigned*)(wsrc[2] + (size_t)ks * wstr[2]), LDS_PTR(base + G1_AT + (16 + wid) * 1024), 16, 0, 0);
    }
  };
  const int ns = D_ / 32;
  stage(0, 0); stage(1, 1); stage(2, 2); stage(3, 3);
  int rchunk = fq ^ (((fr >> 3) & 1) << 1);
  const int xoff = (wr * 64 + fr) * 64 + rchunk * 16;
  const int boff = G1_AT + 14 * 1024 + fr * 64 + rchunk * 16;
  const unsigned sbase = lds_u32(smem);
  auto mainloop = [&](auto WCc) {
    constexpr int WC = decltype(WCc)::value;
    constexpr int NTR = WC ? 6 : 8;
    int kb = fq, q = (lane >> 2) & 3, pp = lane & 3;
    int sw = ((q >> 1) << 1) ^ ((kb & 1) << 2);
    int wo[NTR];
#pragma unroll
    for (int i = 0; i < NTR; ++i) {
      if (WC == 0) wo[i] = G1_AT + ((i >> 2) * 4 + kb) * 1024 + q * 128 + ((((i & 3) * 2 + (pp >> 1)) ^ sw) * 16) + (pp & 1) * 8;
      else if (i < 4) wo[i] = G1_AT + (8 + kb) * 1024 + q * 128 + (((i * 2 + (pp >> 1)) ^ sw) * 16) + (pp & 1) * 8;
      else wo[i] = G1_AT + (12 + (kb >> 1)) * 1024 + q * 128 + ((((kb & 1) * 4 + (i - 4) * 2 + (pp >> 1)) ^ ((q >> 1) << 1)) * 16) + (pp & 1) * 8;
    }
    unsigned wpk[NTR / 2];
#pragma unroll
    for (int i = 0; i < NTR / 2; ++i) wpk[i] = (unsigned)wo[2 * i] | ((unsigned)wo[2 * i + 1] << 16);
    auto ldw = [&](int i, const char* nb, unsigned nbu) -> bf16x8 {
      if (i < NTR) return tr_frag<512>(nbu + ((i & 1) ? (wpk[i >> 1] >> 16) : (wpk[i >> 1] & 0xFFFFu)));
      return *reinterpret_cast<const bf16x8*>(nb + boff + (WC ? (i - NTR + 1) : 0) * 1024);
    };
    bf16x8 xa_[4], xb_[4], wf[9];
    if (wid < 2) WAIT_VM(15); else WAIT_VM(12);
    __builtin_amdgcn_s_barrier();
#pragma unroll
    for (int j = 0; j < 4; ++j) xa_[j] = *reinterpret_cast<const bf16x8*>(smem + xoff + j * 1024);
#pragma unroll
    for (int i = 0; i < 9; ++i) wf[i] = ldw(i, smem, sbase);

    auto body = [&](int s, bf16x8 (&xc)[4], bf16x8 (&xnx)[4]) {
      if (wid < 2) WAIT_VM(10); else WAIT_VM(8);
      __builtin_amdgcn_s_barrier();
      const char* nb = smem + ((s + 1) & 3) * G1_STAGE;
      unsigned nbu = sbase + ((s + 1) & 3) * G1_STAGE;
      int ksn = min(s + 4, ns - 1);
      TR_WAIT();
#pragma unroll
      for (int i = 0; i < 9; ++i) {
#pragma unroll
        for (int j = 0; j < 4; ++j) acc[i][j] = __builtin_amdgcn_mfma_f32_16x16x32_bf16(wf[i], xc[j], acc[i][j], 0, 0, 0);
        wf[i] = ldw(i, nb, nbu);
        if (i < 4) xnx[i] = *reinterpret_cast<const bf16x8*>(nb + xoff + i * 1024);
        if (i >= 1 && i <= 5) stage_piece(s & 3, ksn, i - 1);
      }
#pragma unroll
      for (int i = 0; i < 9; ++i) {
        __builtin_amdgcn_sched_group_barrier(0x008, 4, 0);
        __builtin_amdgcn_sched_group_barrier(0x100, 1, 0);
      }
    };
    STAMP(0, 10); CSTAMP(0, 11);
    for (int s = 0; s < ns; s += 2) {
      body(s, xa_, xb_);
      body(s + 1, xb_, xa_);
    }
    STAMP(0, 12); CSTAMP(0, 13);
  };
  if (wc == 0) mainloop(IC<0>{}); else mainloop(IC<1>{});

  WAIT_VM(0);
  __syncthreads();
  STAMP(0, 1);
  float* bul = reinterpret_cast<float*>(smem + E_BUL);
  unsigned short* actl = reinterpret_cast<unsigned short*>(smem + E_ACT);
  float2* cag = reinterpret_cast<float2*>(smem + E_CAG);
  float2* term = reinterpret_cast<float2*>(smem + E_TERM);
  int tl = wr * 64 + fr;
  if (wc == 0) {
#pragma unroll
    for (int j = 0; j < 4; ++j) *reinterpret_cast<f32x4*>(bul + (tl + j * 16) * 68 + fq * 4) = acc[8][j];
  } else {
#pragma unroll
    for (int j = 0; j < 4; ++j) {
      *reinterpret_cast<f32x4*>(bul + (tl + j * 16) * 68 + 16 + fq * 4) = acc[6][j];
      *reinterpret_cast<f32x4*>(bul + (tl + j * 16) * 68 + 32 + fq * 4) = acc[7][j];
      *reinterpret_cast<f32x4*>(bul + (tl + j * 16) * 68 + 48 + fq * 4) = acc[8][j];
    }
  }
  __syncthreads();
  int ch = tid & 31, ck = tid >> 5;
  int p = tn * 32 + ch;
  float2 lam, cf;
  {
    float lr = bf2f(ga.lre[p]), li = bf2f(ga.lim[p]);
    float dt = rbf(expf(bf2f(ga.logdt[p])));
    float zr = dt * lr, zi = dt * li;
    float e = expf(zr);
    lam = make_float2(e * cosf(zi), e * sinf(zi));
    float nr = lam.x - 1.0f, ni = lam.y;
    float den = lr * lr + li * li;
    cf = make_float2((nr * lr + ni * li) / den, (ni * lr - nr * li) / den);
  }
  float hr[16], hi[16];
  {
    float sr = 0.f, si = 0.f;
#pragma unroll
    for (int i = 0; i < 16; ++i) {
      float rr = bul[(ck * 16 + i) * 68 + ch], ri = bul[(ck * 16 + i) * 68 + 32 + ch];
      float br = cf.x * rr - cf.y * ri, bi = cf.x * ri + cf.y * rr;
      float nr = lam.x * sr - lam.y * si + br;
      float ni = lam.x * si + lam.y * sr + bi;
      sr = nr; si = ni; hr[i] = sr; hi[i] = si;
    }
    cag[ck * 32 + ch] = make_float2(sr, si);
  }
  float2 l16 = lam;
#pragma unroll
  for (int i = 0; i < 4; ++i) l16 = cmul(l16, l16);
  float2 l256 = l16;
#pragma unroll
  for (int i = 0; i < 4; ++i) l256 = cmul(l256, l256);
  __syncthreads();
  if (tid < 32) {
    float2 c = make_float2(0.f, 0.f);
#pragma unroll
    for (int k = 0; k < 16; ++k) {
      float2 aa = cag[k * 32 + ch];
      cag[k * 32 + ch] = c;
      float2 t2 = cmul(l16, c);
      c = make_float2(t2.x + aa.x, t2.y + aa.y);
    }
    if (tm < 7) {
      unsigned long long* g = agg + ((size_t)(tn * 8 + tm) * 32 + ch) * 2;
      __hip_atomic_store(g, (1ull << 32) | (unsigned long long)__float_as_uint(c.x), __ATOMIC_RELAXED, __HIP_MEMORY_SCOPE_AGENT);
      __hip_atomic_store(g + 1, (1ull << 32) | (unsigned long long)__float_as_uint(c.y), __ATOMIC_RELAXED, __HIP_MEMORY_SCOPE_AGENT);
    }
  }
  STAMP(0, 2);
  auto geglu4 = [&](const f32x4& gt, const f32x4& up) -> uint2 {
    unsigned g01 = pk2bf(gt[0], gt[1]), g23 = pk2bf(gt[2], gt[3]);
    unsigned u01 = pk2bf(up[0], up[1]), u23 = pk2bf(up[2], up[3]);
    unsigned a01 = pk2bf(gelu_tanh(lo2f(g01)), gelu_tanh(hi2f(g01)));
    unsigned a23 = pk2bf(gelu_tanh(lo2f(g23)), gelu_tanh(hi2f(g23)));
    uint2 o;
    o.x = pk2bf(lo2f(a01) * lo2f(u01), hi2f(a01) * hi2f(u01));
    o.y = pk2bf(lo2f(a23) * lo2f(u23), hi2f(a23) * hi2f(u23));
    return o;
  };
  if (wc == 0) {
#pragma unroll
    for (int i = 0; i < 4; ++i)
#pragma unroll
      for (int j = 0; j < 4; ++j)
        *reinterpret_cast<uint2*>(actl + (tl + j * 16) * 120 + i * 16 + fq * 4) = geglu4(acc[i][j], acc[i + 4][j]);
  } else {
#pragma unroll
    for (int i = 0; i < 3; ++i)
#pragma unroll
      for (int j = 0; j < 4; ++j)
        *reinterpret_cast<uint2*>(actl + (tl + j * 16) * 120 + 64 + i * 16 + fq * 4) = geglu4(acc[i][j], acc[i + 3][j]);
  }
  __syncthreads();
  STAMP(0, 3);
#pragma unroll
  for (int it = 0; it < 7; ++it) {
    int idx = it * 512 + tid;
    int row = idx / 14, c = idx - row * 14;
    bf16x8 v = *reinterpret_cast<const bf16x8*>(actl + row * 120 + c * 8);
    *reinterpret_cast<bf16x8*>(a2 + (size_t)(tm * 256 + row) * K2_ + tn * 112 + c * 8) = v;
  }
  STAMP(0, 4);
#pragma unroll
  for (int i = 0; i < 6; ++i) {
    int g = min((blockIdx.x * 8 + wid) * 6 + i, 11263);
    const unsigned short* src = (g < 7168) ? ga.wdown + (size_t)g * 512 : (g < 9216 ? ga.cre + (size_t)(g - 7168) * 512 : ga.cim + (size_t)(g - 9216) * 512);
    __builtin_amdgcn_global_load_lds((const unsigned*)(src + lane * 8), LDS_PTR(smem + G1_DUMMY), 16, 0, 0);
  }
  if (ck < tm) {
    const unsigned long long* g = agg + ((size_t)(tn * 8 + ck) * 32 + ch) * 2;
    unsigned long long v0, v1;
    for (;;) {
      v0 = __hip_atomic_load(g, __ATOMIC_RELAXED, __HIP_MEMORY_SCOPE_AGENT);
      v1 = __hip_atomic_load(g + 1, __ATOMIC_RELAXED, __HIP_MEMORY_SCOPE_AGENT);
      if ((v0 >> 32) == 1ull && (v1 >> 32) == 1ull) break;
      __builtin_amdgcn_s_sleep(1);
    }
    float2 av = make_float2(__uint_as_float((unsigned)v0), __uint_as_float((unsigned)v1));
    for (int e = tm - 1 - ck; e > 0; --e) av = cmul(av, l256);
    term[ck * 32 + ch] = av;
  }
  __syncthreads();
  STAMP(0, 5);
  float2 bc = make_float2(0.f, 0.f);
  for (int j = 0; j < tm; ++j) { float2 tv = term[j * 32 + ch]; bc.x += tv.x; bc.y += tv.y; }
  float2 pw = make_float2(1.f, 0.f);
  for (int e = 0; e < ck; ++e) pw = cmul(pw, l16);
  float2 c0 = cag[ck * 32 + ch];
  float2 t3 = cmul(pw, bc);
  c0.x += t3.x; c0.y += t3.y;
  unsigned short* hl = reinterpret_cast<unsigned short*>(smem + E_BUL);
  {
    float2 pq = lam;
#pragma unroll
    for (int i = 0; i < 16; ++i) {
      float vr = hr[i] + pq.x * c0.x - pq.y * c0.y;
      float vi = hi[i] + pq.x * c0.y + pq.y * c0.x;
      hl[(ck * 16 + i) * 72 + ch] = f2bf(vr);
      hl[(ck * 16 + i) * 72 + 32 + ch] = f2bf(-vi);
      pq = cmul(pq, lam);
    }
  }
  __syncthreads();
#pragma unroll
  for (int it = 0; it < 4; ++it) {
    int idx = it * 512 + tid;
    int row = idx >> 3, c = idx & 7;
    bf16x8 v = *reinterpret_cast<const bf16x8*>(hl + row * 72 + c * 8);
    *reinterpret_cast<bf16x8*>(a2 + (size_t)(tm * 256 + row) * K2_ + F_ + (c >> 2) * P_ + tn * 32 + (c & 3) * 8) = v;
  }
  STAMP(0, 6);
}

constexpr int G2_STAGE = 32768;
constexpr int G2_NSLOT = 4;
constexpr int G2_LDS = G2_NSLOT * G2_STAGE + 16;
__global__ __launch_bounds__(512, 2) void k_gemm2(const unsigned short* __restrict__ a2, const unsigned short* __restrict__ wdown,
                                                  const unsigned short* __restrict__ cre, const unsigned short* __restrict__ cim,
                                                  const unsigned short* __restrict__ x,
                                                  const unsigned short* __restrict__ xn, const unsigned short* __restrict__ dskip,
                                                  float* __restrict__ out,
                                                  f32x4* __restrict__ slab, unsigned* __restrict__ cnt) {
  extern __shared__ __attribute__((aligned(16))) char smem[];
  int bid = blockIdx.x;
  int xcd = bid & 7, loc = bid >> 3;
  int tm = (xcd >> 1) * 4 + ((loc >> 3) & 3), tn = (xcd & 1) * 4 + ((loc >> 1) & 3), kz = loc & 1;
  int tile = tm * 8 + tn;
  STAMP(1, 0);
  int tid = threadIdx.x, wid = __builtin_amdgcn_readfirstlane(tid >> 6), lane = tid & 63;
  int kg = wid >> 2, wr = (wid >> 1) & 1, wc = wid & 1, fr = lane & 15, fq = lane >> 4;
  const unsigned short* ag = a2 + (size_t)tm * 128 * K2_ + kz * 2560;
  f32x4 acc[4][4], accf[4][4];
#pragma unroll
  for (int i = 0; i < 4; ++i)
#pragma unroll
    for (int j = 0; j < 4; ++j) { acc[i][j] = (f32x4){0, 0, 0, 0}; accf[i][j] = (f32x4){0, 0, 0, 0}; }
  int srow = lane >> 3, schunk = (lane & 7) ^ (srow & 7);
  int wlofs[2];
#pragma unroll
  for (int i = 0; i < 2; ++i) {
    int reg = wid * 2 + i, Q = reg >> 3, kb = reg & 7;
    int r = lane >> 3, c = (lane & 7) ^ (((r >> 1) & 1) << 1) ^ ((kb & 1) << 2);
    wlofs[i] = (kb * 8 + r) * D_ + tn * 128 + Q * 64 + c * 8;
  }
  auto stage = [&](int slot, int kt) {
    char* base = smem + slot * G2_STAGE;
    int kabs = kz * 2560 + kt * 64;
    bool ffn = kabs < F_;
    const unsigned short* wb = ffn ? wdown + (size_t)kabs * D_ : ((kabs < F_ + P_) ? cre + (kabs - F_) : cim + (kabs - F_ - P_));
#pragma unroll
    for (int i = 0; i < 2; ++i) {
      int ins = wid * 2 + i;
      __builtin_amdgcn_global_load_lds((const unsigned*)(ag + (size_t)(ins * 8 + srow) * K2_ + kt * 64 + schunk * 8),
                                       LDS_PTR(base + ins * 1024), 16, 0, 0);
      __builtin_amdgcn_global_load_lds((const unsigned*)(wb + (ffn ? wlofs[i] : (tn * 128 + ins * 8 + srow) * P_ + schunk * 8)),
                                       LDS_PTR(base + 16384 + ins * 1024), 16, 0, 0);
    }
  };
  auto stage_piece = [&](int slot, int kt, int pc) {
    char* base = smem + slot * G2_STAGE;
    int ins = wid * 2 + (pc >> 1);
    if ((pc & 1) == 0) {
      __builtin_amdgcn_global_load_lds((const unsigned*)(ag + (size_t)(ins * 8 + srow) * K2_ + kt * 64 + schunk * 8),
                                       LDS_PTR(base + ins * 1024), 16, 0, 0);
    } else {
      int kabs = kz * 2560 + kt * 64;
      bool ffn = kabs < F_;
      const unsigned short* wb = ffn ? wdown + (size_t)kabs * D_ : ((kabs < F_ + P_) ? cre + (kabs - F_) : cim + (kabs - F_ - P_));
      __builtin_amdgcn_global_load_lds((const unsigned*)(wb + (ffn ? wlofs[pc >> 1] : (tn * 128 + ins * 8 + srow) * P_ + schunk * 8)),
                                       LDS_PTR(base + 16384 + ins * 1024), 16, 0, 0);
    }
  };
  const int ns = kz ? 48 : 40;
  const int nsf = kz ? 16 : 40;
  stage(0, 0); stage(1, 1); stage(2, 2); stage(3, 3);
  int rch = ((kg * 4 + fq) ^ (fr & 7)) * 16;
  const int xoff = (wr * 64 + fr) * 128 + rch;
  const int woffc = 16384 + (wc * 64 + fr) * 128 + rch;
  int wofs[4];
  {
    int kb = kg * 4 + fq, q = (lane >> 2) & 3, pp = lane & 3;
    int sw = ((q >> 1) << 1) ^ ((kb & 1) << 2);
#pragma unroll
    for (int i = 0; i < 4; ++i) wofs[i] = 16384 + (wc * 8 + kb) * 1024 + q * 128 + (((i * 2 + (pp >> 1)) ^ sw) * 16) + (pp & 1) * 8;
  }
  bf16x8 xa_[4], xb_[4], wa_[4], wb_[4];
  const unsigned sbase = lds_u32(smem);
  WAIT_VM(12);
  __builtin_amdgcn_s_barrier();
#pragma unroll
  for (int j = 0; j < 4; ++j) {
    xa_[j] = *reinterpret_cast<const bf16x8*>(smem + xoff + j * 2048);
    wa_[j] = tr_frag<512>(sbase + wofs[j]);
  }
  auto body = [&](auto NTRc, int s, bf16x8 (&xc)[4], bf16x8 (&wcur)[4], bf16x8 (&xnx)[4], bf16x8 (&wnx)[4]) {
    constexpr int NTR = decltype(NTRc)::value;
    WAIT_VM(8);
    __builtin_amdgcn_s_barrier();
    const char* nb = smem + ((s + 1) & 3) * G2_STAGE;
    unsigned nbu = sbase + ((s + 1) & 3) * G2_STAGE;
    int ktn = min(s + 4, ns - 1);
    TR_WAIT();
#pragma unroll
    for (int i = 0; i < 4; ++i) {
#pragma unroll
      for (int j = 0; j < 4; ++j) acc[i][j] = __builtin_amdgcn_mfma_f32_16x16x32_bf16(wcur[i], xc[j], acc[i][j], 0, 0, 0);
      if (NTR) wnx[i] = tr_frag<512>(nbu + wofs[i]);
      else wnx[i] = *reinterpret_cast<const bf16x8*>(nb + woffc + i * 2048);
      xnx[i] = *reinterpret_cast<const bf16x8*>(nb + xoff + i * 2048);
      stage_piece(s & 3, ktn, i);
    }
#pragma unroll
    for (int i = 0; i < 4; ++i) {
      __builtin_amdgcn_sched_group_barrier(0x008, 4, 0);
      __builtin_amdgcn_sched_group_barrier(0x100, 3, 0);
      __builtin_amdgcn_sched_group_barrier(0x020, 1, 0);
    }
    if (s + 1 == nsf) {
#pragma unroll
      for (int i = 0; i < 4; ++i)
#pragma unroll
        for (int j = 0; j < 4; ++j) { accf[i][j] = acc[i][j]; acc[i][j] = (f32x4){0, 0, 0, 0}; }
    }
  };
  STAMP(1, 10); CSTAMP(1, 11);
  for (int s = 0; s < nsf - 2; s += 2) {
    body(IC<1>{}, s, xa_, wa_, xb_, wb_);
    body(IC<1>{}, s + 1, xb_, wb_, xa_, wa_);
  }
  body(IC<1>{}, nsf - 2, xa_, wa_, xb_, wb_);
  body(IC<0>{}, nsf - 1, xb_, wb_, xa_, wa_);
  for (int s = nsf; s < ns; s += 2) {
    body(IC<0>{}, s, xa_, wa_, xb_, wb_);
    body(IC<0>{}, s + 1, xb_, wb_, xa_, wa_);
  }
  WAIT_VM(0);
  STAMP(1, 12); CSTAMP(1, 13);
  __syncthreads();
  STAMP(1, 1);
  f32x4* ex = reinterpret_cast<f32x4*>(smem);
  int wq = wid & 3;
#pragma unroll
  for (int ii = 0; ii < 2; ++ii)
#pragma unroll
    for (int j = 0; j < 4; ++j) {
      f32x4 sf = kg ? accf[ii][j] : accf[2 + ii][j];
      f32x4 sy = kg ? acc[ii][j] : acc[2 + ii][j];
      ex[(((0 * 2 + kg) * 4 + wq) * 8 + ii * 4 + j) * 64 + lane] = sf;
      if (kz) ex[(((1 * 2 + kg) * 4 + wq) * 8 + ii * 4 + j) * 64 + lane] = sy;
    }
  unsigned* role_s = reinterpret_cast<unsigned*>(smem + G2_NSLOT * G2_STAGE);
  if (tid == 0) *role_s = __hip_atomic_fetch_add(&cnt[tile * 32], 1u, __ATOMIC_RELAXED, __HIP_MEMORY_SCOPE_AGENT);
  __syncthreads();
  f32x4 mf[2][4], my[2][4];
#pragma unroll
  for (int ii = 0; ii < 2; ++ii)
#pragma unroll
    for (int j = 0; j < 4; ++j) {
      f32x4 ownf = kg ? accf[2 + ii][j] : accf[ii][j];
      f32x4 owny = kg ? acc[2 + ii][j] : acc[ii][j];
      mf[ii][j] = ownf + ex[(((0 * 2 + (kg ^ 1)) * 4 + wq) * 8 + ii * 4 + j) * 64 + lane];
      my[ii][j] = owny;
      if (kz) my[ii][j] += ex[(((1 * 2 + (kg ^ 1)) * 4 + wq) * 8 + ii * 4 + j) * 64 + lane];
    }
  unsigned role = *role_s;
  STAMP(1, 2);
  f32x4* sl = slab + (size_t)tile * 8192;
  if (role == 0) {
    __amdgpu_buffer_rsrc_t rs = __builtin_amdgcn_make_buffer_rsrc((void*)sl, 0, 8192 * 16, 0x00020000);
#pragma unroll
    for (int ii = 0; ii < 2; ++ii)
#pragma unroll
      for (int j = 0; j < 4; ++j) {
        u32x4 v = __builtin_bit_cast(u32x4, mf[ii][j]);
        __builtin_amdgcn_raw_buffer_store_b128(v, rs, ((ii * 4 + j) * 512 + tid) * 16, 0, 16);
        if (kz) {
          u32x4 w = __builtin_bit_cast(u32x4, my[ii][j]);
          __builtin_amdgcn_raw_buffer_store_b128(w, rs, (4096 + (ii * 4 + j) * 512 + tid) * 16, 0, 16);
        }
      }
    asm volatile("s_waitcnt vmcnt(0)" ::: "memory");
    __syncthreads();
    if (tid == 0) __hip_atomic_store(&cnt[tile * 32 + 16], 1u, __ATOMIC_RELAXED, __HIP_MEMORY_SCOPE_AGENT);
    STAMP(1, 3);
    return;
  }
  u16x4 xv[2][4], xnv[2][4], dv[2];
#pragma unroll
  for (int ii = 0; ii < 2; ++ii) {
    int d = tn * 128 + wc * 64 + (kg * 2 + ii) * 16 + fq * 4;
    dv[ii] = *reinterpret_cast<const u16x4*>(dskip + d);
#pragma unroll
    for (int j = 0; j < 4; ++j) {
      int t = tm * 128 + wr * 64 + j * 16 + fr;
      xv[ii][j] = *reinterpret_cast<const u16x4*>(x + (size_t)t * D_ + d);
      xnv[ii][j] = *reinterpret_cast<const u16x4*>(xn + (size_t)t * D_ + d);
    }
  }
  if (tid == 0) {
    while (__hip_atomic_load(&cnt[tile * 32 + 16], __ATOMIC_RELAXED, __HIP_MEMORY_SCOPE_AGENT) == 0u) __builtin_amdgcn_s_sleep(1);
  }
  __syncthreads();
  __builtin_amdgcn_fence(__ATOMIC_ACQUIRE, "wavefront");
  STAMP(1, 3);
  {
    __amdgpu_buffer_rsrc_t rs = __builtin_amdgcn_make_buffer_rsrc((void*)sl, 0, 8192 * 16, 0x00020000);
#pragma unroll
    for (int ii = 0; ii < 2; ++ii)
#pragma unroll
      for (int j = 0; j < 4; ++j) {
        u32x4 v = __builtin_amdgcn_raw_buffer_load_b128(rs, ((ii * 4 + j) * 512 + tid) * 16, 0, 16);
        mf[ii][j] += __builtin_bit_cast(f32x4, v);
        if (!kz) {
          u32x4 w = __builtin_amdgcn_raw_buffer_load_b128(rs, (4096 + (ii * 4 + j) * 512 + tid) * 16, 0, 16);
          my[ii][j] = __builtin_bit_cast(f32x4, w);
        }
      }
  }
#pragma unroll
  for (int ii = 0; ii < 2; ++ii)
#pragma unroll
    for (int j = 0; j < 4; ++j) {
      int t = tm * 128 + wr * 64 + j * 16 + fr;
      int d = tn * 128 + wc * 64 + (kg * 2 + ii) * 16 + fq * 4;
      f32x4 o;
#pragma unroll
      for (int r = 0; r < 4; ++r) {
        float f = rbf(mf[ii][j][r]);
        float g = rbf(f * __builtin_amdgcn_rcpf(1.0f + __expf(-f)));
        float s = 2.0f * my[ii][j][r] + rbf(bf2f(xnv[ii][j][r]) * bf2f(dv[ii][r]));
        o[r] = bf2f(xv[ii][j][r]) + s * g;
      }
      *reinterpret_cast<f32x4*>(out + (size_t)t * D_ + d) = o;
    }
  STAMP(1, 4);
}

extern "C" void kernel_launch(void* const* d_in, const int* in_sizes, int n_in, void* d_out, int out_size, void* d_ws,
                              size_t ws_size, hipStream_t stream) {
  const unsigned short* x = (const unsigned short*)d_in[0];
  const unsigned short* cre = (const unsigned short*)d_in[5];
  const unsigned short* cim = (const unsigned short*)d_in[6];
  const unsigned short* dskip = (const unsigned short*)d_in[8];
  const unsigned short* wdown = (const unsigned short*)d_in[12];
  char* ws = (char*)d_ws;
  unsigned short* xn = (unsigned short*)(ws + OFF_XN);
  unsigned short* a2 = (unsigned short*)(ws + OFF_A2);
  float* out = (float*)d_out;
  G1Args ga;
  ga.xn = xn;
  ga.wgate = (const unsigned short*)d_in[10];
  ga.wup = (const unsigned short*)d_in[11];
  ga.bre = (const unsigned short*)d_in[3];
  ga.bim = (const unsigned short*)d_in[4];
  ga.lre = (const unsigned short*)d_in[1];
  ga.lim = (const unsigned short*)d_in[2];
  ga.logdt = (const unsigned short*)d_in[7];
  ga.wdown = wdown; ga.cre = cre; ga.cim = cim;
  ga.a2 = a2;
  ga.agg = (unsigned long long*)(ws + OFF_AGG);

  PrepArgs pa; pa.x = x; pa.wdown = wdown; pa.cre = cre; pa.cim = cim; pa.wgate = ga.wgate; pa.wup = ga.wup; pa.bre = ga.bre; pa.bim = ga.bim; pa.ws = ws;
  k_prep<<<PB_LN, 256, 0, stream>>>(pa);
  (void)hipFuncSetAttribute((const void*)k_gemm1, hipFuncAttributeMaxDynamicSharedMemorySize, G1_LDS);
  k_gemm1<<<256, 512, G1_LDS, stream>>>(ga);
  (void)hipFuncSetAttribute((const void*)k_gemm2, hipFuncAttributeMaxDynamicSharedMemorySize, G2_LDS);
  k_gemm2<<<256, 512, G2_LDS, stream>>>(a2, wdown, cre, cim, x, xn, dskip, out, (f32x4*)(ws + OFF_SLAB), (unsigned*)(ws + OFF_CNT));
}
```

```cpp
#include <hip/hip_runtime.h>
#include <hip/hip_bf16.h>
#include <stdint.h>

typedef __attribute__((ext_vector_type(8))) short bf16x8;
typedef __attribute__((ext_vector_type(4))) float f32x4;
typedef __attribute__((ext_vector_type(4))) unsigned short u16x4;
typedef __attribute__((ext_vector_type(4))) unsigned u32x4;

constexpr int S_ = 2048;
constexpr int D_ = 1024;
constexpr int P_ = 1024;
constexpr int F_ = 3584;
constexpr int N1_ = 9216;
constexpr int K2_ = 5632;

constexpr size_t OFF_AGG = 0;
constexpr size_t OFF_CNT = OFF_AGG + 32 * 8 * 32 * 2 * 8;
constexpr size_t SYNC_BYTES = OFF_CNT + 128 * 32 * 4;
constexpr size_t OFF_XN = SYNC_BYTES;
constexpr size_t OFF_A2 = OFF_XN + (size_t)S_ * D_ * 2;
constexpr size_t OFF_SLAB = OFF_A2 + (size_t)S_ * K2_ * 2;

__device__ __forceinline__ float bf2f(unsigned short v) { return __uint_as_float(((unsigned)v) << 16); }
__device__ __forceinline__ unsigned short f2bf(float f) {
  __bf16 b = (__bf16)f;
  return __builtin_bit_cast(unsigned short, b);
}
typedef __attribute__((ext_vector_type(2))) __bf16 bf16x2n;
typedef __attribute__((ext_vector_type(2))) float f32x2;
__device__ __forceinline__ unsigned pk2bf(float a, float b) {
  f32x2 v = {a, b};
  bf16x2n r = __builtin_convertvector(v, bf16x2n);
  return __builtin_bit_cast(unsigned, r);
}
__device__ __forceinline__ float lo2f(unsigned u) { return __uint_as_float(u << 16); }
__device__ __forceinline__ float hi2f(unsigned u) { return __uint_as_float(u & 0xFFFF0000u); }
__device__ __forceinline__ float rbf(float f) { return bf2f(f2bf(f)); }
__device__ __forceinline__ float2 cmul(float2 a, float2 b) { return make_float2(a.x * b.x - a.y * b.y, a.x * b.y + a.y * b.x); }

#define STAMP(k, i)
#define CSTAMP(k, i)
#define WAIT_VM(n) asm volatile("s_waitcnt vmcnt(" #n ")" ::: "memory")
#define LDS_PTR(p) ((__attribute__((address_space(3))) unsigned*)(p))

constexpr int PB_LN = 512;
#define PF_ALL 0
constexpr int PF_N = 6;
struct PrepArgs { const unsigned short *x, *wdown, *cre, *cim, *wgate, *wup, *bre, *bim; char* ws; };
__global__ __launch_bounds__(256) void k_prep(PrepArgs pa) {
  const unsigned short* __restrict__ x = pa.x; char* __restrict__ ws = pa.ws;
  int b = blockIdx.x, tid = threadIdx.x, wave = tid >> 6, lane = tid & 63;
  unsigned short* xn = (unsigned short*)(ws + OFF_XN);
  for (int i = b * 256 + tid; i < (int)(SYNC_BYTES / 16); i += PB_LN * 256)
    reinterpret_cast<u32x4*>(ws)[i] = (u32x4){0u, 0u, 0u, 0u};
  int row = b * 4 + wave;
  const bf16x8* src = reinterpret_cast<const bf16x8*>(x + (size_t)row * D_);
  bf16x8 v0 = src[lane], v1 = src[64 + lane];
  float f[16];
#pragma unroll
  for (int j = 0; j < 8; ++j) { f[j] = bf2f((unsigned short)v0[j]); f[8 + j] = bf2f((unsigned short)v1[j]); }
  float s = 0.f;
#pragma unroll
  for (int j = 0; j < 16; ++j) s += f[j];
#pragma unroll
  for (int o = 32; o >= 1; o >>= 1) s += __shfl_xor(s, o);
  float mean = s * (1.0f / D_);
  float q = 0.f;
#pragma unroll
  for (int j = 0; j < 16; ++j) { float d = f[j] - mean; q += d * d; }
#pragma unroll
  for (int o = 32; o >= 1; o >>= 1) q += __shfl_xor(q, o);
  float var = q * (1.0f / D_);
  float mb = rbf(mean), vb = rbf(var);
  float den = rbf(sqrtf(rbf(vb + 1e-5f)));
  bf16x8 o0, o1;
#pragma unroll
  for (int j = 0; j < 8; ++j) {
    o0[j] = (short)f2bf(rbf(f[j] - mb) / den);
    o1[j] = (short)f2bf(rbf(f[8 + j] - mb) / den);
  }
  bf16x8* dst = reinterpret_cast<bf16x8*>(xn + (size_t)row * D_);
  dst[lane] = o0; dst[64 + lane] = o1;
}

typedef __attribute__((ext_vector_type(4))) short s16x4;
template <int OFF2>
__device__ __forceinline__ bf16x8 tr_frag(unsigned lds_addr) {
  s16x4 lo, hi;
  asm volatile("ds_read_b64_tr_b16 %0, %2\n\tds_read_b64_tr_b16 %1, %2 offset:%c3"
               : "=&v"(lo), "=&v"(hi) : "v"(lds_addr), "i"(OFF2));
  return __builtin_shufflevector(lo, hi, 0, 1, 2, 3, 4, 5, 6, 7);
}
#define TR_WAIT() do { asm volatile("s_waitcnt lgkmcnt(0)" ::: "memory"); __builtin_amdgcn_sched_barrier(0); } while (0)
__device__ __forceinline__ unsigned lds_u32(const void* p) {
  return (unsigned)(uintptr_t)(__attribute__((address_space(3))) const char*)p;
}

__device__ __forceinline__ float gelu_tanh(float x) {
  float w = x * __builtin_fmaf(x * x, -0.10294323970650479f, -2.3022081985952363f);
  return x * __builtin_amdgcn_rcpf(1.0f + __builtin_amdgcn_exp2f(w));
}

constexpr int G1_AT = 256 * 64;
constexpr int G1_WT = 288 * 64;
constexpr int G1_STAGE = G1_AT + G1_WT;
constexpr int G1_NSLOT = 4;
constexpr int G1_DUMMY = G1_NSLOT * G1_STAGE;
constexpr int G1_COEF = G1_DUMMY + 1024;
constexpr int G1_LDS = G1_COEF + 512;
constexpr int E_BUL = 0;
constexpr int E_ACT = 69632;
constexpr int E_CAG = 131072;
constexpr int E_TERM = 135168;
constexpr int E_MISC = 137216;

struct G1Args {
  const unsigned short *xn, *wgate, *wup, *bre, *bim, *lre, *lim, *logdt, *wdown, *cre, *cim;
  unsigned short* a2;
  unsigned long long* agg;
};
template <int V> struct IC { static constexpr int value = V; };

__global__ __launch_bounds__(512, 2) void k_gemm1(G1Args ga) {
  extern __shared__ __attribute__((aligned(16))) char smem[];
  int tid = threadIdx.x, wid = __builtin_amdgcn_readfirstlane(tid >> 6), lane = tid & 63;
  int xl = blockIdx.x & 7, loc = blockIdx.x >> 3;
  STAMP(0, 0);
  int tn = xl * 4 + (loc >> 3), tm = loc & 7;
  int wr = wid >> 1, wc = wid & 1;
  int fr = lane & 15, fq = lane >> 4;
  const unsigned short* xg = ga.xn + (size_t)tm * 256 * D_;
  unsigned short* a2 = ga.a2;
  unsigned long long* agg = ga.agg;

  f32x4 acc[9][4];
#pragma unroll
  for (int i = 0; i < 9; ++i)
#pragma unroll
    for (int j = 0; j < 4; ++j) acc[i][j] = (f32x4){0.f, 0.f, 0.f, 0.f};

  int srow = lane >> 2;
  int schunk = (lane & 3) ^ (((srow >> 3) & 1) << 1);
  const char* wsrc[3]; int wstr[3];
#pragma unroll
  for (int i = 0; i < 3; ++i) {
    int reg = (i < 2) ? wid * 2 + i : 16 + (wid & 1);
    const unsigned short* ptr; int str;
    if (reg < 12) {
      int Q = reg >> 2, kb = reg & 3;
      int r = lane >> 3, c = (lane & 7) ^ (((r >> 1) & 1) << 1) ^ ((kb & 1) << 2);
      int k = kb * 8 + r;
      const unsigned short* mat; int col;
      if (Q == 0) { mat = ga.wgate; col = c * 8; }
      else if (Q == 1) { mat = ga.wup; col = c * 8; }
      else if (c < 6) { mat = ga.wgate; col = 64 + c * 8; }
      else { mat = ga.wup; col = 64 + (c - 6) * 8; }
      ptr = mat + (size_t)k * F_ + tn * 112 + col; str = F_ * 64;
    } else if (reg < 14) {
      int r = lane >> 3, c = (lane & 7) ^ (((r >> 1) & 1) << 1);
      int k = ((reg - 12) * 2 + (c >> 2)) * 8 + r;
      ptr = ga.wup + (size_t)k * F_ + tn * 112 + 80 + (c & 3) * 8; str = F_ * 64;
    } else {
      int row = (reg - 14) * 16 + srow;
      const unsigned short* mat = (row < 32) ? ga.bre : ga.bim;
      ptr = mat + (size_t)(tn * 32 + (row & 31)) * D_ + schunk * 8; str = 64;
    }
    wsrc[i] = (const char*)ptr; wstr[i] = str;
  }
  auto stage = [&](int slot, int ks) {
    char* base = smem + slot * G1_STAGE;
#pragma unroll
    for (int i = 0; i < 2; ++i) {
      int ins = wid * 2 + i;
      __builtin_amdgcn_global_load_lds((const unsigned*)(xg + (size_t)(ins * 16 + srow) * D_ + ks * 32 + schunk * 8),
                                       LDS_PTR(base + ins * 1024), 16, 0, 0);
    }
#pragma unroll
    for (int i = 0; i < 2; ++i)
      __builtin_amdgcn_global_load_lds((const unsigned*)(wsrc[i] + (size_t)ks * wstr[i]), LDS_PTR(base + G1_AT + (wid * 2 + i) * 1024), 16, 0, 0);
    if (wid < 2)
      __builtin_amdgcn_global_load_lds((const unsigned*)(wsrc[2] + (size_t)ks * wstr[2]), LDS_PTR(base + G1_AT + (16 + wid) * 1024), 16, 0, 0);
  };

  auto stage_piece = [&](int slot, int ks, int pc) {
    char* base = smem + slot * G1_STAGE;
    if (pc < 2) {
      int ins = wid * 2 + pc;
      __builtin_amdgcn_global_load_lds((const unsigned*)(xg + (size_t)(ins * 16 + srow) * D_ + ks * 32 + schunk * 8),
                                       LDS_PTR(base + ins * 1024), 16, 0, 0);
    } else if (pc < 4) {
      int i = pc - 2;
      __builtin_amdgcn_global_load_lds((const unsigned*)(wsrc[i] + (size_t)ks * wstr[i]), LDS_PTR(base + G1_AT + (wid * 2 + i) * 1024), 16, 0, 0);
    } else if (wid < 2) {
      __builtin_amdgcn_global_load_lds((const unsigned*)(wsrc[2] + (size_t)ks * wstr[2]), LDS_PTR(base + G1_AT + (16 + wid) * 1024), 16, 0, 0);
    }
  };
  float c_lr = 0.f, c_li = 0.f, c_ld = 0.f;
  if (tid >= 448 && tid < 480) {
    int p = tn * 32 + (tid - 448);
    c_lr = bf2f(ga.lre[p]); c_li = bf2f(ga.lim[p]); c_ld = bf2f(ga.logdt[p]);
  }
  asm volatile("s_waitcnt vmcnt(0)" ::: "memory");
  const int ns = D_ / 32;
  stage(0, 0); stage(1, 1); stage(2, 2); stage(3, 3);
  if (tid >= 448 && tid < 480) {
    float lr = c_lr, li = c_li;
    float dt = rbf(expf(c_ld));
    float zr = dt * lr, zi = dt * li;
    float e = expf(zr);
    float lx = e * cosf(zi), ly = e * sinf(zi);
    float nr = lx - 1.0f, ni = ly;
    float den = lr * lr + li * li;
    *reinterpret_cast<f32x4*>(smem + G1_COEF + (tid - 448) * 16) = (f32x4){lx, ly, (nr * lr + ni * li) / den, (ni * lr - nr * li) / den};
  }
  int rchunk = fq ^ (((fr >> 3) & 1) << 1);
  const int xoff = (wr * 64 + fr) * 64 + rchunk * 16;
  const int boff = G1_AT + 14 * 1024 + fr * 64 + rchunk * 16;
  const unsigned sbase = lds_u32(smem);
  auto mainloop = [&](auto WCc) {
    constexpr int WC = decltype(WCc)::value;
    constexpr int NTR = WC ? 6 : 8;
    int kb = fq, q = (lane >> 2) & 3, pp = lane & 3;
    int sw = ((q >> 1) << 1) ^ ((kb & 1) << 2);
    int wo[NTR];
#pragma unroll
    for (int i = 0; i < NTR; ++i) {
      if (WC == 0) wo[i] = G1_AT + ((i >> 2) * 4 + kb) * 1024 + q * 128 + ((((i & 3) * 2 + (pp >> 1)) ^ sw) * 16) + (pp & 1) * 8;
      else if (i < 4) wo[i] = G1_AT + (8 + kb) * 1024 + q * 128 + (((i * 2 + (pp >> 1)) ^ sw) * 16) + (pp & 1) * 8;
      else wo[i] = G1_AT + (12 + (kb >> 1)) * 1024 + q * 128 + ((((kb & 1) * 4 + (i - 4) * 2 + (pp >> 1)) ^ ((q >> 1) << 1)) * 16) + (pp & 1) * 8;
    }
    unsigned wpk[NTR / 2];
#pragma unroll
    for (int i = 0; i < NTR / 2; ++i) wpk[i] = (unsigned)wo[2 * i] | ((unsigned)wo[2 * i + 1] << 16);
    auto ldw = [&](int i, const char* nb, unsigned nbu) -> bf16x8 {
      if (i < NTR) return tr_frag<512>(nbu + ((i & 1) ? (wpk[i >> 1] >> 16) : (wpk[i >> 1] & 0xFFFFu)));
      return *reinterpret_cast<const bf16x8*>(nb + boff + (WC ? (i - NTR + 1) : 0) * 1024);
    };
    bf16x8 xa_[4], xb_[4], wf[9];
    if (wid < 2) WAIT_VM(15); else WAIT_VM(12);
    __builtin_amdgcn_s_barrier();
#pragma unroll
    for (int j = 0; j < 4; ++j) xa_[j] = *reinterpret_cast<const bf16x8*>(smem + xoff + j * 1024);
#pragma unroll
    for (int i = 0; i < 9; ++i) wf[i] = ldw(i, smem, sbase);

    auto body = [&](int s, bf16x8 (&xc)[4], bf16x8 (&xnx)[4]) {
      if (wid < 2) WAIT_VM(10); else WAIT_VM(8);
      __builtin_amdgcn_s_barrier();
      const char* nb = smem + ((s + 1) & 3) * G1_STAGE;
      unsigned nbu = sbase + ((s + 1) & 3) * G1_STAGE;
      int ksn = min(s + 4, ns - 1);
      TR_WAIT();
#pragma unroll
      for (int i = 0; i < 9; ++i) {
#pragma unroll
        for (int j = 0; j < 4; ++j) acc[i][j] = __builtin_amdgcn_mfma_f32_16x16x32_bf16(wf[i], xc[j], acc[i][j], 0, 0, 0);
        wf[i] = ldw(i, nb, nbu);
        if (i < 4) xnx[i] = *reinterpret_cast<const bf16x8*>(nb + xoff + i * 1024);
        if (i >= 1 && i <= 5) stage_piece(s & 3, ksn, i - 1);
      }
#pragma unroll
      for (int i = 0; i < 9; ++i) {
        __builtin_amdgcn_sched_group_barrier(0x008, 4, 0);
        __builtin_amdgcn_sched_group_barrier(0x100, 1, 0);
      }
    };
    STAMP(0, 10); CSTAMP(0, 11);
    for (int s = 0; s < ns; s += 2) {
      body(s, xa_, xb_);
      body(s + 1, xb_, xa_);
    }
    STAMP(0, 12); CSTAMP(0, 13);
  };
  if (wc == 0) mainloop(IC<0>{}); else mainloop(IC<1>{});

  WAIT_VM(0);
  __syncthreads();
  STAMP(0, 1);
  float* bul = reinterpret_cast<float*>(smem + E_BUL);
  unsigned short* actl = reinterpret_cast<unsigned short*>(smem + E_ACT);
  float2* cag = reinterpret_cast<float2*>(smem + E_CAG);
  float2* term = reinterpret_cast<float2*>(smem + E_TERM);
  int tl = wr * 64 + fr;
  if (wc == 0) {
#pragma unroll
    for (int j = 0; j < 4; ++j) *reinterpret_cast<f32x4*>(bul + (tl + j * 16) * 68 + fq * 4) = acc[8][j];
  } else {
#pragma unroll
    for (int j = 0; j < 4; ++j) {
      *reinterpret_cast<f32x4*>(bul + (tl + j * 16) * 68 + 16 + fq * 4) = acc[6][j];
      *reinterpret_cast<f32x4*>(bul + (tl + j * 16) * 68 + 32 + fq * 4) = acc[7][j];
      *reinterpret_cast<f32x4*>(bul + (tl + j * 16) * 68 + 48 + fq * 4) = acc[8][j];
    }
  }
  __syncthreads();
  int ch = tid & 31, ck = tid >> 5;
  int p = tn * 32 + ch;
  float2 lam, cf;
  {
    f32x4 lc = *reinterpret_cast<const f32x4*>(smem + G1_COEF + ch * 16);
    lam = make_float2(lc[0], lc[1]); cf = make_float2(lc[2], lc[3]);
  }
  float hr[16], hi[16];
  {
    float sr = 0.f, si = 0.f;
#pragma unroll
    for (int i = 0; i < 16; ++i) {
      float rr = bul[(ck * 16 + i) * 68 + ch], ri = bul[(ck * 16 + i) * 68 + 32 + ch];
      float br = cf.x * rr - cf.y * ri, bi = cf.x * ri + cf.y * rr;
      float nr = lam.x * sr - lam.y * si + br;
      float ni = lam.x * si + lam.y * sr + bi;
      sr = nr; si = ni; hr[i] = sr; hi[i] = si;
    }
    cag[ck * 32 + ch] = make_float2(sr, si);
  }
  float2 l16 = lam;
#pragma unroll
  for (int i = 0; i < 4; ++i) l16 = cmul(l16, l16);
  float2 l256 = l16;
#pragma unroll
  for (int i = 0; i < 4; ++i) l256 = cmul(l256, l256);
  __syncthreads();
  if (tid < 32) {
    float2 c = make_float2(0.f, 0.f);
#pragma unroll
    for (int k = 0; k < 16; ++k) {
      float2 aa = cag[k * 32 + ch];
      cag[k * 32 + ch] = c;
      float2 t2 = cmul(l16, c);
      c = make_float2(t2.x + aa.x, t2.y + aa.y);
    }
    if (tm < 7) {
      unsigned long long* g = agg + ((size_t)(tn * 8 + tm) * 32 + ch) * 2;
      __hip_atomic_store(g, (1ull << 32) | (unsigned long long)__float_as_uint(c.x), __ATOMIC_RELAXED, __HIP_MEMORY_SCOPE_AGENT);
      __hip_atomic_store(g + 1, (1ull << 32) | (unsigned long long)__float_as_uint(c.y), __ATOMIC_RELAXED, __HIP_MEMORY_SCOPE_AGENT);
    }
  }
  STAMP(0, 2);
  unsigned long long pv0 = 0ull, pv1 = 0ull;
  if (ck < tm) {
    const unsigned long long* g = agg + ((size_t)(tn * 8 + ck) * 32 + ch) * 2;
    pv0 = __hip_atomic_load(g, __ATOMIC_RELAXED, __HIP_MEMORY_SCOPE_AGENT);
    pv1 = __hip_atomic_load(g + 1, __ATOMIC_RELAXED, __HIP_MEMORY_SCOPE_AGENT);
  }
#pragma unroll
  for (int i = 0; i < 6; ++i) {
    int g = min((blockIdx.x * 8 + wid) * 6 + i, 11263);
    const unsigned short* src = (g < 7168) ? ga.wdown + (size_t)g * 512 : (g < 9216 ? ga.cre + (size_t)(g - 7168) * 512 : ga.cim + (size_t)(g - 9216) * 512);
    __builtin_amdgcn_global_load_lds((const unsigned*)(src + lane * 8), LDS_PTR(smem + G1_DUMMY), 16, 0, 0);
  }
  auto geglu4 = [&](const f32x4& gt, const f32x4& up) -> uint2 {
    unsigned g01 = pk2bf(gt[0], gt[1]), g23 = pk2bf(gt[2], gt[3]);
    unsigned u01 = pk2bf(up[0], up[1]), u23 = pk2bf(up[2], up[3]);
    unsigned a01 = pk2bf(gelu_tanh(lo2f(g01)), gelu_tanh(hi2f(g01)));
    unsigned a23 = pk2bf(gelu_tanh(lo2f(g23)), gelu_tanh(hi2f(g23)));
    uint2 o;
    o.x = pk2bf(lo2f(a01) * lo2f(u01), hi2f(a01) * hi2f(u01));
    o.y = pk2bf(lo2f(a23) * lo2f(u23), hi2f(a23) * hi2f(u23));
    return o;
  };
  if (wc == 0) {
#pragma unroll
    for (int i = 0; i < 4; ++i)
#pragma unroll
      for (int j = 0; j < 4; ++j)
        *reinterpret_cast<uint2*>(actl + (tl + j * 16) * 120 + i * 16 + fq * 4) = geglu4(acc[i][j], acc[i + 4][j]);
  } else {
#pragma unroll
    for (int i = 0; i < 3; ++i)
#pragma unroll
      for (int j = 0; j < 4; ++j)
        *reinterpret_cast<uint2*>(actl + (tl + j * 16) * 120 + 64 + i * 16 + fq * 4) = geglu4(acc[i][j], acc[i + 3][j]);
  }
  __syncthreads();
  STAMP(0, 3);
#pragma unroll
  for (int it = 0; it < 7; ++it) {
    int idx = it * 512 + tid;
    int row = idx / 14, c = idx - row * 14;
    bf16x8 v = *reinterpret_cast<const bf16x8*>(actl + row * 120 + c * 8);
    *reinterpret_cast<bf16x8*>(a2 + (size_t)(tm * 256 + row) * K2_ + tn * 112 + c * 8) = v;
  }
  STAMP(0, 4);
  if (ck < tm) {
    const unsigned long long* g = agg + ((size_t)(tn * 8 + ck) * 32 + ch) * 2;
    unsigned long long v0 = pv0, v1 = pv1;
    while (!((v0 >> 32) == 1ull && (v1 >> 32) == 1ull)) {
      __builtin_amdgcn_s_sleep(1);
      v0 = __hip_atomic_load(g, __ATOMIC_RELAXED, __HIP_MEMORY_SCOPE_AGENT);
      v1 = __hip_atomic_load(g + 1, __ATOMIC_RELAXED, __HIP_MEMORY_SCOPE_AGENT);
    }
    float2 av = make_float2(__uint_as_float((unsigned)v0), __uint_as_float((unsigned)v1));
    for (int e = tm - 1 - ck; e > 0; --e) av = cmul(av, l256);
    term[ck * 32 + ch] = av;
  }
  __syncthreads();
  STAMP(0, 5);
  float2 bc = make_float2(0.f, 0.f);
  for (int j = 0; j < tm; ++j) { float2 tv = term[j * 32 + ch]; bc.x += tv.x; bc.y += tv.y; }
  float2 pw = make_float2(1.f, 0.f);
  for (int e = 0; e < ck; ++e) pw = cmul(pw, l16);
  float2 c0 = cag[ck * 32 + ch];
  float2 t3 = cmul(pw, bc);
  c0.x += t3.x; c0.y += t3.y;
  unsigned short* hl = reinterpret_cast<unsigned short*>(smem + E_BUL);
  {
    float2 pq = lam;
#pragma unroll
    for (int i = 0; i < 16; ++i) {
      float vr = hr[i] + pq.x * c0.x - pq.y * c0.y;
      float vi = hi[i] + pq.x * c0.y + pq.y * c0.x;
      hl[(ck * 16 + i) * 72 + ch] = f2bf(vr);
      hl[(ck * 16 + i) * 72 + 32 + ch] = f2bf(-vi);
      pq = cmul(pq, lam);
    }
  }
  __syncthreads();
#pragma unroll
  for (int it = 0; it < 4; ++it) {
    int idx = it * 512 + tid;
    int row = idx >> 3, c = idx & 7;
    bf16x8 v = *reinterpret_cast<const bf16x8*>(hl + row * 72 + c * 8);
    *reinterpret_cast<bf16x8*>(a2 + (size_t)(tm * 256 + row) * K2_ + F_ + (c >> 2) * P_ + tn * 32 + (c & 3) * 8) = v;
  }
  STAMP(0, 6);
}

constexpr int G2_STAGE = 32768;
constexpr int G2_NSLOT = 4;
constexpr int G2_LDS = G2_NSLOT * G2_STAGE + 16;
__global__ __launch_bounds__(512, 2) void k_gemm2(const unsigned short* __restrict__ a2, const unsigned short* __restrict__ wdown,
                                                  const unsigned short* __restrict__ cre, const unsigned short* __restrict__ cim,
                                                  const unsigned short* __restrict__ x,
                                                  const unsigned short* __restrict__ xn, const unsigned short* __restrict__ dskip,
                                                  float* __restrict__ out,
                                                  f32x4* __restrict__ slab, unsigned* __restrict__ cnt) {
  extern __shared__ __attribute__((aligned(16))) char smem[];
  int bid = blockIdx.x;
  int xcd = bid & 7, loc = bid >> 3;
  int tm = (xcd >> 1) * 4 + ((loc >> 3) & 3), tn = (xcd & 1) * 4 + ((loc >> 1) & 3), kz = loc & 1;
  int tile = tm * 8 + tn;
  STAMP(1, 0);
  int tid = threadIdx.x, wid = __builtin_amdgcn_readfirstlane(tid >> 6), lane = tid & 63;
  int kg = wid >> 2, wr = (wid >> 1) & 1, wc = wid & 1, fr = lane & 15, fq = lane >> 4;
  const unsigned short* ag = a2 + (size_t)tm * 128 * K2_ + kz * 2560;
  f32x4 acc[4][4], accf[4][4];
#pragma unroll
  for (int i = 0; i < 4; ++i)
#pragma unroll
    for (int j = 0; j < 4; ++j) { acc[i][j] = (f32x4){0, 0, 0, 0}; accf[i][j] = (f32x4){0, 0, 0, 0}; }
  int srow = lane >> 3, schunk = (lane & 7) ^ (srow & 7);
  int wlofs[2];
#pragma unroll
  for (int i = 0; i < 2; ++i) {
    int reg = wid * 2 + i, Q = reg >> 3, kb = reg & 7;
    int r = lane >> 3, c = (lane & 7) ^ (((r >> 1) & 1) << 1) ^ ((kb & 1) << 2);
    wlofs[i] = (kb * 8 + r) * D_ + tn * 128 + Q * 64 + c * 8;
  }
  auto stage = [&](int slot, int kt) {
    char* base = smem + slot * G2_STAGE;
    int kabs = kz * 2560 + kt * 64;
    bool ffn = kabs < F_;
    const unsigned short* wb = ffn ? wdown + (size_t)kabs * D_ : ((kabs < F_ + P_) ? cre + (kabs - F_) : cim + (kabs - F_ - P_));
#pragma unroll
    for (int i = 0; i < 2; ++i) {
      int ins = wid * 2 + i;
      __builtin_amdgcn_global_load_lds((const unsigned*)(ag + (size_t)(ins * 8 + srow) * K2_ + kt * 64 + schunk * 8),
                                       LDS_PTR(base + ins * 1024), 16, 0, 0);
      __builtin_amdgcn_global_load_lds((const unsigned*)(wb + (ffn ? wlofs[i] : (tn * 128 + ins * 8 + srow) * P_ + schunk * 8)),
                                       LDS_PTR(base + 16384 + ins * 1024), 16, 0, 0);
    }
  };
  auto stage_piece = [&](int slot, int kt, int pc) {
    char* base = smem + slot * G2_STAGE;
    int ins = wid * 2 + (pc >> 1);
    if ((pc & 1) == 0) {
      __builtin_amdgcn_global_load_lds((const unsigned*)(ag + (size_t)(ins * 8 + srow) * K2_ + kt * 64 + schunk * 8),
                                       LDS_PTR(base + ins * 1024), 16, 0, 0);
    } else {
      int kabs = kz * 2560 + kt * 64;
      bool ffn = kabs < F_;
      const unsigned short* wb = ffn ? wdown + (size_t)kabs * D_ : ((kabs < F_ + P_) ? cre + (kabs - F_) : cim + (kabs - F_ - P_));
      __builtin_amdgcn_global_load_lds((const unsigned*)(wb + (ffn ? wlofs[pc >> 1] : (tn * 128 + ins * 8 + srow) * P_ + schunk * 8)),
                                       LDS_PTR(base + 16384 + ins * 1024), 16, 0, 0);
    }
  };
  const int ns = kz ? 48 : 40;
  const int nsf = kz ? 16 : 40;
  stage(0, 0); stage(1, 1); stage(2, 2); stage(3, 3);
  int rch = ((kg * 4 + fq) ^ (fr & 7)) * 16;
  const int xoff = (wr * 64 + fr) * 128 + rch;
  const int woffc = 16384 + (wc * 64 + fr) * 128 + rch;
  int wofs[4];
  {
    int kb = kg * 4 + fq, q = (lane >> 2) & 3, pp = lane & 3;
    int sw = ((q >> 1) << 1) ^ ((kb & 1) << 2);
#pragma unroll
    for (int i = 0; i < 4; ++i) wofs[i] = 16384 + (wc * 8 + kb) * 1024 + q * 128 + (((i * 2 + (pp >> 1)) ^ sw) * 16) + (pp & 1) * 8;
  }
  bf16x8 xa_[4], xb_[4], wa_[4], wb_[4];
  const unsigned sbase = lds_u32(smem);
  WAIT_VM(12);
  __builtin_amdgcn_s_barrier();
#pragma unroll
  for (int j = 0; j < 4; ++j) {
    xa_[j] = *reinterpret_cast<const bf16x8*>(smem + xoff + j * 2048);
    wa_[j] = tr_frag<512>(sbase + wofs[j]);
  }
  auto body = [&](auto NTRc, int s, bf16x8 (&xc)[4], bf16x8 (&wcur)[4], bf16x8 (&xnx)[4], bf16x8 (&wnx)[4]) {
    constexpr int NTR = decltype(NTRc)::value;
    WAIT_VM(8);
    __builtin_amdgcn_s_barrier();
    const char* nb = smem + ((s + 1) & 3) * G2_STAGE;
    unsigned nbu = sbase + ((s + 1) & 3) * G2_STAGE;
    int ktn = min(s + 4, ns - 1);
    TR_WAIT();
#pragma unroll
    for (int i = 0; i < 4; ++i) {
#pragma unroll
      for (int j = 0; j < 4; ++j) acc[i][j] = __builtin_amdgcn_mfma_f32_16x16x32_bf16(wcur[i], xc[j], acc[i][j], 0, 0, 0);
      if (NTR) wnx[i] = tr_frag<512>(nbu + wofs[i]);
      else wnx[i] = *reinterpret_cast<const bf16x8*>(nb + woffc + i * 2048);
      xnx[i] = *reinterpret_cast<const bf16x8*>(nb + xoff + i * 2048);
      stage_piece(s & 3, ktn, i);
    }
#pragma unroll
    for (int i = 0; i < 4; ++i) {
      __builtin_amdgcn_sched_group_barrier(0x008, 4, 0);
      __builtin_amdgcn_sched_group_barrier(0x100, 3, 0);
      __builtin_amdgcn_sched_group_barrier(0x020, 1, 0);
    }
    if (s + 1 == nsf) {
#pragma unroll
      for (int i = 0; i < 4; ++i)
#pragma unroll
        for (int j = 0; j < 4; ++j) { accf[i][j] = acc[i][j]; acc[i][j] = (f32x4){0, 0, 0, 0}; }
    }
  };
  STAMP(1, 10); CSTAMP(1, 11);
  for (int s = 0; s < nsf - 2; s += 2) {
    body(IC<1>{}, s, xa_, wa_, xb_, wb_);
    body(IC<1>{}, s + 1, xb_, wb_, xa_, wa_);
  }
  body(IC<1>{}, nsf - 2, xa_, wa_, xb_, wb_);
  body(IC<0>{}, nsf - 1, xb_, wb_, xa_, wa_);
  for (int s = nsf; s < ns; s += 2) {
    body(IC<0>{}, s, xa_, wa_, xb_, wb_);
    body(IC<0>{}, s + 1, xb_, wb_, xa_, wa_);
  }
  WAIT_VM(0);
  STAMP(1, 12); CSTAMP(1, 13);
  __syncthreads();
  STAMP(1, 1);
  f32x4* ex = reinterpret_cast<f32x4*>(smem);
  int wq = wid & 3;
#pragma unroll
  for (int ii = 0; ii < 2; ++ii)
#pragma unroll
    for (int j = 0; j < 4; ++j) {
      f32x4 sf = kg ? accf[ii][j] : accf[2 + ii][j];
      f32x4 sy = kg ? acc[ii][j] : acc[2 + ii][j];
      ex[(((0 * 2 + kg) * 4 + wq) * 8 + ii * 4 + j) * 64 + lane] = sf;
      if (kz) ex[(((1 * 2 + kg) * 4 + wq) * 8 + ii * 4 + j) * 64 + lane] = sy;
    }
  unsigned* role_s = reinterpret_cast<unsigned*>(smem + G2_NSLOT * G2_STAGE);
  if (tid == 0) *role_s = __hip_atomic_fetch_add(&cnt[tile * 32], 1u, __ATOMIC_RELAXED, __HIP_MEMORY_SCOPE_AGENT);
  __syncthreads();
  f32x4 mf[2][4], my[2][4];
#pragma unroll
  for (int ii = 0; ii < 2; ++ii)
#pragma unroll
    for (int j = 0; j < 4; ++j) {
      f32x4 ownf = kg ? accf[2 + ii][j] : accf[ii][j];
      f32x4 owny = kg ? acc[2 + ii][j] : acc[ii][j];
      mf[ii][j] = ownf + ex[(((0 * 2 + (kg ^ 1)) * 4 + wq) * 8 + ii * 4 + j) * 64 + lane];
      my[ii][j] = owny;
      if (kz) my[ii][j] += ex[(((1 * 2 + (kg ^ 1)) * 4 + wq) * 8 + ii * 4 + j) * 64 + lane];
    }
  unsigned role = *role_s;
  STAMP(1, 2);
  f32x4* sl = slab + (size_t)tile * 8192;
  if (role == 0) {
    __amdgpu_buffer_rsrc_t rs = __builtin_amdgcn_make_buffer_rsrc((void*)sl, 0, 8192 * 16, 0x00020000);
#pragma unroll
    for (int ii = 0; ii < 2; ++ii)
#pragma unroll
      for (int j = 0; j < 4; ++j) {
        u32x4 v = __builtin_bit_cast(u32x4, mf[ii][j]);
        __builtin_amdgcn_raw_buffer_store_b128(v, rs, ((ii * 4 + j) * 512 + tid) * 16, 0, 16);
        if (kz) {
          u32x4 w = __builtin_bit_cast(u32x4, my[ii][j]);
          __builtin_amdgcn_raw_buffer_store_b128(w, rs, (4096 + (ii * 4 + j) * 512 + tid) * 16, 0, 16);
        }
      }
    asm volatile("s_waitcnt vmcnt(0)" ::: "memory");
    __syncthreads();
    if (tid == 0) __hip_atomic_store(&cnt[tile * 32 + 16], 1u, __ATOMIC_RELAXED, __HIP_MEMORY_SCOPE_AGENT);
    STAMP(1, 3);
    return;
  }
  u16x4 xv[2][4], xnv[2][4], dv[2];
#pragma unroll
  for (int ii = 0; ii < 2; ++ii) {
    int d = tn * 128 + wc * 64 + (kg * 2 + ii) * 16 + fq * 4;
    dv[ii] = *reinterpret_cast<const u16x4*>(dskip + d);
#pragma unroll
    for (int j = 0; j < 4; ++j) {
      int t = tm * 128 + wr * 64 + j * 16 + fr;
      xv[ii][j] = *reinterpret_cast<const u16x4*>(x + (size_t)t * D_ + d);
      xnv[ii][j] = *reinterpret_cast<const u16x4*>(xn + (size_t)t * D_ + d);
    }
  }
  if (tid == 0) {
    while (__hip_atomic_load(&cnt[tile * 32 + 16], __ATOMIC_RELAXED, __HIP_MEMORY_SCOPE_AGENT) == 0u) __builtin_amdgcn_s_sleep(1);
  }
  __syncthreads();
  __builtin_amdgcn_fence(__ATOMIC_ACQUIRE, "wavefront");
  STAMP(1, 3);
  {
    __amdgpu_buffer_rsrc_t rs = __builtin_amdgcn_make_buffer_rsrc((void*)sl, 0, 8192 * 16, 0x00020000);
#pragma unroll
    for (int ii = 0; ii < 2; ++ii)
#pragma unroll
      for (int j = 0; j < 4; ++j) {
        u32x4 v = __builtin_amdgcn_raw_buffer_load_b128(rs, ((ii * 4 + j) * 512 + tid) * 16, 0, 16);
        mf[ii][j] += __builtin_bit_cast(f32x4, v);
        if (!kz) {
          u32x4 w = __builtin_amdgcn_raw_buffer_load_b128(rs, (4096 + (ii * 4 + j) * 512 + tid) * 16, 0, 16);
          my[ii][j] = __builtin_bit_cast(f32x4, w);
        }
      }
  }
#pragma unroll
  for (int ii = 0; ii < 2; ++ii)
#pragma unroll
    for (int j = 0; j < 4; ++j) {
      int t = tm * 128 + wr * 64 + j * 16 + fr;
      int d = tn * 128 + wc * 64 + (kg * 2 + ii) * 16 + fq * 4;
      f32x4 o;
#pragma unroll
      for (int r = 0; r < 4; ++r) {
        float f = rbf(mf[ii][j][r]);
        float g = rbf(f * __builtin_amdgcn_rcpf(1.0f + __expf(-f)));
        float s = 2.0f * my[ii][j][r] + rbf(bf2f(xnv[ii][j][r]) * bf2f(dv[ii][r]));
        o[r] = bf2f(xv[ii][j][r]) + s * g;
      }
      *reinterpret_cast<f32x4*>(out + (size_t)t * D_ + d) = o;
    }
  STAMP(1, 4);
}

extern "C" void kernel_launch(void* const* d_in, const int* in_sizes, int n_in, void* d_out, int out_size, void* d_ws,
                              size_t ws_size, hipStream_t stream) {
  const unsigned short* x = (const unsigned short*)d_in[0];
  const unsigned short* cre = (const unsigned short*)d_in[5];
  const unsigned short* cim = (const unsigned short*)d_in[6];
  const unsigned short* dskip = (const unsigned short*)d_in[8];
  const unsigned short* wdown = (const unsigned short*)d_in[12];
  char* ws = (char*)d_ws;
  unsigned short* xn = (unsigned short*)(ws + OFF_XN);
  unsigned short* a2 = (unsigned short*)(ws + OFF_A2);
  float* out = (float*)d_out;
  G1Args ga;
  ga.xn = xn;
  ga.wgate = (const unsigned short*)d_in[10];
  ga.wup = (const unsigned short*)d_in[11];
  ga.bre = (const unsigned short*)d_in[3];
  ga.bim = (const unsigned short*)d_in[4];
  ga.lre = (const unsigned short*)d_in[1];
  ga.lim = (const unsigned short*)d_in[2];
  ga.logdt = (const unsigned short*)d_in[7];
  ga.wdown = wdown; ga.cre = cre; ga.cim = cim;
  ga.a2 = a2;
  ga.agg = (unsigned long long*)(ws + OFF_AGG);

  PrepArgs pa; pa.x = x; pa.wdown = wdown; pa.cre = cre; pa.cim = cim; pa.wgate = ga.wgate; pa.wup = ga.wup; pa.bre = ga.bre; pa.bim = ga.bim; pa.ws = ws;
  k_prep<<<PB_LN, 256, 0, stream>>>(pa);
  (void)hipFuncSetAttribute((const void*)k_gemm1, hipFuncAttributeMaxDynamicSharedMemorySize, G1_LDS);
  k_gemm1<<<256, 512, G1_LDS, stream>>>(ga);
  (void)hipFuncSetAttribute((const void*)k_gemm2, hipFuncAttributeMaxDynamicSharedMemorySize, G2_LDS);
  k_gemm2<<<256, 512, G2_LDS, stream>>>(a2, wdown, cre, cim, x, xn, dskip, out, (f32x4*)(ws + OFF_SLAB), (unsigned*)(ws + OFF_CNT));
}
```

```cpp
#include <hip/hip_runtime.h>
#include <hip/hip_bf16.h>
#include <stdint.h>

typedef __attribute__((ext_vector_type(8))) short bf16x8;
typedef __attribute__((ext_vector_type(4))) float f32x4;
typedef __attribute__((ext_vector_type(4))) unsigned short u16x4;
typedef __attribute__((ext_vector_type(4))) unsigned u32x4;

constexpr int S_ = 2048;
constexpr int D_ = 1024;
constexpr int P_ = 1024;
constexpr int F_ = 3584;
constexpr int N1_ = 9216;
constexpr int K2_ = 5632;

constexpr size_t OFF_AGG = 0;
constexpr size_t OFF_CNT = OFF_AGG + 32 * 8 * 32 * 2 * 8;
constexpr size_t SYNC_BYTES = OFF_CNT + 128 * 32 * 4;
constexpr size_t OFF_XN = SYNC_BYTES;
constexpr size_t OFF_A2 = OFF_XN + (size_t)S_ * D_ * 2;
constexpr size_t OFF_SLAB = OFF_A2 + (size_t)S_ * K2_ * 2;

__device__ __forceinline__ float bf2f(unsigned short v) { return __uint_as_float(((unsigned)v) << 16); }
__device__ __forceinline__ unsigned short f2bf(float f) {
  __bf16 b = (__bf16)f;
  return __builtin_bit_cast(unsigned short, b);
}
typedef __attribute__((ext_vector_type(2))) __bf16 bf16x2n;
typedef __attribute__((ext_vector_type(2))) float f32x2;
__device__ __forceinline__ unsigned pk2bf(float a, float b) {
  f32x2 v = {a, b};
  bf16x2n r = __builtin_convertvector(v, bf16x2n);
  return __builtin_bit_cast(unsigned, r);
}
__device__ __forceinline__ float lo2f(unsigned u) { return __uint_as_float(u << 16); }
__device__ __forceinline__ float hi2f(unsigned u) { return __uint_as_float(u & 0xFFFF0000u); }
__device__ __forceinline__ float rbf(float f) { return bf2f(f2bf(f)); }
__device__ __forceinline__ float2 cmul(float2 a, float2 b) { return make_float2(a.x * b.x - a.y * b.y, a.x * b.y + a.y * b.x); }

#define STAMP(k, i)
#define CSTAMP(k, i)
#define WAIT_VM(n) asm volatile("s_waitcnt vmcnt(" #n ")" ::: "memory")
#define LDS_PTR(p) ((__attribute__((address_space(3))) unsigned*)(p))

constexpr int PB_LN = 512;
#define PF_ALL 0
constexpr int PF_N = 6;
struct PrepArgs { const unsigned short *x, *wdown, *cre, *cim, *wgate, *wup, *bre, *bim; char* ws; };
__global__ __launch_bounds__(256) void k_prep(PrepArgs pa) {
  const unsigned short* __restrict__ x = pa.x; char* __restrict__ ws = pa.ws;
  int b = blockIdx.x, tid = threadIdx.x, wave = tid >> 6, lane = tid & 63;
  unsigned short* xn = (unsigned short*)(ws + OFF_XN);
  for (int i = b * 256 + tid; i < (int)(SYNC_BYTES / 16); i += PB_LN * 256)
    reinterpret_cast<u32x4*>(ws)[i] = (u32x4){0u, 0u, 0u, 0u};
  int row = b * 4 + wave;
  const bf16x8* src = reinterpret_cast<const bf16x8*>(x + (size_t)row * D_);
  bf16x8 v0 = src[lane], v1 = src[64 + lane];
  float f[16];
#pragma unroll
  for (int j = 0; j < 8; ++j) { f[j] = bf2f((unsigned short)v0[j]); f[8 + j] = bf2f((unsigned short)v1[j]); }
  float s = 0.f;
#pragma unroll
  for (int j = 0; j < 16; ++j) s += f[j];
#pragma unroll
  for (int o = 32; o >= 1; o >>= 1) s += __shfl_xor(s, o);
  float mean = s * (1.0f / D_);
  float q = 0.f;
#pragma unroll
  for (int j = 0; j < 16; ++j) { float d = f[j] - mean; q += d * d; }
#pragma unroll
  for (int o = 32; o >= 1; o >>= 1) q += __shfl_xor(q, o);
  float var = q * (1.0f / D_);
  float mb = rbf(mean), vb = rbf(var);
  float den = rbf(sqrtf(rbf(vb + 1e-5f)));
  bf16x8 o0, o1;
#pragma unroll
  for (int j = 0; j < 8; ++j) {
    o0[j] = (short)f2bf(rbf(f[j] - mb) / den);
    o1[j] = (short)f2bf(rbf(f[8 + j] - mb) / den);
  }
  bf16x8* dst = reinterpret_cast<bf16x8*>(xn + (size_t)row * D_);
  dst[lane] = o0; dst[64 + lane] = o1;
}

typedef __attribute__((ext_vector_type(4))) short s16x4;
template <int OFF2>
__device__ __forceinline__ bf16x8 tr_frag(unsigned lds_addr) {
  s16x4 lo, hi;
  asm volatile("ds_read_b64_tr_b16 %0, %2\n\tds_read_b64_tr_b16 %1, %2 offset:%c3"
               : "=&v"(lo), "=&v"(hi) : "v"(lds_addr), "i"(OFF2));
  return __builtin_shufflevector(lo, hi, 0, 1, 2, 3, 4, 5, 6, 7);
}
#define TR_WAIT() do { asm volatile("s_waitcnt lgkmcnt(0)" ::: "memory"); __builtin_amdgcn_sched_barrier(0); } while (0)
__device__ __forceinline__ unsigned lds_u32(const void* p) {
  return (unsigned)(uintptr_t)(__attribute__((address_space(3))) const char*)p;
}

__device__ __forceinline__ float gelu_tanh(float x) {
  float w = x * __builtin_fmaf(x * x, -0.10294323970650479f, -2.3022081985952363f);
  return x * __builtin_amdgcn_rcpf(1.0f + __builtin_amdgcn_exp2f(w));
}

constexpr int G1_AT = 256 * 64;
constexpr int G1_WT = 288 * 64;
constexpr int G1_STAGE = G1_AT + G1_WT;
constexpr int G1_NSLOT = 4;
constexpr int G1_DUMMY = G1_NSLOT * G1_STAGE;
constexpr int G1_COEF = G1_DUMMY + 1024;
constexpr int G1_LDS = G1_COEF + 512;
constexpr int E_BUL = 0;
constexpr int E_ACT = 69632;
constexpr int E_CAG = 131072;
constexpr int E_TERM = 135168;
constexpr int E_MISC = 137216;

struct G1Args {
  const unsigned short *xn, *wgate, *wup, *bre, *bim, *lre, *lim, *logdt, *wdown, *cre, *cim;
  unsigned short* a2;
  unsigned long long* agg;
};
template <int V> struct IC { static constexpr int value = V; };

__global__ __launch_bounds__(512, 2) void k_gemm1(G1Args ga) {
  extern __shared__ __attribute__((aligned(16))) char smem[];
  int tid = threadIdx.x, wid = __builtin_amdgcn_readfirstlane(tid >> 6), lane = tid & 63;
  int xl = blockIdx.x & 7, loc = blockIdx.x >> 3;
  STAMP(0, 0);
  int tn = xl * 4 + (loc >> 3), tm = loc & 7;
  int wr = wid >> 1, wc = wid & 1;
  int fr = lane & 15, fq = lane >> 4;
  const unsigned short* xg = ga.xn + (size_t)tm * 256 * D_;
  unsigned short* a2 = ga.a2;
  unsigned long long* agg = ga.agg;

  f32x4 acc[9][4];
#pragma unroll
  for (int i = 0; i < 9; ++i)
#pragma unroll
    for (int j = 0; j < 4; ++j) acc[i][j] = (f32x4){0.f, 0.f, 0.f, 0.f};

  int srow = lane >> 2;
  int schunk = (lane & 3) ^ (((srow >> 3) & 1) << 1);
  const char* wsrc[3]; int wstr[3];
#pragma unroll
  for (int i = 0; i < 3; ++i) {
    int reg = (i < 2) ? wid * 2 + i : 16 + (wid & 1);
    const unsigned short* ptr; int str;
    if (reg < 12) {
      int Q = reg >> 2, kb = reg & 3;
      int r = lane >> 3, c = (lane & 7) ^ (((r >> 1) & 1) << 1) ^ ((kb & 1) << 2);
      int k = kb * 8 + r;
      const unsigned short* mat; int col;
      if (Q == 0) { mat = ga.wgate; col = c * 8; }
      else if (Q == 1) { mat = ga.wup; col = c * 8; }
      else if (c < 6) { mat = ga.wgate; col = 64 + c * 8; }
      else { mat = ga.wup; col = 64 + (c - 6) * 8; }
      ptr = mat + (size_t)k * F_ + tn * 112 + col; str = F_ * 64;
    } else if (reg < 14) {
      int r = lane >> 3, c = (lane & 7) ^ (((r >> 1) & 1) << 1);
      int k = ((reg - 12) * 2 + (c >> 2)) * 8 + r;
      ptr = ga.wup + (size_t)k * F_ + tn * 112 + 80 + (c & 3) * 8; str = F_ * 64;
    } else {
      int row = (reg - 14) * 16 + srow;
      const unsigned short* mat = (row < 32) ? ga.bre : ga.bim;
      ptr = mat + (size_t)(tn * 32 + (row & 31)) * D_ + schunk * 8; str = 64;
    }
    wsrc[i] = (const char*)ptr; wstr[i] = str;
  }
  auto stage = [&](int slot, int ks) {
    char* base = smem + slot * G1_STAGE;
#pragma unroll
    for (int i = 0; i < 2; ++i) {
      int ins = wid * 2 + i;
      __builtin_amdgcn_global_load_lds((const unsigned*)(xg + (size_t)(ins * 16 + srow) * D_ + ks * 32 + schunk * 8),
                                       LDS_PTR(base + ins * 1024), 16, 0, 0);
    }
#pragma unroll
    for (int i = 0; i < 2; ++i)
      __builtin_amdgcn_global_load_lds((const unsigned*)(wsrc[i] + (size_t)ks * wstr[i]), LDS_PTR(base + G1_AT + (wid * 2 + i) * 1024), 16, 0, 0);
    if (wid < 2)
      __builtin_amdgcn_global_load_lds((const unsigned*)(wsrc[2] + (size_t)ks * wstr[2]), LDS_PTR(base + G1_AT + (16 + wid) * 1024), 16, 0, 0);
  };

  auto stage_piece = [&](int slot, int ks, int pc) {
    char* base = smem + slot * G1_STAGE;
    if (pc < 2) {
      int ins = wid * 2 + pc;
      __builtin_amdgcn_global_load_lds((const unsigned*)(xg + (size_t)(ins * 16 + srow) * D_ + ks * 32 + schunk * 8),
                                       LDS_PTR(base + ins * 1024), 16, 0, 0);
    } else if (pc < 4) {
      int i = pc - 2;
      __builtin_amdgcn_global_load_lds((const unsigned*)(wsrc[i] + (size_t)ks * wstr[i]), LDS_PTR(base + G1_AT + (wid * 2 + i) * 1024), 16, 0, 0);
    } else if (wid < 2) {
      __builtin_amdgcn_global_load_lds((const unsigned*)(wsrc[2] + (size_t)ks * wstr[2]), LDS_PTR(base + G1_AT + (16 + wid) * 1024), 16, 0, 0);
    }
  };
  float c_lr = 0.f, c_li = 0.f, c_ld = 0.f;
  if (tid >= 448 && tid < 480) {
    int p = tn * 32 + (tid - 448);
    c_lr = bf2f(ga.lre[p]); c_li = bf2f(ga.lim[p]); c_ld = bf2f(ga.logdt[p]);
  }
  asm volatile("s_waitcnt vmcnt(0)" ::: "memory");
  const int ns = D_ / 32;
  stage(0, 0); stage(1, 1); stage(2, 2); stage(3, 3);
  if (tid >= 448 && tid < 480) {
    float lr = c_lr, li = c_li;
    float dt = rbf(expf(c_ld));
    float zr = dt * lr, zi = dt * li;
    float e = expf(zr);
    float lx = e * cosf(zi), ly = e * sinf(zi);
    float nr = lx - 1.0f, ni = ly;
    float den = lr * lr + li * li;
    *reinterpret_cast<f32x4*>(smem + G1_COEF + (tid - 448) * 16) = (f32x4){lx, ly, (nr * lr + ni * li) / den, (ni * lr - nr * li) / den};
  }
  int rchunk = fq ^ (((fr >> 3) & 1) << 1);
  const int xoff = (wr * 64 + fr) * 64 + rchunk * 16;
  const int boff = G1_AT + 14 * 1024 + fr * 64 + rchunk * 16;
  const unsigned sbase = lds_u32(smem);
  auto mainloop = [&](auto WCc) {
    constexpr int WC = decltype(WCc)::value;
    constexpr int NTR = WC ? 6 : 8;
    int kb = fq, q = (lane >> 2) & 3, pp = lane & 3;
    int sw = ((q >> 1) << 1) ^ ((kb & 1) << 2);
    int wo[NTR];
#pragma unroll
    for (int i = 0; i < NTR; ++i) {
      if (WC == 0) wo[i] = G1_AT + ((i >> 2) * 4 + kb) * 1024 + q * 128 + ((((i & 3) * 2 + (pp >> 1)) ^ sw) * 16) + (pp & 1) * 8;
      else if (i < 4) wo[i] = G1_AT + (8 + kb) * 1024 + q * 128 + (((i * 2 + (pp >> 1)) ^ sw) * 16) + (pp & 1) * 8;
      else wo[i] = G1_AT + (12 + (kb >> 1)) * 1024 + q * 128 + ((((kb & 1) * 4 + (i - 4) * 2 + (pp >> 1)) ^ ((q >> 1) << 1)) * 16) + (pp & 1) * 8;
    }
    unsigned wpk[NTR / 2];
#pragma unroll
    for (int i = 0; i < NTR / 2; ++i) wpk[i] = (unsigned)wo[2 * i] | ((unsigned)wo[2 * i + 1] << 16);
    auto ldw = [&](int i, const char* nb, unsigned nbu) -> bf16x8 {
      if (i < NTR) return tr_frag<512>(nbu + ((i & 1) ? (wpk[i >> 1] >> 16) : (wpk[i >> 1] & 0xFFFFu)));
      return *reinterpret_cast<const bf16x8*>(nb + boff + (WC ? (i - NTR + 1) : 0) * 1024);
    };
    bf16x8 xa_[4], xb_[4], wf[9];
    if (wid < 2) WAIT_VM(15); else WAIT_VM(12);
    __builtin_amdgcn_s_barrier();
#pragma unroll
    for (int j = 0; j < 4; ++j) xa_[j] = *reinterpret_cast<const bf16x8*>(smem + xoff + j * 1024);
#pragma unroll
    for (int i = 0; i < 9; ++i) wf[i] = ldw(i, smem, sbase);

    auto body = [&](int s, bf16x8 (&xc)[4], bf16x8 (&xnx)[4]) {
      if (wid < 2) WAIT_VM(10); else WAIT_VM(8);
      __builtin_amdgcn_s_barrier();
      const char* nb = smem + ((s + 1) & 3) * G1_STAGE;
      unsigned nbu = sbase + ((s + 1) & 3) * G1_STAGE;
      int ksn = min(s + 4, ns - 1);
      TR_WAIT();
#pragma unroll
      for (int i = 0; i < 9; ++i) {
#pragma unroll
        for (int j = 0; j < 4; ++j) acc[i][j] = __builtin_amdgcn_mfma_f32_16x16x32_bf16(wf[i], xc[j], acc[i][j], 0, 0, 0);
        wf[i] = ldw(i, nb, nbu);
        if (i < 4) xnx[i] = *reinterpret_cast<const bf16x8*>(nb + xoff + i * 1024);
        if (i >= 1 && i <= 5) stage_piece(s & 3, ksn, i - 1);
      }
#pragma unroll
      for (int i = 0; i < 9; ++i) {
        __builtin_amdgcn_sched_group_barrier(0x008, 4, 0);
        __builtin_amdgcn_sched_group_barrier(0x100, 1, 0);
      }
    };
    STAMP(0, 10); CSTAMP(0, 11);
    for (int s = 0; s < ns; s += 2) {
      body(s, xa_, xb_);
      body(s + 1, xb_, xa_);
    }
    STAMP(0, 12); CSTAMP(0, 13);
  };
  if (wc == 0) mainloop(IC<0>{}); else mainloop(IC<1>{});

  WAIT_VM(0);
  __syncthreads();
  STAMP(0, 1);
  float* bul = reinterpret_cast<float*>(smem + E_BUL);
  unsigned short* actl = reinterpret_cast<unsigned short*>(smem + E_ACT);
  float2* cag = reinterpret_cast<float2*>(smem + E_CAG);
  float2* term = reinterpret_cast<float2*>(smem + E_TERM);
  int tl = wr * 64 + fr;
  if (wc == 0) {
#pragma unroll
    for (int j = 0; j < 4; ++j) *reinterpret_cast<f32x4*>(bul + (tl + j * 16) * 68 + fq * 4) = acc[8][j];
  } else {
#pragma unroll
    for (int j = 0; j < 4; ++j) {
      *reinterpret_cast<f32x4*>(bul + (tl + j * 16) * 68 + 16 + fq * 4) = acc[6][j];
      *reinterpret_cast<f32x4*>(bul + (tl + j * 16) * 68 + 32 + fq * 4) = acc[7][j];
      *reinterpret_cast<f32x4*>(bul + (tl + j * 16) * 68 + 48 + fq * 4) = acc[8][j];
    }
  }
  __syncthreads();
  int ch = tid & 31, ck = tid >> 5;
  int p = tn * 32 + ch;
  float2 lam, cf;
  {
    f32x4 lc = *reinterpret_cast<const f32x4*>(smem + G1_COEF + ch * 16);
    lam = make_float2(lc[0], lc[1]); cf = make_float2(lc[2], lc[3]);
  }
  float hr[16], hi[16];
  {
    float sr = 0.f, si = 0.f;
#pragma unroll
    for (int i = 0; i < 16; ++i) {
      float rr = bul[(ck * 16 + i) * 68 + ch], ri = bul[(ck * 16 + i) * 68 + 32 + ch];
      float br = cf.x * rr - cf.y * ri, bi = cf.x * ri + cf.y * rr;
      float nr = lam.x * sr - lam.y * si + br;
      float ni = lam.x * si + lam.y * sr + bi;
      sr = nr; si = ni; hr[i] = sr; hi[i] = si;
    }
    cag[ck * 32 + ch] = make_float2(sr, si);
  }
  float2 l16 = lam;
#pragma unroll
  for (int i = 0; i < 4; ++i) l16 = cmul(l16, l16);
  float2 l256 = l16;
#pragma unroll
  for (int i = 0; i < 4; ++i) l256 = cmul(l256, l256);
  __syncthreads();
  if (tid < 32) {
    float2 c = make_float2(0.f, 0.f);
#pragma unroll
    for (int k = 0; k < 16; ++k) {
      float2 aa = cag[k * 32 + ch];
      cag[k * 32 + ch] = c;
      float2 t2 = cmul(l16, c);
      c = make_float2(t2.x + aa.x, t2.y + aa.y);
    }
    if (tm < 7) {
      unsigned long long* g = agg + ((size_t)(tn * 8 + tm) * 32 + ch) * 2;
      __hip_atomic_store(g, (1ull << 32) | (unsigned long long)__float_as_uint(c.x), __ATOMIC_RELAXED, __HIP_MEMORY_SCOPE_AGENT);
      __hip_atomic_store(g + 1, (1ull << 32) | (unsigned long long)__float_as_uint(c.y), __ATOMIC_RELAXED, __HIP_MEMORY_SCOPE_AGENT);
    }
  }
  STAMP(0, 2);
  unsigned long long pv0 = 0ull, pv1 = 0ull;
  if (ck < tm) {
    const unsigned long long* g = agg + ((size_t)(tn * 8 + ck) * 32 + ch) * 2;
    pv0 = __hip_atomic_load(g, __ATOMIC_RELAXED, __HIP_MEMORY_SCOPE_AGENT);
    pv1 = __hip_atomic_load(g + 1, __ATOMIC_RELAXED, __HIP_MEMORY_SCOPE_AGENT);
  }
#pragma unroll
  for (int i = 0; i < 6; ++i) {
    int g = min((blockIdx.x * 8 + wid) * 6 + i, 11263);
    const unsigned short* src = (g < 7168) ? ga.wdown + (size_t)g * 512 : (g < 9216 ? ga.cre + (size_t)(g - 7168) * 512 : ga.cim + (size_t)(g - 9216) * 512);
    __builtin_amdgcn_global_load_lds((const unsigned*)(src + lane * 8), LDS_PTR(smem + G1_DUMMY), 16, 0, 0);
  }
  auto geglu4 = [&](const f32x4& gt, const f32x4& up) -> uint2 {
    unsigned g01 = pk2bf(gt[0], gt[1]), g23 = pk2bf(gt[2], gt[3]);
    unsigned u01 = pk2bf(up[0], up[1]), u23 = pk2bf(up[2], up[3]);
    unsigned a01 = pk2bf(gelu_tanh(lo2f(g01)), gelu_tanh(hi2f(g01)));
    unsigned a23 = pk2bf(gelu_tanh(lo2f(g23)), gelu_tanh(hi2f(g23)));
    uint2 o;
    o.x = pk2bf(lo2f(a01) * lo2f(u01), hi2f(a01) * hi2f(u01));
    o.y = pk2bf(lo2f(a23) * lo2f(u23), hi2f(a23) * hi2f(u23));
    return o;
  };
  if (wc == 0) {
#pragma unroll
    for (int i = 0; i < 4; ++i)
#pragma unroll
      for (int j = 0; j < 4; ++j)
        *reinterpret_cast<uint2*>(actl + (tl + j * 16) * 120 + i * 16 + fq * 4) = geglu4(acc[i][j], acc[i + 4][j]);
  } else {
#pragma unroll
    for (int i = 0; i < 3; ++i)
#pragma unroll
      for (int j = 0; j < 4; ++j)
        *reinterpret_cast<uint2*>(actl + (tl + j * 16) * 120 + 64 + i * 16 + fq * 4) = geglu4(acc[i][j], acc[i + 3][j]);
  }
  __syncthreads();
  STAMP(0, 3);
#pragma unroll
  for (int it = 0; it < 7; ++it) {
    int idx = it * 512 + tid;
    int row = idx / 14, c = idx - row * 14;
    bf16x8 v = *reinterpret_cast<const bf16x8*>(actl + row * 120 + c * 8);
    *reinterpret_cast<bf16x8*>(a2 + (size_t)(tm * 256 + row) * K2_ + tn * 112 + c * 8) = v;
  }
  STAMP(0, 4);
  if (ck < tm) {
    const unsigned long long* g = agg + ((size_t)(tn * 8 + ck) * 32 + ch) * 2;
    unsigned long long v0 = pv0, v1 = pv1;
    while (!((v0 >> 32) == 1ull && (v1 >> 32) == 1ull)) {
      __builtin_amdgcn_s_sleep(1);
      v0 = __hip_atomic_load(g, __ATOMIC_RELAXED, __HIP_MEMORY_SCOPE_AGENT);
      v1 = __hip_atomic_load(g + 1, __ATOMIC_RELAXED, __HIP_MEMORY_SCOPE_AGENT);
    }
    float2 av = make_float2(__uint_as_float((unsigned)v0), __uint_as_float((unsigned)v1));
    for (int e = tm - 1 - ck; e > 0; --e) av = cmul(av, l256);
    term[ck * 32 + ch] = av;
  }
  __syncthreads();
  STAMP(0, 5);
  float2 bc = make_float2(0.f, 0.f);
  for (int j = 0; j < tm; ++j) { float2 tv = term[j * 32 + ch]; bc.x += tv.x; bc.y += tv.y; }
  float2 pw = make_float2(1.f, 0.f);
  for (int e = 0; e < ck; ++e) pw = cmul(pw, l16);
  float2 c0 = cag[ck * 32 + ch];
  float2 t3 = cmul(pw, bc);
  c0.x += t3.x; c0.y += t3.y;
  unsigned short* hl = reinterpret_cast<unsigned short*>(smem + E_BUL);
  {
    float2 pq = lam;
#pragma unroll
    for (int i = 0; i < 16; ++i) {
      float vr = hr[i] + pq.x * c0.x - pq.y * c0.y;
      float vi = hi[i] + pq.x * c0.y + pq.y * c0.x;
      hl[(ck * 16 + i) * 72 + ch] = f2bf(vr);
      hl[(ck * 16 + i) * 72 + 32 + ch] = f2bf(-vi);
      pq = cmul(pq, lam);
    }
  }
  __syncthreads();
#pragma unroll
  for (int it = 0; it < 4; ++it) {
    int idx = it * 512 + tid;
    int row = idx >> 3, c = idx & 7;
    bf16x8 v = *reinterpret_cast<const bf16x8*>(hl + row * 72 + c * 8);
    *reinterpret_cast<bf16x8*>(a2 + (size_t)(tm * 256 + row) * K2_ + F_ + (c >> 2) * P_ + tn * 32 + (c & 3) * 8) = v;
  }
  STAMP(0, 6);
}

constexpr int G2_S0 = 44;
constexpr int G2_STAGE = 32768;
constexpr int G2_NSLOT = 4;
constexpr int G2_LDS = G2_NSLOT * G2_STAGE + 16;
__global__ __launch_bounds__(512, 2) void k_gemm2(const unsigned short* __restrict__ a2, const unsigned short* __restrict__ wdown,
                                                  const unsigned short* __restrict__ cre, const unsigned short* __restrict__ cim,
                                                  const unsigned short* __restrict__ x,
                                                  const unsigned short* __restrict__ xn, const unsigned short* __restrict__ dskip,
                                                  float* __restrict__ out,
                                                  f32x4* __restrict__ slab, unsigned* __restrict__ cnt) {
  extern __shared__ __attribute__((aligned(16))) char smem[];
  int bid = blockIdx.x;
  int xcd = bid & 7, loc = bid >> 3;
  int tm = (xcd >> 1) * 4 + ((loc >> 3) & 3), tn = (xcd & 1) * 4 + ((loc >> 1) & 3), kz = loc & 1;
  int tile = tm * 8 + tn;
  STAMP(1, 0);
  int tid = threadIdx.x, wid = __builtin_amdgcn_readfirstlane(tid >> 6), lane = tid & 63;
  int kg = wid >> 2, wr = (wid >> 1) & 1, wc = wid & 1, fr = lane & 15, fq = lane >> 4;
  const unsigned short* ag = a2 + (size_t)tm * 128 * K2_ + kz * (G2_S0 * 64);
  f32x4 acc[4][4], accf[4][4];
#pragma unroll
  for (int i = 0; i < 4; ++i)
#pragma unroll
    for (int j = 0; j < 4; ++j) { acc[i][j] = (f32x4){0, 0, 0, 0}; accf[i][j] = (f32x4){0, 0, 0, 0}; }
  int srow = lane >> 3, schunk = (lane & 7) ^ (srow & 7);
  int wlofs[2];
#pragma unroll
  for (int i = 0; i < 2; ++i) {
    int reg = wid * 2 + i, Q = reg >> 3, kb = reg & 7;
    int r = lane >> 3, c = (lane & 7) ^ (((r >> 1) & 1) << 1) ^ ((kb & 1) << 2);
    wlofs[i] = (kb * 8 + r) * D_ + tn * 128 + Q * 64 + c * 8;
  }
  auto stage = [&](int slot, int kt) {
    char* base = smem + slot * G2_STAGE;
    int kabs = kz * (G2_S0 * 64) + kt * 64;
    bool ffn = kabs < F_;
    const unsigned short* wb = ffn ? wdown + (size_t)kabs * D_ : ((kabs < F_ + P_) ? cre + (kabs - F_) : cim + (kabs - F_ - P_));
#pragma unroll
    for (int i = 0; i < 2; ++i) {
      int ins = wid * 2 + i;
      __builtin_amdgcn_global_load_lds((const unsigned*)(ag + (size_t)(ins * 8 + srow) * K2_ + kt * 64 + schunk * 8),
                                       LDS_PTR(base + ins * 1024), 16, 0, 0);
      __builtin_amdgcn_global_load_lds((const unsigned*)(wb + (ffn ? wlofs[i] : (tn * 128 + ins * 8 + srow) * P_ + schunk * 8)),
                                       LDS_PTR(base + 16384 + ins * 1024), 16, 0, 0);
    }
  };
  auto stage_piece = [&](int slot, int kt, int pc) {
    char* base = smem + slot * G2_STAGE;
    int ins = wid * 2 + (pc >> 1);
    if ((pc & 1) == 0) {
      __builtin_amdgcn_global_load_lds((const unsigned*)(ag + (size_t)(ins * 8 + srow) * K2_ + kt * 64 + schunk * 8),
                                       LDS_PTR(base + ins * 1024), 16, 0, 0);
    } else {
      int kabs = kz * (G2_S0 * 64) + kt * 64;
      bool ffn = kabs < F_;
      const unsigned short* wb = ffn ? wdown + (size_t)kabs * D_ : ((kabs < F_ + P_) ? cre + (kabs - F_) : cim + (kabs - F_ - P_));
      __builtin_amdgcn_global_load_lds((const unsigned*)(wb + (ffn ? wlofs[pc >> 1] : (tn * 128 + ins * 8 + srow) * P_ + schunk * 8)),
                                       LDS_PTR(base + 16384 + ins * 1024), 16, 0, 0);
    }
  };
  const int ns = kz ? 88 - G2_S0 : G2_S0;
  const int nsf = kz ? 56 - G2_S0 : G2_S0;
  stage(0, 0); stage(1, 1); stage(2, 2); stage(3, 3);
  int rch = ((kg * 4 + fq) ^ (fr & 7)) * 16;
  const int xoff = (wr * 64 + fr) * 128 + rch;
  const int woffc = 16384 + (wc * 64 + fr) * 128 + rch;
  int wofs[4];
  {
    int kb = kg * 4 + fq, q = (lane >> 2) & 3, pp = lane & 3;
    int sw = ((q >> 1) << 1) ^ ((kb & 1) << 2);
#pragma unroll
    for (int i = 0; i < 4; ++i) wofs[i] = 16384 + (wc * 8 + kb) * 1024 + q * 128 + (((i * 2 + (pp >> 1)) ^ sw) * 16) + (pp & 1) * 8;
  }
  bf16x8 xa_[4], xb_[4], wa_[4], wb_[4];
  const unsigned sbase = lds_u32(smem);
  WAIT_VM(12);
  __builtin_amdgcn_s_barrier();
#pragma unroll
  for (int j = 0; j < 4; ++j) {
    xa_[j] = *reinterpret_cast<const bf16x8*>(smem + xoff + j * 2048);
    wa_[j] = tr_frag<512>(sbase + wofs[j]);
  }
  auto body = [&](auto NTRc, int s, bf16x8 (&xc)[4], bf16x8 (&wcur)[4], bf16x8 (&xnx)[4], bf16x8 (&wnx)[4]) {
    constexpr int NTR = decltype(NTRc)::value;
    WAIT_VM(8);
    __builtin_amdgcn_s_barrier();
    const char* nb = smem + ((s + 1) & 3) * G2_STAGE;
    unsigned nbu = sbase + ((s + 1) & 3) * G2_STAGE;
    int ktn = min(s + 4, ns - 1);
    TR_WAIT();
#pragma unroll
    for (int i = 0; i < 4; ++i) {
#pragma unroll
      for (int j = 0; j < 4; ++j) acc[i][j] = __builtin_amdgcn_mfma_f32_16x16x32_bf16(wcur[i], xc[j], acc[i][j], 0, 0, 0);
      if (NTR) wnx[i] = tr_frag<512>(nbu + wofs[i]);
      else wnx[i] = *reinterpret_cast<const bf16x8*>(nb + woffc + i * 2048);
      xnx[i] = *reinterpret_cast<const bf16x8*>(nb + xoff + i * 2048);
      stage_piece(s & 3, ktn, i);
    }
#pragma unroll
    for (int i = 0; i < 4; ++i) {
      __builtin_amdgcn_sched_group_barrier(0x008, 4, 0);
      __builtin_amdgcn_sched_group_barrier(0x100, 3, 0);
      __builtin_amdgcn_sched_group_barrier(0x020, 1, 0);
    }
    if (s + 1 == nsf) {
#pragma unroll
      for (int i = 0; i < 4; ++i)
#pragma unroll
        for (int j = 0; j < 4; ++j) { accf[i][j] = acc[i][j]; acc[i][j] = (f32x4){0, 0, 0, 0}; }
    }
  };
  STAMP(1, 10); CSTAMP(1, 11);
  for (int s = 0; s < nsf - 2; s += 2) {
    body(IC<1>{}, s, xa_, wa_, xb_, wb_);
    body(IC<1>{}, s + 1, xb_, wb_, xa_, wa_);
  }
  body(IC<1>{}, nsf - 2, xa_, wa_, xb_, wb_);
  body(IC<0>{}, nsf - 1, xb_, wb_, xa_, wa_);
  for (int s = nsf; s < ns; s += 2) {
    body(IC<0>{}, s, xa_, wa_, xb_, wb_);
    body(IC<0>{}, s + 1, xb_, wb_, xa_, wa_);
  }
  WAIT_VM(0);
  STAMP(1, 12); CSTAMP(1, 13);
  __syncthreads();
  STAMP(1, 1);
  f32x4* ex = reinterpret_cast<f32x4*>(smem);
  int wq = wid & 3;
#pragma unroll
  for (int ii = 0; ii < 2; ++ii)
#pragma unroll
    for (int j = 0; j < 4; ++j) {
      f32x4 sf = kg ? accf[ii][j] : accf[2 + ii][j];
      f32x4 sy = kg ? acc[ii][j] : acc[2 + ii][j];
      ex[(((0 * 2 + kg) * 4 + wq) * 8 + ii * 4 + j) * 64 + lane] = sf;
      if (kz) ex[(((1 * 2 + kg) * 4 + wq) * 8 + ii * 4 + j) * 64 + lane] = sy;
    }
  unsigned* role_s = reinterpret_cast<unsigned*>(smem + G2_NSLOT * G2_STAGE);
  if (tid == 0) *role_s = __hip_atomic_fetch_add(&cnt[tile * 32], 1u, __ATOMIC_RELAXED, __HIP_MEMORY_SCOPE_AGENT);
  __syncthreads();
  f32x4 mf[2][4], my[2][4];
#pragma unroll
  for (int ii = 0; ii < 2; ++ii)
#pragma unroll
    for (int j = 0; j < 4; ++j) {
      f32x4 ownf = kg ? accf[2 + ii][j] : accf[ii][j];
      f32x4 owny = kg ? acc[2 + ii][j] : acc[ii][j];
      mf[ii][j] = ownf + ex[(((0 * 2 + (kg ^ 1)) * 4 + wq) * 8 + ii * 4 + j) * 64 + lane];
      my[ii][j] = owny;
      if (kz) my[ii][j] += ex[(((1 * 2 + (kg ^ 1)) * 4 + wq) * 8 + ii * 4 + j) * 64 + lane];
    }
  unsigned role = *role_s;
  STAMP(1, 2);
  f32x4* sl = slab + (size_t)tile * 8192;
  if (role == 0) {
    __amdgpu_buffer_rsrc_t rs = __builtin_amdgcn_make_buffer_rsrc((void*)sl, 0, 8192 * 16, 0x00020000);
#pragma unroll
    for (int ii = 0; ii < 2; ++ii)
#pragma unroll
      for (int j = 0; j < 4; ++j) {
        u32x4 v = __builtin_bit_cast(u32x4, mf[ii][j]);
        __builtin_amdgcn_raw_buffer_store_b128(v, rs, ((ii * 4 + j) * 512 + tid) * 16, 0, 16);
        if (kz) {
          u32x4 w = __builtin_bit_cast(u32x4, my[ii][j]);
          __builtin_amdgcn_raw_buffer_store_b128(w, rs, (4096 + (ii * 4 + j) * 512 + tid) * 16, 0, 16);
        }
      }
    asm volatile("s_waitcnt vmcnt(0)" ::: "memory");
    __syncthreads();
    if (tid == 0) __hip_atomic_store(&cnt[tile * 32 + 16], 1u, __ATOMIC_RELAXED, __HIP_MEMORY_SCOPE_AGENT);
    STAMP(1, 3);
    return;
  }
  u16x4 xv[2][4], xnv[2][4], dv[2];
#pragma unroll
  for (int ii = 0; ii < 2; ++ii) {
    int d = tn * 128 + wc * 64 + (kg * 2 + ii) * 16 + fq * 4;
    dv[ii] = *reinterpret_cast<const u16x4*>(dskip + d);
#pragma unroll
    for (int j = 0; j < 4; ++j) {
      int t = tm * 128 + wr * 64 + j * 16 + fr;
      xv[ii][j] = *reinterpret_cast<const u16x4*>(x + (size_t)t * D_ + d);
      xnv[ii][j] = *reinterpret_cast<const u16x4*>(xn + (size_t)t * D_ + d);
    }
  }
  if (tid == 0) {
    while (__hip_atomic_load(&cnt[tile * 32 + 16], __ATOMIC_RELAXED, __HIP_MEMORY_SCOPE_AGENT) == 0u) __builtin_amdgcn_s_sleep(1);
  }
  __syncthreads();
  __builtin_amdgcn_fence(__ATOMIC_ACQUIRE, "wavefront");
  STAMP(1, 3);
  {
    __amdgpu_buffer_rsrc_t rs = __builtin_amdgcn_make_buffer_rsrc((void*)sl, 0, 8192 * 16, 0x00020000);
#pragma unroll
    for (int ii = 0; ii < 2; ++ii)
#pragma unroll
      for (int j = 0; j < 4; ++j) {
        u32x4 v = __builtin_amdgcn_raw_buffer_load_b128(rs, ((ii * 4 + j) * 512 + tid) * 16, 0, 16);
        mf[ii][j] += __builtin_bit_cast(f32x4, v);
        if (!kz) {
          u32x4 w = __builtin_amdgcn_raw_buffer_load_b128(rs, (4096 + (ii * 4 + j) * 512 + tid) * 16, 0, 16);
          my[ii][j] = __builtin_bit_cast(f32x4, w);
        }
      }
  }
#pragma unroll
  for (int ii = 0; ii < 2; ++ii)
#pragma unroll
    for (int j = 0; j < 4; ++j) {
      int t = tm * 128 + wr * 64 + j * 16 + fr;
      int d = tn * 128 + wc * 64 + (kg * 2 + ii) * 16 + fq * 4;
      f32x4 o;
#pragma unroll
      for (int r = 0; r < 4; ++r) {
        float f = rbf(mf[ii][j][r]);
        float g = rbf(f * __builtin_amdgcn_rcpf(1.0f + __expf(-f)));
        float s = 2.0f * my[ii][j][r] + rbf(bf2f(xnv[ii][j][r]) * bf2f(dv[ii][r]));
        o[r] = bf2f(xv[ii][j][r]) + s * g;
      }
      *reinterpret_cast<f32x4*>(out + (size_t)t * D_ + d) = o;
    }
  STAMP(1, 4);
}

extern "C" void kernel_launch(void* const* d_in, const int* in_sizes, int n_in, void* d_out, int out_size, void* d_ws,
                              size_t ws_size, hipStream_t stream) {
  const unsigned short* x = (const unsigned short*)d_in[0];
  const unsigned short* cre = (const unsigned short*)d_in[5];
  const unsigned short* cim = (const unsigned short*)d_in[6];
  const unsigned short* dskip = (const unsigned short*)d_in[8];
  const unsigned short* wdown = (const unsigned short*)d_in[12];
  char* ws = (char*)d_ws;
  unsigned short* xn = (unsigned short*)(ws + OFF_XN);
  unsigned short* a2 = (unsigned short*)(ws + OFF_A2);
  float* out = (float*)d_out;
  G1Args ga;
  ga.xn = xn;
  ga.wgate = (const unsigned short*)d_in[10];
  ga.wup = (const unsigned short*)d_in[11];
  ga.bre = (const unsigned short*)d_in[3];
  ga.bim = (const unsigned short*)d_in[4];
  ga.lre = (const unsigned short*)d_in[1];
  ga.lim = (const unsigned short*)d_in[2];
  ga.logdt = (const unsigned short*)d_in[7];
  ga.wdown = wdown; ga.cre = cre; ga.cim = cim;
  ga.a2 = a2;
  ga.agg = (unsigned long long*)(ws + OFF_AGG);

  PrepArgs pa; pa.x = x; pa.wdown = wdown; pa.cre = cre; pa.cim = cim; pa.wgate = ga.wgate; pa.wup = ga.wup; pa.bre = ga.bre; pa.bim = ga.bim; pa.ws = ws;
  k_prep<<<PB_LN, 256, 0, stream>>>(pa);
  (void)hipFuncSetAttribute((const void*)k_gemm1, hipFuncAttributeMaxDynamicSharedMemorySize, G1_LDS);
  k_gemm1<<<256, 512, G1_LDS, stream>>>(ga);
  (void)hipFuncSetAttribute((const void*)k_gemm2, hipFuncAttributeMaxDynamicSharedMemorySize, G2_LDS);
  k_gemm2<<<256, 512, G2_LDS, stream>>>(a2, wdown, cre, cim, x, xn, dskip, out, (f32x4*)(ws + OFF_SLAB), (unsigned*)(ws + OFF_CNT));
}
```

```cpp
#include <hip/hip_runtime.h>
#include <hip/hip_bf16.h>
#include <stdint.h>

typedef __attribute__((ext_vector_type(8))) short bf16x8;
typedef __attribute__((ext_vector_type(4))) float f32x4;
typedef __attribute__((ext_vector_type(4))) unsigned short u16x4;
typedef __attribute__((ext_vector_type(4))) unsigned u32x4;

constexpr int S_ = 2048;
constexpr int D_ = 1024;
constexpr int P_ = 1024;
constexpr int F_ = 3584;
constexpr int N1_ = 9216;
constexpr int K2_ = 5632;

constexpr size_t OFF_AGG = 0;
constexpr size_t OFF_CNT = OFF_AGG + 32 * 8 * 32 * 2 * 8;
constexpr size_t SYNC_BYTES = OFF_CNT + 128 * 32 * 4;
constexpr size_t OFF_XN = SYNC_BYTES;
constexpr size_t OFF_A2 = OFF_XN + (size_t)S_ * D_ * 2;
constexpr size_t OFF_SLAB = OFF_A2 + (size_t)S_ * K2_ * 2;

__device__ __forceinline__ float bf2f(unsigned short v) { return __uint_as_float(((unsigned)v) << 16); }
__device__ __forceinline__ unsigned short f2bf(float f) {
  __bf16 b = (__bf16)f;
  return __builtin_bit_cast(unsigned short, b);
}
typedef __attribute__((ext_vector_type(2))) __bf16 bf16x2n;
typedef __attribute__((ext_vector_type(2))) float f32x2;
__device__ __forceinline__ unsigned pk2bf(float a, float b) {
  f32x2 v = {a, b};
  bf16x2n r = __builtin_convertvector(v, bf16x2n);
  return __builtin_bit_cast(unsigned, r);
}
__device__ __forceinline__ float lo2f(unsigned u) { return __uint_as_float(u << 16); }
__device__ __forceinline__ float hi2f(unsigned u) { return __uint_as_float(u & 0xFFFF0000u); }
__device__ __forceinline__ float rbf(float f) { return bf2f(f2bf(f)); }
__device__ __forceinline__ float2 cmul(float2 a, float2 b) { return make_float2(a.x * b.x - a.y * b.y, a.x * b.y + a.y * b.x); }

#define WAIT_VM(n) asm volatile("s_waitcnt vmcnt(" #n ")" ::: "memory")
#define LDS_PTR(p) ((__attribute__((address_space(3))) unsigned*)(p))

constexpr int PB_LN = 512;
struct PrepArgs { const unsigned short *x, *wdown, *cre, *cim, *wgate, *wup, *bre, *bim; char* ws; };
__global__ __launch_bounds__(256) void k_prep(PrepArgs pa) {
  const unsigned short* __restrict__ x = pa.x; char* __restrict__ ws = pa.ws;
  int b = blockIdx.x, tid = threadIdx.x, wave = tid >> 6, lane = tid & 63;
  unsigned short* xn = (unsigned short*)(ws + OFF_XN);
  for (int i = b * 256 + tid; i < (int)(SYNC_BYTES / 16); i += PB_LN * 256)
    reinterpret_cast<u32x4*>(ws)[i] = (u32x4){0u, 0u, 0u, 0u};
  int row = b * 4 + wave;
  const bf16x8* src = reinterpret_cast<const bf16x8*>(x + (size_t)row * D_);
  bf16x8 v0 = src[lane], v1 = src[64 + lane];
  float f[16];
#pragma unroll
  for (int j = 0; j < 8; ++j) { f[j] = bf2f((unsigned short)v0[j]); f[8 + j] = bf2f((unsigned short)v1[j]); }
  float s = 0.f;
#pragma unroll
  for (int j = 0; j < 16; ++j) s += f[j];
#pragma unroll
  for (int o = 32; o >= 1; o >>= 1) s += __shfl_xor(s, o);
  float mean = s * (1.0f / D_);
  float q = 0.f;
#pragma unroll
  for (int j = 0; j < 16; ++j) { float d = f[j] - mean; q += d * d; }
#pragma unroll
  for (int o = 32; o >= 1; o >>= 1) q += __shfl_xor(q, o);
  float var = q * (1.0f / D_);
  float mb = rbf(mean), vb = rbf(var);
  float den = rbf(sqrtf(rbf(vb + 1e-5f)));
  bf16x8 o0, o1;
#pragma unroll
  for (int j = 0; j < 8; ++j) {
    o0[j] = (short)f2bf(rbf(f[j] - mb) / den);
    o1[j] = (short)f2bf(rbf(f[8 + j] - mb) / den);
  }
  bf16x8* dst = reinterpret_cast<bf16x8*>(xn + (size_t)row * D_);
  dst[lane] = o0; dst[64 + lane] = o1;
}

typedef __attribute__((ext_vector_type(4))) short s16x4;
template <int OFF2>
__device__ __forceinline__ bf16x8 tr_frag(unsigned lds_addr) {
  s16x4 lo, hi;
  asm volatile("ds_read_b64_tr_b16 %0, %2\n\tds_read_b64_tr_b16 %1, %2 offset:%c3"
               : "=&v"(lo), "=&v"(hi) : "v"(lds_addr), "i"(OFF2));
  return __builtin_shufflevector(lo, hi, 0, 1, 2, 3, 4, 5, 6, 7);
}
#define TR_WAIT() do { asm volatile("s_waitcnt lgkmcnt(0)" ::: "memory"); __builtin_amdgcn_sched_barrier(0); } while (0)
__device__ __forceinline__ unsigned lds_u32(const void* p) {
  return (unsigned)(uintptr_t)(__attribute__((address_space(3))) const char*)p;
}

__device__ __forceinline__ float gelu_tanh(float x) {
  float w = x * __builtin_fmaf(x * x, -0.10294323970650479f, -2.3022081985952363f);
  return x * __builtin_amdgcn_rcpf(1.0f + __builtin_amdgcn_exp2f(w));
}

constexpr int G1_AT = 256 * 64;
constexpr int G1_WT = 288 * 64;
constexpr int G1_STAGE = G1_AT + G1_WT;
constexpr int G1_NSLOT = 4;
constexpr int G1_DUMMY = G1_NSLOT * G1_STAGE;
constexpr int G1_COEF = G1_DUMMY + 1024;
constexpr int G1_LDS = G1_COEF + 512;
constexpr int E_BUL = 0;
constexpr int E_ACT = 69632;
constexpr int E_CAG = 131072;
constexpr int E_TERM = 135168;
constexpr int E_MISC = 137216;

struct G1Args {
  const unsigned short *xn, *wgate, *wup, *bre, *bim, *lre, *lim, *logdt, *wdown, *cre, *cim;
  unsigned short* a2;
  unsigned long long* agg;
};
template <int V> struct IC { static constexpr int value = V; };

__global__ __launch_bounds__(512, 2) void k_gemm1(G1Args ga) {
  extern __shared__ __attribute__((aligned(16))) char smem[];
  int tid = threadIdx.x, wid = __builtin_amdgcn_readfirstlane(tid >> 6), lane = tid & 63;
  int xl = blockIdx.x & 7, loc = blockIdx.x >> 3;
  int tn = xl * 4 + (loc >> 3), tm = loc & 7;
  int wr = wid >> 1, wc = wid & 1;
  int fr = lane & 15, fq = lane >> 4;
  const unsigned short* xg = ga.xn + (size_t)tm * 256 * D_;
  unsigned short* a2 = ga.a2;
  unsigned long long* agg = ga.agg;

  f32x4 acc[9][4];
#pragma unroll
  for (int i = 0; i < 9; ++i)
#pragma unroll
    for (int j = 0; j < 4; ++j) acc[i][j] = (f32x4){0.f, 0.f, 0.f, 0.f};

  int srow = lane >> 2;
  int schunk = (lane & 3) ^ (((srow >> 3) & 1) << 1);
  const char* wsrc[3]; int wstr[3];
#pragma unroll
  for (int i = 0; i < 3; ++i) {
    int reg = (i < 2) ? wid * 2 + i : 16 + (wid & 1);
    const unsigned short* ptr; int str;
    if (reg < 12) {
      int Q = reg >> 2, kb = reg & 3;
      int r = lane >> 3, c = (lane & 7) ^ (((r >> 1) & 1) << 1) ^ ((kb & 1) << 2);
      int k = kb * 8 + r;
      const unsigned short* mat; int col;
      if (Q == 0) { mat = ga.wgate; col = c * 8; }
      else if (Q == 1) { mat = ga.wup; col = c * 8; }
      else if (c < 6) { mat = ga.wgate; col = 64 + c * 8; }
      else { mat = ga.wup; col = 64 + (c - 6) * 8; }
      ptr = mat + (size_t)k * F_ + tn * 112 + col; str = F_ * 64;
    } else if (reg < 14) {
      int r = lane >> 3, c = (lane & 7) ^ (((r >> 1) & 1) << 1);
      int k = ((reg - 12) * 2 + (c >> 2)) * 8 + r;
      ptr = ga.wup + (size_t)k * F_ + tn * 112 + 80 + (c & 3) * 8; str = F_ * 64;
    } else {
      int row = (reg - 14) * 16 + srow;
      const unsigned short* mat = (row < 32) ? ga.bre : ga.bim;
      ptr = mat + (size_t)(tn * 32 + (row & 31)) * D_ + schunk * 8; str = 64;
    }
    wsrc[i] = (const char*)ptr; wstr[i] = str;
  }
  auto stage = [&](int slot, int ks) {
    char* base = smem + slot * G1_STAGE;
#pragma unroll
    for (int i = 0; i < 2; ++i) {
      int ins = wid * 2 + i;
      __builtin_amdgcn_global_load_lds((const unsigned*)(xg + (size_t)(ins * 16 + srow) * D_ + ks * 32 + schunk * 8),
                                       LDS_PTR(base + ins * 1024), 16, 0, 0);
    }
#pragma unroll
    for (int i = 0; i < 2; ++i)
      __builtin_amdgcn_global_load_lds((const unsigned*)(wsrc[i] + (size_t)ks * wstr[i]), LDS_PTR(base + G1_AT + (wid * 2 + i) * 1024), 16, 0, 0);
    if (wid < 2)
      __builtin_amdgcn_global_load_lds((const unsigned*)(wsrc[2] + (size_t)ks * wstr[2]), LDS_PTR(base + G1_AT + (16 + wid) * 1024), 16, 0, 0);
  };

  auto stage_piece = [&](int slot, int ks, int pc) {
    char* base = smem + slot * G1_STAGE;
    if (pc < 2) {
      int ins = wid * 2 + pc;
      __builtin_amdgcn_global_load_lds((const unsigned*)(xg + (size_t)(ins * 16 + srow) * D_ + ks * 32 + schunk * 8),
                                       LDS_PTR(base + ins * 1024), 16, 0, 0);
    } else if (pc < 4) {
      int i = pc - 2;
      __builtin_amdgcn_global_load_lds((const unsigned*)(wsrc[i] + (size_t)ks * wstr[i]), LDS_PTR(base + G1_AT + (wid * 2 + i) * 1024), 16, 0, 0);
    } else if (wid < 2) {
      __builtin_amdgcn_global_load_lds((const unsigned*)(wsrc[2] + (size_t)ks * wstr[2]), LDS_PTR(base + G1_AT + (16 + wid) * 1024), 16, 0, 0);
    }
  };
  float c_lr = 0.f, c_li = 0.f, c_ld = 0.f;
  if (tid >= 448 && tid < 480) {
    int p = tn * 32 + (tid - 448);
    c_lr = bf2f(ga.lre[p]); c_li = bf2f(ga.lim[p]); c_ld = bf2f(ga.logdt[p]);
  }
  asm volatile("s_waitcnt vmcnt(0)" ::: "memory");
  const int ns = D_ / 32;
  stage(0, 0); stage(1, 1); stage(2, 2); stage(3, 3);
  if (tid >= 448 && tid < 480) {
    float lr = c_lr, li = c_li;
    float dt = rbf(expf(c_ld));
    float zr = dt * lr, zi = dt * li;
    float e = expf(zr);
    float lx = e * cosf(zi), ly = e * sinf(zi);
    float nr = lx - 1.0f, ni = ly;
    float den = lr * lr + li * li;
    *reinterpret_cast<f32x4*>(smem + G1_COEF + (tid - 448) * 16) = (f32x4){lx, ly, (nr * lr + ni * li) / den, (ni * lr - nr * li) / den};
  }
  int rchunk = fq ^ (((fr >> 3) & 1) << 1);
  const int xoff = (wr * 64 + fr) * 64 + rchunk * 16;
  const int boff = G1_AT + 14 * 1024 + fr * 64 + rchunk * 16;
  const unsigned sbase = lds_u32(smem);
  auto mainloop = [&](auto WCc) {
    constexpr int WC = decltype(WCc)::value;
    constexpr int NTR = WC ? 6 : 8;
    int kb = fq, q = (lane >> 2) & 3, pp = lane & 3;
    int sw = ((q >> 1) << 1) ^ ((kb & 1) << 2);
    int wo[NTR];
#pragma unroll
    for (int i = 0; i < NTR; ++i) {
      if (WC == 0) wo[i] = G1_AT + ((i >> 2) * 4 + kb) * 1024 + q * 128 + ((((i & 3) * 2 + (pp >> 1)) ^ sw) * 16) + (pp & 1) * 8;
      else if (i < 4) wo[i] = G1_AT + (8 + kb) * 1024 + q * 128 + (((i * 2 + (pp >> 1)) ^ sw) * 16) + (pp & 1) * 8;
      else wo[i] = G1_AT + (12 + (kb >> 1)) * 1024 + q * 128 + ((((kb & 1) * 4 + (i - 4) * 2 + (pp >> 1)) ^ ((q >> 1) << 1)) * 16) + (pp & 1) * 8;
    }
    unsigned wpk[NTR / 2];
#pragma unroll
    for (int i = 0; i < NTR / 2; ++i) wpk[i] = (unsigned)wo[2 * i] | ((unsigned)wo[2 * i + 1] << 16);
    auto ldw = [&](int i, const char* nb, unsigned nbu) -> bf16x8 {
      if (i < NTR) return tr_frag<512>(nbu + ((i & 1) ? (wpk[i >> 1] >> 16) : (wpk[i >> 1] & 0xFFFFu)));
      return *reinterpret_cast<const bf16x8*>(nb + boff + (WC ? (i - NTR + 1) : 0) * 1024);
    };
    bf16x8 xa_[4], xb_[4], wf[9];
    if (wid < 2) WAIT_VM(15); else WAIT_VM(12);
    __builtin_amdgcn_s_barrier();
#pragma unroll
    for (int j = 0; j < 4; ++j) xa_[j] = *reinterpret_cast<const bf16x8*>(smem + xoff + j * 1024);
#pragma unroll
    for (int i = 0; i < 9; ++i) wf[i] = ldw(i, smem, sbase);

    auto body = [&](int s, bf16x8 (&xc)[4], bf16x8 (&xnx)[4]) {
      if (wid < 2) WAIT_VM(10); else WAIT_VM(8);
      __builtin_amdgcn_s_barrier();
      const char* nb = smem + ((s + 1) & 3) * G1_STAGE;
      unsigned nbu = sbase + ((s + 1) & 3) * G1_STAGE;
      int ksn = min(s + 4, ns - 1);
      TR_WAIT();
#pragma unroll
      for (int i = 0; i < 9; ++i) {
#pragma unroll
        for (int j = 0; j < 4; ++j) acc[i][j] = __builtin_amdgcn_mfma_f32_16x16x32_bf16(wf[i], xc[j], acc[i][j], 0, 0, 0);
        wf[i] = ldw(i, nb, nbu);
        if (i < 4) xnx[i] = *reinterpret_cast<const bf16x8*>(nb + xoff + i * 1024);
        if (i >= 1 && i <= 5) stage_piece(s & 3, ksn, i - 1);
      }
#pragma unroll
      for (int i = 0; i < 9; ++i) {
        __builtin_amdgcn_sched_group_barrier(0x008, 4, 0);
        __builtin_amdgcn_sched_group_barrier(0x100, 1, 0);
      }
    };
    for (int s = 0; s < ns; s += 2) {
      body(s, xa_, xb_);
      body(s + 1, xb_, xa_);
    }
  };
  if (wc == 0) mainloop(IC<0>{}); else mainloop(IC<1>{});

  WAIT_VM(0);
  __syncthreads();
  float* bul = reinterpret_cast<float*>(smem + E_BUL);
  unsigned short* actl = reinterpret_cast<unsigned short*>(smem + E_ACT);
  float2* cag = reinterpret_cast<float2*>(smem + E_CAG);
  float2* term = reinterpret_cast<float2*>(smem + E_TERM);
  int tl = wr * 64 + fr;
  if (wc == 0) {
#pragma unroll
    for (int j = 0; j < 4; ++j) *reinterpret_cast<f32x4*>(bul + (tl + j * 16) * 68 + fq * 4) = acc[8][j];
  } else {
#pragma unroll
    for (int j = 0; j < 4; ++j) {
      *reinterpret_cast<f32x4*>(bul + (tl + j * 16) * 68 + 16 + fq * 4) = acc[6][j];
      *reinterpret_cast<f32x4*>(bul + (tl + j * 16) * 68 + 32 + fq * 4) = acc[7][j];
      *reinterpret_cast<f32x4*>(bul + (tl + j * 16) * 68 + 48 + fq * 4) = acc[8][j];
    }
  }
  __syncthreads();
  int ch = tid & 31, ck = tid >> 5;
  int p = tn * 32 + ch;
  float2 lam, cf;
  {
    f32x4 lc = *reinterpret_cast<const f32x4*>(smem + G1_COEF + ch * 16);
    lam = make_float2(lc[0], lc[1]); cf = make_float2(lc[2], lc[3]);
  }
  float hr[16], hi[16];
  {
    float sr = 0.f, si = 0.f;
#pragma unroll
    for (int i = 0; i < 16; ++i) {
      float rr = bul[(ck * 16 + i) * 68 + ch], ri = bul[(ck * 16 + i) * 68 + 32 + ch];
      float br = cf.x * rr - cf.y * ri, bi = cf.x * ri + cf.y * rr;
      float nr = lam.x * sr - lam.y * si + br;
      float ni = lam.x * si + lam.y * sr + bi;
      sr = nr; si = ni; hr[i] = sr; hi[i] = si;
    }
    cag[ck * 32 + ch] = make_float2(sr, si);
  }
  float2 l16 = lam;
#pragma unroll
  for (int i = 0; i < 4; ++i) l16 = cmul(l16, l16);
  float2 l256 = l16;
#pragma unroll
  for (int i = 0; i < 4; ++i) l256 = cmul(l256, l256);
  __syncthreads();
  if (tid < 32) {
    float2 c = make_float2(0.f, 0.f);
#pragma unroll
    for (int k = 0; k < 16; ++k) {
      float2 aa = cag[k * 32 + ch];
      cag[k * 32 + ch] = c;
      float2 t2 = cmul(l16, c);
      c = make_float2(t2.x + aa.x, t2.y + aa.y);
    }
    if (tm < 7) {
      unsigned long long* g = agg + ((size_t)(tn * 8 + tm) * 32 + ch) * 2;
      __hip_atomic_store(g, (1ull << 32) | (unsigned long long)__float_as_uint(c.x), __ATOMIC_RELAXED, __HIP_MEMORY_SCOPE_AGENT);
      __hip_atomic_store(g + 1, (1ull << 32) | (unsigned long long)__float_as_uint(c.y), __ATOMIC_RELAXED, __HIP_MEMORY_SCOPE_AGENT);
    }
  }
  unsigned long long pv0 = 0ull, pv1 = 0ull;
  if (ck < tm) {
    const unsigned long long* g = agg + ((size_t)(tn * 8 + ck) * 32 + ch) * 2;
    pv0 = __hip_atomic_load(g, __ATOMIC_RELAXED, __HIP_MEMORY_SCOPE_AGENT);
    pv1 = __hip_atomic_load(g + 1, __ATOMIC_RELAXED, __HIP_MEMORY_SCOPE_AGENT);
  }
#pragma unroll
  for (int i = 0; i < 6; ++i) {
    int g = min((blockIdx.x * 8 + wid) * 6 + i, 11263);
    const unsigned short* src = (g < 7168) ? ga.wdown + (size_t)g * 512 : (g < 9216 ? ga.cre + (size_t)(g - 7168) * 512 : ga.cim + (size_t)(g - 9216) * 512);
    __builtin_amdgcn_global_load_lds((const unsigned*)(src + lane * 8), LDS_PTR(smem + G1_DUMMY), 16, 0, 0);
  }
  auto geglu4 = [&](const f32x4& gt, const f32x4& up) -> uint2 {
    unsigned g01 = pk2bf(gt[0], gt[1]), g23 = pk2bf(gt[2], gt[3]);
    unsigned u01 = pk2bf(up[0], up[1]), u23 = pk2bf(up[2], up[3]);
    unsigned a01 = pk2bf(gelu_tanh(lo2f(g01)), gelu_tanh(hi2f(g01)));
    unsigned a23 = pk2bf(gelu_tanh(lo2f(g23)), gelu_tanh(hi2f(g23)));
    uint2 o;
    o.x = pk2bf(lo2f(a01) * lo2f(u01), hi2f(a01) * hi2f(u01));
    o.y = pk2bf(lo2f(a23) * lo2f(u23), hi2f(a23) * hi2f(u23));
    return o;
  };
  if (wc == 0) {
#pragma unroll
    for (int i = 0; i < 4; ++i)
#pragma unroll
      for (int j = 0; j < 4; ++j)
        *reinterpret_cast<uint2*>(actl + (tl + j * 16) * 120 + i * 16 + fq * 4) = geglu4(acc[i][j], acc[i + 4][j]);
  } else {
#pragma unroll
    for (int i = 0; i < 3; ++i)
#pragma unroll
      for (int j = 0; j < 4; ++j)
        *reinterpret_cast<uint2*>(actl + (tl + j * 16) * 120 + 64 + i * 16 + fq * 4) = geglu4(acc[i][j], acc[i + 3][j]);
  }
  __syncthreads();
#pragma unroll
  for (int it = 0; it < 7; ++it) {
    int idx = it * 512 + tid;
    int row = idx / 14, c = idx - row * 14;
    bf16x8 v = *reinterpret_cast<const bf16x8*>(actl + row * 120 + c * 8);
    *reinterpret_cast<bf16x8*>(a2 + (size_t)(tm * 256 + row) * K2_ + tn * 112 + c * 8) = v;
  }
  if (ck < tm) {
    const unsigned long long* g = agg + ((size_t)(tn * 8 + ck) * 32 + ch) * 2;
    unsigned long long v0 = pv0, v1 = pv1;
    while (!((v0 >> 32) == 1ull && (v1 >> 32) == 1ull)) {
      __builtin_amdgcn_s_sleep(1);
      v0 = __hip_atomic_load(g, __ATOMIC_RELAXED, __HIP_MEMORY_SCOPE_AGENT);
      v1 = __hip_atomic_load(g + 1, __ATOMIC_RELAXED, __HIP_MEMORY_SCOPE_AGENT);
    }
    float2 av = make_float2(__uint_as_float((unsigned)v0), __uint_as_float((unsigned)v1));
    for (int e = tm - 1 - ck; e > 0; --e) av = cmul(av, l256);
    term[ck * 32 + ch] = av;
  }
  __syncthreads();
  float2 bc = make_float2(0.f, 0.f);
  for (int j = 0; j < tm; ++j) { float2 tv = term[j * 32 + ch]; bc.x += tv.x; bc.y += tv.y; }
  float2 pw = make_float2(1.f, 0.f);
  for (int e = 0; e < ck; ++e) pw = cmul(pw, l16);
  float2 c0 = cag[ck * 32 + ch];
  float2 t3 = cmul(pw, bc);
  c0.x += t3.x; c0.y += t3.y;
  unsigned short* hl = reinterpret_cast<unsigned short*>(smem + E_BUL);
  {
    float2 pq = lam;
#pragma unroll
    for (int i = 0; i < 16; ++i) {
      float vr = hr[i] + pq.x * c0.x - pq.y * c0.y;
      float vi = hi[i] + pq.x * c0.y + pq.y * c0.x;
      hl[(ck * 16 + i) * 72 + ch] = f2bf(vr);
      hl[(ck * 16 + i) * 72 + 32 + ch] = f2bf(-vi);
      pq = cmul(pq, lam);
    }
  }
  __syncthreads();
#pragma unroll
  for (int it = 0; it < 4; ++it) {
    int idx = it * 512 + tid;
    int row = idx >> 3, c = idx & 7;
    bf16x8 v = *reinterpret_cast<const bf16x8*>(hl + row * 72 + c * 8);
    *reinterpret_cast<bf16x8*>(a2 + (size_t)(tm * 256 + row) * K2_ + F_ + (c >> 2) * P_ + tn * 32 + (c & 3) * 8) = v;
  }
}

constexpr int G2_S0 = 44;
constexpr int G2_STAGE = 32768;
constexpr int G2_NSLOT = 4;
constexpr int G2_LDS = G2_NSLOT * G2_STAGE + 16;
__global__ __launch_bounds__(512, 2) void k_gemm2(const unsigned short* __restrict__ a2, const unsigned short* __restrict__ wdown,
                                                  const unsigned short* __restrict__ cre, const unsigned short* __restrict__ cim,
                                                  const unsigned short* __restrict__ x,
                                                  const unsigned short* __restrict__ xn, const unsigned short* __restrict__ dskip,
                                                  float* __restrict__ out,
                                                  f32x4* __restrict__ slab, unsigned* __restrict__ cnt) {
  extern __shared__ __attribute__((aligned(16))) char smem[];
  int bid = blockIdx.x;
  int xcd = bid & 7, loc = bid >> 3;
  int tm = (xcd >> 1) * 4 + ((loc >> 3) & 3), tn = (xcd & 1) * 4 + ((loc >> 1) & 3), kz = loc & 1;
  int tile = tm * 8 + tn;
  int tid = threadIdx.x, wid = __builtin_amdgcn_readfirstlane(tid >> 6), lane = tid & 63;
  int kg = wid >> 2, wr = (wid >> 1) & 1, wc = wid & 1, fr = lane & 15, fq = lane >> 4;
  const unsigned short* ag = a2 + (size_t)tm * 128 * K2_ + kz * (G2_S0 * 64);
  f32x4 acc[4][4], accf[4][4];
#pragma unroll
  for (int i = 0; i < 4; ++i)
#pragma unroll
    for (int j = 0; j < 4; ++j) { acc[i][j] = (f32x4){0, 0, 0, 0}; accf[i][j] = (f32x4){0, 0, 0, 0}; }
  int srow = lane >> 3, schunk = (lane & 7) ^ (srow & 7);
  int wlofs[2];
#pragma unroll
  for (int i = 0; i < 2; ++i) {
    int reg = wid * 2 + i, Q = reg >> 3, kb = reg & 7;
    int r = lane >> 3, c = (lane & 7) ^ (((r >> 1) & 1) << 1) ^ ((kb & 1) << 2);
    wlofs[i] = (kb * 8 + r) * D_ + tn * 128 + Q * 64 + c * 8;
  }
  auto stage = [&](int slot, int kt) {
    char* base = smem + slot * G2_STAGE;
    int kabs = kz * (G2_S0 * 64) + kt * 64;
    bool ffn = kabs < F_;
    const unsigned short* wb = ffn ? wdown + (size_t)kabs * D_ : ((kabs < F_ + P_) ? cre + (kabs - F_) : cim + (kabs - F_ - P_));
#pragma unroll
    for (int i = 0; i < 2; ++i) {
      int ins = wid * 2 + i;
      __builtin_amdgcn_global_load_lds((const unsigned*)(ag + (size_t)(ins * 8 + srow) * K2_ + kt * 64 + schunk * 8),
                                       LDS_PTR(base + ins * 1024), 16, 0, 0);
      __builtin_amdgcn_global_load_lds((const unsigned*)(wb + (ffn ? wlofs[i] : (tn * 128 + ins * 8 + srow) * P_ + schunk * 8)),
                                       LDS_PTR(base + 16384 + ins * 1024), 16, 0, 0);
    }
  };
  auto stage_piece = [&](int slot, int kt, int pc) {
    char* base = smem + slot * G2_STAGE;
    int ins = wid * 2 + (pc >> 1);
    if ((pc & 1) == 0) {
      __builtin_amdgcn_global_load_lds((const unsigned*)(ag + (size_t)(ins * 8 + srow) * K2_ + kt * 64 + schunk * 8),
                                       LDS_PTR(base + ins * 1024), 16, 0, 0);
    } else {
      int kabs = kz * (G2_S0 * 64) + kt * 64;
      bool ffn = kabs < F_;
      const unsigned short* wb = ffn ? wdown + (size_t)kabs * D_ : ((kabs < F_ + P_) ? cre + (kabs - F_) : cim + (kabs - F_ - P_));
      __builtin_amdgcn_global_load_lds((const unsigned*)(wb + (ffn ? wlofs[pc >> 1] : (tn * 128 + ins * 8 + srow) * P_ + schunk * 8)),
                                       LDS_PTR(base + 16384 + ins * 1024), 16, 0, 0);
    }
  };
  const int ns = kz ? 88 - G2_S0 : G2_S0;
  const int nsf = kz ? 56 - G2_S0 : G2_S0;
  stage(0, 0); stage(1, 1); stage(2, 2); stage(3, 3);
  int rch = ((kg * 4 + fq) ^ (fr & 7)) * 16;
  const int xoff = (wr * 64 + fr) * 128 + rch;
  const int woffc = 16384 + (wc * 64 + fr) * 128 + rch;
  int wofs[4];
  {
    int kb = kg * 4 + fq, q = (lane >> 2) & 3, pp = lane & 3;
    int sw = ((q >> 1) << 1) ^ ((kb & 1) << 2);
#pragma unroll
    for (int i = 0; i < 4; ++i) wofs[i] = 16384 + (wc * 8 + kb) * 1024 + q * 128 + (((i * 2 + (pp >> 1)) ^ sw) * 16) + (pp & 1) * 8;
  }
  bf16x8 xa_[4], xb_[4], wa_[4], wb_[4];
  const unsigned sbase = lds_u32(smem);
  WAIT_VM(12);
  __builtin_amdgcn_s_barrier();
#pragma unroll
  for (int j = 0; j < 4; ++j) {
    xa_[j] = *reinterpret_cast<const bf16x8*>(smem + xoff + j * 2048);
    wa_[j] = tr_frag<512>(sbase + wofs[j]);
  }
  auto body = [&](auto NTRc, int s, bf16x8 (&xc)[4], bf16x8 (&wcur)[4], bf16x8 (&xnx)[4], bf16x8 (&wnx)[4]) {
    constexpr int NTR = decltype(NTRc)::value;
    WAIT_VM(8);
    __builtin_amdgcn_s_barrier();
    const char* nb = smem + ((s + 1) & 3) * G2_STAGE;
    unsigned nbu = sbase + ((s + 1) & 3) * G2_STAGE;
    int ktn = min(s + 4, ns - 1);
    TR_WAIT();
#pragma unroll
    for (int i = 0; i < 4; ++i) {
#pragma unroll
      for (int j = 0; j < 4; ++j) acc[i][j] = __builtin_amdgcn_mfma_f32_16x16x32_bf16(wcur[i], xc[j], acc[i][j], 0, 0, 0);
      if (NTR) wnx[i] = tr_frag<512>(nbu + wofs[i]);
      else wnx[i] = *reinterpret_cast<const bf16x8*>(nb + woffc + i * 2048);
      xnx[i] = *reinterpret_cast<const bf16x8*>(nb + xoff + i * 2048);
      stage_piece(s & 3, ktn, i);
    }
#pragma unroll
    for (int i = 0; i < 4; ++i) {
      __builtin_amdgcn_sched_group_barrier(0x008, 4, 0);
      __builtin_amdgcn_sched_group_barrier(0x100, 3, 0);
      __builtin_amdgcn_sched_group_barrier(0x020, 1, 0);
    }
    if (s + 1 == nsf) {
#pragma unroll
      for (int i = 0; i < 4; ++i)
#pragma unroll
        for (int j = 0; j < 4; ++j) { accf[i][j] = acc[i][j]; acc[i][j] = (f32x4){0, 0, 0, 0}; }
    }
  };
  for (int s = 0; s < nsf - 2; s += 2) {
    body(IC<1>{}, s, xa_, wa_, xb_, wb_);
    body(IC<1>{}, s + 1, xb_, wb_, xa_, wa_);
  }
  body(IC<1>{}, nsf - 2, xa_, wa_, xb_, wb_);
  body(IC<0>{}, nsf - 1, xb_, wb_, xa_, wa_);
  for (int s = nsf; s < ns; s += 2) {
    body(IC<0>{}, s, xa_, wa_, xb_, wb_);
    body(IC<0>{}, s + 1, xb_, wb_, xa_, wa_);
  }
  WAIT_VM(0);
  __syncthreads();
  f32x4* ex = reinterpret_cast<f32x4*>(smem);
  int wq = wid & 3;
#pragma unroll
  for (int ii = 0; ii < 2; ++ii)
#pragma unroll
    for (int j = 0; j < 4; ++j) {
      f32x4 sf = kg ? accf[ii][j] : accf[2 + ii][j];
      f32x4 sy = kg ? acc[ii][j] : acc[2 + ii][j];
      ex[(((0 * 2 + kg) * 4 + wq) * 8 + ii * 4 + j) * 64 + lane] = sf;
      if (kz) ex[(((1 * 2 + kg) * 4 + wq) * 8 + ii * 4 + j) * 64 + lane] = sy;
    }
  unsigned* role_s = reinterpret_cast<unsigned*>(smem + G2_NSLOT * G2_STAGE);
  if (tid == 0) *role_s = __hip_atomic_fetch_add(&cnt[tile * 32], 1u, __ATOMIC_RELAXED, __HIP_MEMORY_SCOPE_AGENT);
  __syncthreads();
  f32x4 mf[2][4], my[2][4];
#pragma unroll
  for (int ii = 0; ii < 2; ++ii)
#pragma unroll
    for (int j = 0; j < 4; ++j) {
      f32x4 ownf = kg ? accf[2 + ii][j] : accf[ii][j];
      f32x4 owny = kg ? acc[2 + ii][j] : acc[ii][j];
      mf[ii][j] = ownf + ex[(((0 * 2 + (kg ^ 1)) * 4 + wq) * 8 + ii * 4 + j) * 64 + lane];
      my[ii][j] = owny;
      if (kz) my[ii][j] += ex[(((1 * 2 + (kg ^ 1)) * 4 + wq) * 8 + ii * 4 + j) * 64 + lane];
    }
  unsigned role = *role_s;
  f32x4* sl = slab + (size_t)tile * 8192;
  if (role == 0) {
    __amdgpu_buffer_rsrc_t rs = __builtin_amdgcn_make_buffer_rsrc((void*)sl, 0, 8192 * 16, 0x00020000);
#pragma unroll
    for (int ii = 0; ii < 2; ++ii)
#pragma unroll
      for (int j = 0; j < 4; ++j) {
        u32x4 v = __builtin_bit_cast(u32x4, mf[ii][j]);
        __builtin_amdgcn_raw_buffer_store_b128(v, rs, ((ii * 4 + j) * 512 + tid) * 16, 0, 16);
        if (kz) {
          u32x4 w = __builtin_bit_cast(u32x4, my[ii][j]);
          __builtin_amdgcn_raw_buffer_store_b128(w, rs, (4096 + (ii * 4 + j) * 512 + tid) * 16, 0, 16);
        }
      }
    asm volatile("s_waitcnt vmcnt(0)" ::: "memory");
    __syncthreads();
    if (tid == 0) __hip_atomic_store(&cnt[tile * 32 + 16], 1u, __ATOMIC_RELAXED, __HIP_MEMORY_SCOPE_AGENT);
    return;
  }
  u16x4 xv[2][4], xnv[2][4], dv[2];
#pragma unroll
  for (int ii = 0; ii < 2; ++ii) {
    int d = tn * 128 + wc * 64 + (kg * 2 + ii) * 16 + fq * 4;
    dv[ii] = *reinterpret_cast<const u16x4*>(dskip + d);
#pragma unroll
    for (int j = 0; j < 4; ++j) {
      int t = tm * 128 + wr * 64 + j * 16 + fr;
      xv[ii][j] = *reinterpret_cast<const u16x4*>(x + (size_t)t * D_ + d);
      xnv[ii][j] = *reinterpret_cast<const u16x4*>(xn + (size_t)t * D_ + d);
    }
  }
  if (tid == 0) {
    while (__hip_atomic_load(&cnt[tile * 32 + 16], __ATOMIC_RELAXED, __HIP_MEMORY_SCOPE_AGENT) == 0u) __builtin_amdgcn_s_sleep(1);
  }
  __syncthreads();
  __builtin_amdgcn_fence(__ATOMIC_ACQUIRE, "wavefront");
  {
    __amdgpu_buffer_rsrc_t rs = __builtin_amdgcn_make_buffer_rsrc((void*)sl, 0, 8192 * 16, 0x00020000);
#pragma unroll
    for (int ii = 0; ii < 2; ++ii)
#pragma unroll
      for (int j = 0; j < 4; ++j) {
        u32x4 v = __builtin_amdgcn_raw_buffer_load_b128(rs, ((ii * 4 + j) * 512 + tid) * 16, 0, 16);
        mf[ii][j] += __builtin_bit_cast(f32x4, v);
        if (!kz) {
          u32x4 w = __builtin_amdgcn_raw_buffer_load_b128(rs, (4096 + (ii * 4 + j) * 512 + tid) * 16, 0, 16);
          my[ii][j] = __builtin_bit_cast(f32x4, w);
        }
      }
  }
#pragma unroll
  for (int ii = 0; ii < 2; ++ii)
#pragma unroll
    for (int j = 0; j < 4; ++j) {
      int t = tm * 128 + wr * 64 + j * 16 + fr;
      int d = tn * 128 + wc * 64 + (kg * 2 + ii) * 16 + fq * 4;
      f32x4 o;
#pragma unroll
      for (int r = 0; r < 4; ++r) {
        float f = rbf(mf[ii][j][r]);
        float g = rbf(f * __builtin_amdgcn_rcpf(1.0f + __expf(-f)));
        float s = 2.0f * my[ii][j][r] + rbf(bf2f(xnv[ii][j][r]) * bf2f(dv[ii][r]));
        o[r] = bf2f(xv[ii][j][r]) + s * g;
      }
      *reinterpret_cast<f32x4*>(out + (size_t)t * D_ + d) = o;
    }
}

extern "C" void kernel_launch(void* const* d_in, const int* in_sizes, int n_in, void* d_out, int out_size, void* d_ws,
                              size_t ws_size, hipStream_t stream) {
  const unsigned short* x = (const unsigned short*)d_in[0];
  const unsigned short* cre = (const unsigned short*)d_in[5];
  const unsigned short* cim = (const unsigned short*)d_in[6];
  const unsigned short* dskip = (const unsigned short*)d_in[8];
  const unsigned short* wdown = (const unsigned short*)d_in[12];
  char* ws = (char*)d_ws;
  unsigned short* xn = (unsigned short*)(ws + OFF_XN);
  unsigned short* a2 = (unsigned short*)(ws + OFF_A2);
  float* out = (float*)d_out;
  G1Args ga;
  ga.xn = xn;
  ga.wgate = (const unsigned short*)d_in[10];
  ga.wup = (const unsigned short*)d_in[11];
  ga.bre = (const unsigned short*)d_in[3];
  ga.bim = (const unsigned short*)d_in[4];
  ga.lre = (const unsigned short*)d_in[1];
  ga.lim = (const unsigned short*)d_in[2];
  ga.logdt = (const unsigned short*)d_in[7];
  ga.wdown = wdown; ga.cre = cre; ga.cim = cim;
  ga.a2 = a2;
  ga.agg = (unsigned long long*)(ws + OFF_AGG);

  PrepArgs pa; pa.x = x; pa.wdown = wdown; pa.cre = cre; pa.cim = cim; pa.wgate = ga.wgate; pa.wup = ga.wup; pa.bre = ga.bre; pa.bim = ga.bim; pa.ws = ws;
  k_prep<<<PB_LN, 256, 0, stream>>>(pa);
  (void)hipFuncSetAttribute((const void*)k_gemm1, hipFuncAttributeMaxDynamicSharedMemorySize, G1_LDS);
  k_gemm1<<<256, 512, G1_LDS, stream>>>(ga);
  (void)hipFuncSetAttribute((const void*)k_gemm2, hipFuncAttributeMaxDynamicSharedMemorySize, G2_LDS);
  k_gemm2<<<256, 512, G2_LDS, stream>>>(a2, wdown, cre, cim, x, xn, dskip, out, (f32x4*)(ws + OFF_SLAB), (unsigned*)(ws + OFF_CNT));
}
```
